```python
import torch

def _softmax(x, axis=-1):
    return torch.softmax(x, dim=axis)
def _gelu(x):
    return torch.nn.functional.gelu(x, approximate="tanh")
def _silu(x):
    return torch.nn.functional.silu(x)
def _sigmoid(x):
    return torch.sigmoid(x)
def _layernorm(x, axis=-1, eps=1e-5):
    m = x.mean(dim=axis, keepdim=True)
    v = x.var(dim=axis, keepdim=True, unbiased=False)
    return (x - m) / torch.sqrt(v + eps)
def _scatter_add(tgt, idx, src, axis=0):
    return tgt.clone().index_add_(axis, idx, src)
def _segment_sum(src, seg, n):
    out = torch.zeros((n,) + src.shape[1:], dtype=src.dtype)
    return out.index_add_(0, seg, src)
def _pad(x, pw):
    flat = tuple(v for pair in reversed(pw) for v in pair)
    return torch.nn.functional.pad(x, flat)

kernel_name = 'sequential_preLN__ssm_conv_s4__geglu_ffn__prompt'

def setup_inputs():

    _w = (lambda a: (lambda t: t.to(torch.bfloat16) if t.is_floating_point() else t)(torch.as_tensor(a)))
    def _A_setup():
        B, L, D, N = 1, 2048, 448, 256
        A_re = -(lambda _x: torch.exp(torch.as_tensor(_x)))((lambda *s: torch.randn(*s, dtype=torch.bfloat16) * 3.2)(D, N))
        A_im = torch.pi * (lambda *s: torch.randn(*s, dtype=torch.bfloat16) * 0.05)(D, N)
        B_re = (lambda *s: torch.randn(*s, dtype=torch.bfloat16) * 0.05)(D, N)
        B_im = (lambda *s: torch.randn(*s, dtype=torch.bfloat16) * 0.05)(D, N)
        C_re = (lambda *s: torch.randn(*s, dtype=torch.bfloat16) * 0.05)(D, N)
        C_im = (lambda *s: torch.randn(*s, dtype=torch.bfloat16) * 0.05)(D, N)
        log_dt = (lambda *s: torch.randn(*s, dtype=torch.bfloat16) * 0.05)(D) * 0.5 - 4.0
        Dskip = (lambda *s: torch.randn(*s, dtype=torch.bfloat16) * 0.05)(D)
        return {
            "u": _w((lambda *s: torch.randn(*s, dtype=torch.bfloat16) * 0.05)(B, L, D)),
            "A_re": _w(A_re),
            "A_im": _w(A_im),
            "B_re": _w(B_re),
            "B_im": _w(B_im),
            "C_re": _w(C_re),
            "C_im": _w(C_im),
            "log_dt": _w(log_dt),
            "Dskip": _w(Dskip),
        }
    _A_in = _A_setup()

    def _B_setup():
        B, S, D, F = 1, 2048, 448, 1792
        return {
            "x": _w((lambda *s: torch.randn(*s, dtype=torch.bfloat16) * 0.05)(B, S, D)),
            "w_gate": _w((lambda *s: torch.randn(*s, dtype=torch.bfloat16) * 0.05)(D, F)),
            "w_up": _w((lambda *s: torch.randn(*s, dtype=torch.bfloat16) * 0.05)(D, F)),
            "w_down": _w((lambda *s: torch.randn(*s, dtype=torch.bfloat16) * 0.05)(F, D)),
        }
    _B_in = _B_setup()

    return {
        "A_u": _A_in["u"],
        "A_A_re": _A_in["A_re"],
        "A_A_im": _A_in["A_im"],
        "A_B_re": _A_in["B_re"],
        "A_B_im": _A_in["B_im"],
        "A_C_re": _A_in["C_re"],
        "A_C_im": _A_in["C_im"],
        "A_log_dt": _A_in["log_dt"],
        "A_Dskip": _A_in["Dskip"],
        "B_x": _B_in["x"],
        "B_w_gate": _B_in["w_gate"],
        "B_w_up": _B_in["w_up"],
        "B_w_down": _B_in["w_down"],
    }

def reference(A_u, A_A_re, A_A_im, A_B_re, A_B_im, A_C_re, A_C_im, A_log_dt, A_Dskip, B_x, B_w_gate, B_w_up, B_w_down):
    def _A_ref(u, A_re, A_im, B_re, B_im, C_re, C_im, log_dt, Dskip):
        Bsz, L, D = u.shape
        dt = torch.exp(log_dt)[:, None]
        A = A_re + 1j * A_im
        Bc = B_re + 1j * B_im
        Cc = C_re + 1j * C_im
        logdA = dt * A
        dB = dt * Bc
        l = (lambda x, dt: x.to(dt))(torch.arange(L), A_re.dtype)
        P = torch.exp(logdA[:, :, None] * l[None, None, :])
        K = torch.einsum("dn,dnl,dn->dl", Cc, P, dB).real
        L2 = 2 * L
        Kp = _pad(K, ((0, 0), (0, L)))
        up = (lambda x, *d: x.permute(*d))(u, 0, 2, 1)
        up = _pad(up, ((0, 0), (0, 0), (0, L)))
        Kf = (lambda x, axes=(-2,-1): torch.fft.rfft2(x.float(), dim=axes))(Kp[:, None, :], axes=(-2, -1))
        Uf = (lambda x, axes=(-2,-1): torch.fft.rfft2(x.float(), dim=axes))(up[:, :, None, :], axes=(-2, -1))
        Yf = Uf * Kf[None, :, :, :]
        y = (lambda x, s, axes=(-2,-1): torch.fft.irfft2(x, s=s, dim=axes).to(torch.bfloat16))(Yf, (1, L2), axes=(-2, -1))[:, :, 0, :L]
        y = (lambda x, *d: x.permute(*d))(y, 0, 2, 1) + u * Dskip[None, None, :]
        return y

    def _B_ref(x, w_gate, w_up, w_down):
        return (_gelu(x @ w_gate) * (x @ w_up)) @ w_down

    x = A_u
    h = x + _A_ref(_layernorm(x, axis=-1), A_A_re, A_A_im, A_B_re, A_B_im, A_C_re, A_C_im, A_log_dt, A_Dskip)
    h2 = h + _B_ref(_layernorm(h, axis=-1), B_w_gate, B_w_up, B_w_down)
    return h2
```

```cpp
#include <hip/hip_runtime.h>
#include <hip/hip_bf16.h>
#include <stdint.h>

#define L_ 2048
#define D_ 448
#define N_ 256
#define F_ 1792

typedef unsigned short u16;
__device__ __forceinline__ float bf2f(u16 v) { return __uint_as_float(((unsigned)v) << 16); }
__device__ __forceinline__ u16 f2bf(float f) {
    unsigned u = __float_as_uint(f);
    u += 0x7FFF + ((u >> 16) & 1);
    return (u16)(u >> 16);
}
__device__ __forceinline__ float rbf(float f) { return bf2f(f2bf(f)); }

__global__ void k_ln(const u16* __restrict__ x, u16* __restrict__ y) {
    int row = blockIdx.x; int lane = threadIdx.x;
    float v[7]; float s = 0.f;
    for (int i = 0; i < 7; i++) { v[i] = bf2f(x[row * D_ + lane + 64 * i]); s += v[i]; }
    for (int o = 32; o > 0; o >>= 1) s += __shfl_xor(s, o);
    float mean = s / D_;
    float q = 0.f;
    for (int i = 0; i < 7; i++) { float d = v[i] - mean; q += d * d; }
    for (int o = 32; o > 0; o >>= 1) q += __shfl_xor(q, o);
    float var = rbf(q / D_);
    float m = rbf(mean);
    float sd = rbf(sqrtf(rbf(var + 1e-5f)));
    for (int i = 0; i < 7; i++) y[row * D_ + lane + 64 * i] = f2bf(rbf(v[i] - m) / sd);
}

#define EXACT_L 0
__global__ void k_kgen(const u16* Are, const u16* Aim, const u16* Bre, const u16* Bim, const u16* Cre, const u16* Cim,
                       const u16* logdt, float* K) {
    int d = blockIdx.y; int l = blockIdx.x * 256 + threadIdx.x;
    float dt = rbf(expf(bf2f(logdt[d])));
    float lf = EXACT_L ? (float)l : rbf((float)l);
    float acc = 0.f;
    for (int n = 0; n < N_; n++) {
        float ar = dt * bf2f(Are[d * N_ + n]), ai = dt * bf2f(Aim[d * N_ + n]);
        float br = dt * bf2f(Bre[d * N_ + n]), bi = dt * bf2f(Bim[d * N_ + n]);
        float cr = bf2f(Cre[d * N_ + n]), ci = bf2f(Cim[d * N_ + n]);
        float cbr = cr * br - ci * bi, cbi = cr * bi + ci * br;
        float e = expf(ar * lf); float sn, cs; sincosf(ai * lf, &sn, &cs);
        acc += e * (cbr * cs - cbi * sn);
    }
    K[d * L_ + l] = acc;
}

__global__ void k_conv(const u16* x, const u16* u, const float* K, const u16* Dskip, u16* h) {
    int d = blockIdx.y; int t = blockIdx.x * 256 + threadIdx.x;
    float acc = 0.f;
    for (int j = 0; j <= t; j++) acc += K[d * L_ + j] * bf2f(u[(t - j) * D_ + d]);
    float y = rbf(acc);
    float ud = rbf(bf2f(u[t * D_ + d]) * bf2f(Dskip[d]));
    float ya = rbf(y + ud);
    h[t * D_ + d] = f2bf(bf2f(x[t * D_ + d]) + ya);
}

__global__ void k_gemm1(const u16* xn, const u16* wg, const u16* wu, u16* act) {
    int f = blockIdx.x * 256 + threadIdx.x; int t = blockIdx.y;
    float g = 0.f, u = 0.f;
    for (int k = 0; k < D_; k++) { float a = bf2f(xn[t * D_ + k]); g += a * bf2f(wg[k * F_ + f]); u += a * bf2f(wu[k * F_ + f]); }
    g = rbf(g); u = rbf(u);
    float ge = 0.5f * g * (1.f + tanhf(0.7978845608028654f * (g + 0.044715f * g * g * g)));
    act[t * F_ + f] = f2bf(rbf(ge) * u);
}
__global__ void k_gemm2(const u16* act, const u16* wd, const u16* h, u16* out) {
    int d = threadIdx.x; int t = blockIdx.x;
    float a = 0.f;
    for (int k = 0; k < F_; k++) a += bf2f(act[t * F_ + k]) * bf2f(wd[k * D_ + d]);
    out[t * D_ + d] = f2bf(bf2f(h[t * D_ + d]) + rbf(a));
}

extern "C" void kernel_launch(void* const* d_in, const int* in_sizes, int n_in,
                              void* d_out, int out_size, void* d_ws, size_t ws_size,
                              hipStream_t stream) {
    const u16* x = (const u16*)d_in[0];
    char* ws = (char*)d_ws;
    u16* u = (u16*)ws; ws += L_ * D_ * 2;
    u16* h = (u16*)ws; ws += L_ * D_ * 2;
    u16* xn = (u16*)ws; ws += L_ * D_ * 2;
    u16* act = (u16*)ws; ws += L_ * F_ * 2;
    float* K = (float*)ws;
    k_ln<<<L_, 64, 0, stream>>>(x, u);
    k_kgen<<<dim3(L_ / 256, D_), 256, 0, stream>>>((const u16*)d_in[1], (const u16*)d_in[2], (const u16*)d_in[3], (const u16*)d_in[4],
                                                   (const u16*)d_in[5], (const u16*)d_in[6], (const u16*)d_in[7], K);
    k_conv<<<dim3(L_ / 256, D_), 256, 0, stream>>>(x, u, K, (const u16*)d_in[8], h);
    k_ln<<<L_, 64, 0, stream>>>(h, xn);
    k_gemm1<<<dim3(F_ / 256, L_), 256, 0, stream>>>(xn, (const u16*)d_in[10], (const u16*)d_in[11], act);
    k_gemm2<<<L_, D_, 0, stream>>>(act, (const u16*)d_in[12], h, (u16*)d_out);
}
```

```cpp
#include <hip/hip_runtime.h>
#include <stdint.h>

namespace {

constexpr int L = 2048, D = 448, N = 256, F = 1792;

typedef unsigned short u16;
typedef unsigned int u32;
typedef unsigned long long u64;
typedef u32 v4u __attribute__((ext_vector_type(4)));
typedef u32 v2u __attribute__((ext_vector_type(2)));
typedef float f32x2 __attribute__((ext_vector_type(2)));
typedef float f32x4 __attribute__((ext_vector_type(4)));
typedef float f32x16 __attribute__((ext_vector_type(16)));
typedef _Float16 f16x2 __attribute__((ext_vector_type(2)));
typedef _Float16 f16x8 __attribute__((ext_vector_type(8)));
typedef __bf16 bf16x2 __attribute__((ext_vector_type(2)));
typedef __bf16 bf16x8 __attribute__((ext_vector_type(8)));
typedef short s16x4 __attribute__((ext_vector_type(4)));
#define GAS __attribute__((address_space(1)))
#define LAS __attribute__((address_space(3)))
#define RLX_AGENT __ATOMIC_RELAXED, __HIP_MEMORY_SCOPE_AGENT

constexpr size_t WS_EPOCH = 0;
constexpr size_t WS_STATS = 4096;
constexpr size_t WS_W1T = 65536;
constexpr size_t WS_W2T = WS_W1T + (size_t)112 * 28 * 1024;
constexpr size_t WS_HT = WS_W2T + (size_t)14 * 112 * 1024;
constexpr size_t WS_ACT = WS_HT + (size_t)D * L * 2;
constexpr size_t WS_DBG = WS_ACT + (size_t)64 * 112 * 1024;
constexpr int NSTAMP = 32;

#define STAMP(ws, k) do {} while (0)

__device__ __forceinline__ float bflo(u32 v) { return __builtin_bit_cast(float, v << 16); }
__device__ __forceinline__ float bfhi(u32 v) { return __builtin_bit_cast(float, v & 0xffff0000u); }
__device__ __forceinline__ u32 pk_bf16(float a, float b) { f32x2 f = {a, b}; return __builtin_bit_cast(u32, __builtin_convertvector(f, bf16x2)); }
__device__ __forceinline__ u32 pk_f16(float a, float b) { f32x2 f = {a, b}; return __builtin_bit_cast(u32, __builtin_convertvector(f, f16x2)); }
__device__ __forceinline__ float rbf(float a) { return bflo(pk_bf16(a, 0.f) & 0xffffu); }
__device__ __forceinline__ float dot2bf(u32 a, u32 b, float c) {
    return __builtin_amdgcn_fdot2_f32_bf16(__builtin_bit_cast(bf16x2, a), __builtin_bit_cast(bf16x2, b), c, false);
}
__device__ __forceinline__ f32x16 mfma_bf16(v4u a, v4u b, f32x16 c) {
    return __builtin_amdgcn_mfma_f32_32x32x16_bf16(__builtin_bit_cast(bf16x8, a), __builtin_bit_cast(bf16x8, b), c, 0, 0, 0);
}
__device__ __forceinline__ f32x16 mfma_f16(v4u a, v4u b, f32x16 c) {
    return __builtin_amdgcn_mfma_f32_32x32x16_f16(__builtin_bit_cast(f16x8, a), __builtin_bit_cast(f16x8, b), c, 0, 0, 0);
}
__device__ __forceinline__ v2u lds_tr(LAS char* p) {
    return __builtin_bit_cast(v2u, __builtin_amdgcn_ds_read_tr16_b64_v4i16((LAS s16x4*)p));
}
__device__ __forceinline__ float wave_sum(float v) {
#pragma unroll
    for (int o = 32; o > 0; o >>= 1) v += __shfl_xor(v, o);
    return v;
}
__device__ __forceinline__ float wave_max(float v) {
#pragma unroll
    for (int o = 32; o > 0; o >>= 1) v = fmaxf(v, __shfl_xor(v, o));
    return v;
}
struct cf { float r, i; };
__device__ __forceinline__ cf cmul(cf a, cf b) { return {a.r * b.r - a.i * b.i, a.r * b.i + a.i * b.r}; }
__device__ __forceinline__ cf csq(cf a) { return {a.r * a.r - a.i * a.i, 2.f * a.r * a.i}; }

struct P1 {
    const u16 *x, *Are, *Aim, *Bre, *Bim, *Cre, *Cim, *logdt, *Dskip, *wg, *wu, *wd;
    char* ws;
};

constexpr int XY_STRIDE = 272, XY_ROWS = 59, XY_WAVE = XY_STRIDE * XY_ROWS;
constexpr int K1_XY = 0;
constexpr int K1_KP = 8 * XY_WAVE;
constexpr int K1_U = K1_KP + 8 * 1536;
constexpr int K1_SB = K1_U + 2 * 4160;
constexpr int K1_LDS = K1_SB + 64;
constexpr int P_WAVE = 64 * 144;

__device__ __forceinline__ void pretile_task(const P1& p, int task, int lane) {
    const u16* src; int rstride; char* dst;
    if (task < 392) {
        int idx = task * 64 + lane;
        int fg = idx % 224, mk = idx / 224, kg = mk % 56, mat = mk / 56;
        src = (mat ? p.wu : p.wg) + (size_t)kg * 8 * F + fg * 8; rstride = F;
        int r0 = 16 * mat + 4 * (fg & 1);
        dst = p.ws + WS_W1T + ((size_t)((fg >> 1) * 28 + (kg >> 1)) * 64 + (kg & 1) * 32 + r0) * 16;
    } else {
        int idx = (task - 392) * 64 + lane;
        int dg = idx % 56, kg = idx / 56;
        src = p.wd + (size_t)kg * 8 * D + dg * 8; rstride = D;
        int r0 = 16 * ((dg >> 1) & 1) + 4 * (dg & 1);
        dst = p.ws + WS_W2T + ((size_t)((dg >> 2) * 112 + (kg >> 1)) * 64 + (kg & 1) * 32 + r0) * 16;
    }
    v4u in[8];
#pragma unroll
    for (int k = 0; k < 8; k++) in[k] = *(const v4u*)(src + (size_t)k * rstride);
#pragma unroll
    for (int c = 0; c < 8; c++) {
        v4u o;
#pragma unroll
        for (int kp = 0; kp < 4; kp++)
            o[kp] = __builtin_amdgcn_perm(in[2 * kp + 1][c >> 1], in[2 * kp][c >> 1], (c & 1) ? 0x07060302u : 0x05040100u);
        *(v4u*)(dst + ((c & 3) + 8 * (c >> 2)) * 16) = o;
    }
}

__global__ __launch_bounds__(512, 2) void k1_ssm(P1 p) {
    extern __shared__ __attribute__((aligned(16))) char smem_[];
    LAS char* sm = (LAS char*)smem_;
    const int tid = threadIdx.x, lane = tid & 63;
    const int wave = __builtin_amdgcn_readfirstlane(tid >> 6);
    const int b = blockIdx.x;
    STAMP(p.ws, 0);
    const u32 tag = __hip_atomic_load((GAS u32*)(p.ws + WS_EPOCH), RLX_AGENT) + 1u;

    const int row = b * 8 + wave;
    v4u xr = {0, 0, 0, 0};
    if (lane < 56) xr = *(const v4u*)(p.x + (size_t)row * D + lane * 8);

    const bool conv_blk = b < 224;
    const int pair = (b & 7) * 28 + (b >> 3);
    const int ch = wave >> 2, wq = wave & 3;
    const int d0 = conv_blk ? 2 * pair : 0;
    const int dch = d0 + ch;
    const int pi = dch * N + (tid & 255);
    u16 r_are = 0, r_aim = 0, r_bre = 0, r_bim = 0, r_cre = 0, r_cim = 0, r_ldt = 0;
    u32 xc[4] = {0, 0, 0, 0}; u32 r_dsk = 0;
    if (conv_blk) {
        r_are = p.Are[pi]; r_aim = p.Aim[pi]; r_bre = p.Bre[pi]; r_bim = p.Bim[pi]; r_cre = p.Cre[pi]; r_cim = p.Cim[pi];
        r_ldt = p.logdt[dch];
        r_dsk = *(const u32*)(p.Dskip + d0);
#pragma unroll
        for (int i = 0; i < 4; i++) xc[i] = *(const u32*)(p.x + (size_t)(4 * tid + i) * D + d0);
    }
    {
        float v[8];
#pragma unroll
        for (int i = 0; i < 4; i++) { v[2 * i] = bflo(xr[i]); v[2 * i + 1] = bfhi(xr[i]); }
        float s = 0.f;
#pragma unroll
        for (int i = 0; i < 8; i++) s += v[i];
        s = wave_sum(s);
        const float mean = s * (1.f / D);
        float q = 0.f;
#pragma unroll
        for (int i = 0; i < 8; i++) { float dd = v[i] - mean; q += dd * dd; }
        if (lane >= 56) q = 0.f;
        q = wave_sum(q);
        const float var = rbf(q * (1.f / D));
        const float sd = __builtin_sqrtf(rbf(var + 1e-5f));
        const u32 pay = pk_bf16(mean, sd);
        if (lane == 0) __hip_atomic_store((GAS u64*)(p.ws + WS_STATS) + row, ((u64)tag << 32) | pay, RLX_AGENT);
    }
    STAMP(p.ws, 1);
    if (!conv_blk) {
        int t0 = ((b - 224) * 8 + wave) * 2;
        pretile_task(p, t0, lane);
        pretile_task(p, t0 + 1, lane);
        STAMP(p.ws, 2);
        return;
    }
    if (b < 76 && wave == 7) pretile_task(p, 512 + b, lane);

    {
        *(LAS v2u*)(sm + K1_KP + wave * 1536 + lane * 8) = v2u{0, 0};
        if (tid < 32) *(LAS u32*)(sm + K1_U + (tid >> 4) * 4160 + (tid & 15) * 4) = 0;
    }
    float unscale;
    LAS char* xy = sm + K1_XY + wave * XY_WAVE;
    {
        const float dt = rbf(__expf(bflo(r_ldt)));
        const float ar = dt * bflo(r_are), ai = dt * bflo(r_aim);
        const float br = dt * bflo(r_bre), bi = dt * bflo(r_bim);
        const float cr = bflo(r_cre), ci = bflo(r_cim);
        cf cb = {cr * br - ci * bi, cr * bi + ci * br};
        const float a2 = ar * 1.44269504088896340736f;
        const float rh = ai * 0.15915494309189535f;
        const float rl = __builtin_fmaf(ai, 0.15915494309189535f, -rh) + ai * 6.4206382e-9f;
        auto cexp = [&](float s) -> cf {
            float e = __builtin_amdgcn_exp2f(a2 * s);
            float r = __builtin_amdgcn_fractf(rh * s) + rl * s;
            return {e * __builtin_amdgcn_cosf(r), e * __builtin_amdgcn_sinf(r)};
        };
        const float grow = fmaxf(1.f, __builtin_amdgcn_exp2f(a2 * 2048.f));
        const float mag = (fabsf(cb.r) + fabsf(cb.i)) * grow;
        const float mx = wave_max(mag);
        const float sb = wave_sum(mag);
        if (lane == 0) *(LAS float*)(sm + K1_SB + wave * 4) = sb;
        int ex = (int)((__builtin_bit_cast(u32, mx) >> 23) & 255);
        ex = min(max(ex, 30), 230);
        const float sx = __builtin_bit_cast(float, (u32)(127 + 14 + 126 - ex) << 23);
        unscale = __builtin_bit_cast(float, (u32)(127 - 14 - 126 + ex) << 23);
        cb.r *= sx; cb.i *= sx;

        const cf e1 = cexp(1.f), e16 = cexp(16.f), e256 = cexp(256.f);
        const cf e2 = csq(e1), e4 = csq(e2), e8 = csq(e4);
        const cf e32 = csq(e16), e64 = csq(e32), e128 = csq(e64);
        const cf e512 = csq(e256), e1024 = csq(e512);
        LAS char* w = xy + lane * 4;
        auto putY = [&](int q, cf v) { *(LAS u32*)(w + q * XY_STRIDE) = pk_f16(v.r, v.i); };
        auto putX = [&](int q, cf v) { *(LAS u32*)(w + (32 + q) * XY_STRIDE) = pk_f16(v.r, -v.i); };
        cf y[8];
        y[0] = {1.f, 0.f}; y[1] = e8; y[2] = e16; y[3] = cmul(e8, e16); y[4] = e32;
        y[5] = cmul(y[1], e32); y[6] = cmul(y[2], e32); y[7] = cmul(y[3], e32);
        const cf e192 = cmul(e64, e128);
#pragma unroll
        for (int q = 0; q < 8; q++) putY(q, y[q]);
        putY(8, e64); putY(16, e128); putY(24, e192);
#pragma unroll
        for (int q = 1; q < 8; q++) { putY(8 + q, cmul(y[q], e64)); putY(16 + q, cmul(y[q], e128)); putY(24 + q, cmul(y[q], e192)); }
        const cf x0 = cb, x1 = cmul(cb, e1), x2 = cmul(cb, e2), x3 = cmul(x1, e2);
        const cf x4 = cmul(x0, e4), x5 = cmul(x1, e4), x6 = cmul(x2, e4), x7 = cmul(x3, e4), x8 = cmul(x0, e8);
        putX(0, x0); putX(1, x1); putX(2, x2); putX(3, x3); putX(4, x4); putX(5, x5); putX(6, x6); putX(7, x7);
        const cf x256 = cmul(x0, e256), x260 = cmul(x4, e256), x264 = cmul(x8, e256);
        putX(8, x256); putX(9, cmul(x2, e256)); putX(10, x260); putX(11, cmul(x6, e256)); putX(12, x264);
        const cf x512 = cmul(x0, e512), x520 = cmul(x8, e512), x768 = cmul(x256, e512), x776 = cmul(x264, e512);
        putX(13, x512); putX(14, cmul(x4, e512)); putX(15, x520);
        putX(16, x768); putX(17, cmul(x260, e512)); putX(18, x776);
        putX(19, cmul(x0, e1024)); putX(20, cmul(x8, e1024)); putX(21, cmul(x256, e1024)); putX(22, cmul(x264, e1024));
        putX(23, cmul(x512, e1024)); putX(24, cmul(x520, e1024)); putX(25, cmul(x768, e1024)); putX(26, cmul(x776, e1024));
    }
    STAMP(p.ws, 2);
    f32x16 kd;
    {
        const int r = lane & 31, h = lane >> 5;
        const int g = r >> 3, hh = (r >> 2) & 1, i = r & 3;
        const int xi = g == 0 ? 4 * hh + i : g == 1 ? 8 + 2 * hh + i : g == 2 ? 13 + 3 * hh + i : 19 + 4 * hh + i;
        LAS char* pa = xy + (32 + xi) * XY_STRIDE + h * 16;
        LAS char* pb = xy + r * XY_STRIDE + h * 16;
#pragma unroll
        for (int i2 = 0; i2 < 16; i2++) kd[i2] = 0.f;
        __builtin_amdgcn_fence(__ATOMIC_RELEASE, "wavefront");
        __builtin_amdgcn_wave_barrier();
#pragma unroll
        for (int s = 0; s < 8; s++) kd = mfma_f16(*(LAS v4u*)(pa + s * 32), *(LAS v4u*)(pb + s * 32), kd);
#pragma unroll
        for (int i2 = 0; i2 < 16; i2++) kd[i2] *= unscale;
        __builtin_amdgcn_wave_barrier();
#pragma unroll
        for (int g2 = 0; g2 < 4; g2++)
            *(LAS f32x4*)(xy + g2 * 1024 + lane * 16) = f32x4{kd[4 * g2], kd[4 * g2 + 1], kd[4 * g2 + 2], kd[4 * g2 + 3]};
    }
    STAMP(p.ws, 3);
    u32 udk[4];
    {
        GAS u64* g = (GAS u64*)(p.ws + WS_STATS) + 4 * tid;
        u64 gr[4];
        for (int spins = 0; spins < (1 << 20); spins++) {
            bool ok = true;
#pragma unroll
            for (int i = 0; i < 4; i++) { gr[i] = __hip_atomic_load(g + i, RLX_AGENT); ok &= (u32)(gr[i] >> 32) == tag; }
            if (__all(ok)) break;
            __builtin_amdgcn_s_sleep(1);
        }
        float u0[4], u1[4];
#pragma unroll
        for (int i = 0; i < 4; i++) {
            const float m = bflo((u32)gr[i]), rs = 1.f / bfhi((u32)gr[i]);
            const u32 dd = pk_bf16(bflo(xc[i]) - m, bfhi(xc[i]) - m);
            const u32 uu = pk_bf16(bflo(dd) * rs, bfhi(dd) * rs);
            u0[i] = bflo(uu); u1[i] = bfhi(uu);
            udk[i] = pk_bf16(u0[i] * bflo(r_dsk), u1[i] * bfhi(r_dsk));
        }
        *(LAS v2u*)(sm + K1_U + 64 + tid * 8) = v2u{pk_f16(u0[0], u0[1]), pk_f16(u0[2], u0[3])};
        *(LAS v2u*)(sm + K1_U + 4160 + 64 + tid * 8) = v2u{pk_f16(u1[0], u1[1]), pk_f16(u1[2], u1[3])};
    }
    STAMP(p.ws, 4);
    __syncthreads();
    if (b == 0 && tid == 0) __hip_atomic_store((GAS u32*)(p.ws + WS_EPOCH), tag, RLX_AGENT);
    float ik0, ik1;
    {
        const f32x4 s0 = *(LAS f32x4*)(sm + K1_SB), s1 = *(LAS f32x4*)(sm + K1_SB + 16);
        const float b0 = (s0[0] + s0[1]) + (s0[2] + s0[3]), b1 = (s1[0] + s1[1]) + (s1[2] + s1[3]);
        int e0 = (int)((__builtin_bit_cast(u32, b0) >> 23) & 255), e1 = (int)((__builtin_bit_cast(u32, b1) >> 23) & 255);
        e0 = min(max(e0, 30), 230); e1 = min(max(e1, 30), 230);
        ik0 = __builtin_bit_cast(float, (u32)(127 - 15 - 126 + e0) << 23);
        ik1 = __builtin_bit_cast(float, (u32)(127 - 15 - 126 + e1) << 23);
        const float sk = __builtin_bit_cast(float, (u32)(127 + 15 + 126 - (ch ? e1 : e0)) << 23);
        LAS char* ex = sm + K1_XY + (ch * 4) * XY_WAVE + wq * 1024 + lane * 16;
        const f32x4 a0 = *(LAS f32x4*)(ex), a1 = *(LAS f32x4*)(ex + XY_WAVE), a2 = *(LAS f32x4*)(ex + 2 * XY_WAVE), a3 = *(LAS f32x4*)(ex + 3 * XY_WAVE);
        const f32x4 v = ((a0 + a1) + (a2 + a3)) * sk;
        const int q = lane & 31, h = lane >> 5;
        LAS char* kp = sm + K1_KP + ch * 4 * 1536 + 512;
        const u32 p01 = pk_f16(v[0], v[1]), p23 = pk_f16(v[2], v[3]);
        if (wq == 0) {
            LAS char* o = kp + (2 * q + h) * 2;
            *(LAS u16*)(o) = (u16)p01; *(LAS u16*)(o + 1536) = (u16)(p01 >> 16);
            *(LAS u16*)(o + 2 * 1536) = (u16)p23; *(LAS u16*)(o + 3 * 1536) = (u16)(p23 >> 16);
        } else if (wq == 1) {
            LAS char* o = kp + (64 + 2 * q + h) * 2;
            *(LAS u16*)(o) = (u16)p01; *(LAS u16*)(o + 1536) = (u16)p01;
            *(LAS u16*)(o + 2 * 1536) = (u16)(p01 >> 16); *(LAS u16*)(o + 3 * 1536) = (u16)p23;
        } else if (wq == 2) {
            LAS char* o = kp + (64 * (2 + h) + 2 * q) * 2;
            *(LAS u32*)(o) = p01; *(LAS u32*)(o + 1536) = p01;
            *(LAS u32*)(o + 2 * 1536) = (p01 & 0xffffu) | (p23 << 16);
            *(LAS u32*)(o + 3 * 1536) = (p01 >> 16) | (p23 << 16);
        } else {
            LAS char* o = kp + (64 * (4 + 2 * h) + 2 * q) * 2;
            const bool ev = (q & 1) == 0;
            *(LAS u32*)(o) = ev ? ((p01 & 0xffffu) | (p01 << 16)) : p01;
            *(LAS u32*)(o + 1536) = p01; *(LAS u32*)(o + 2 * 1536) = p01; *(LAS u32*)(o + 3 * 1536) = p01;
            *(LAS u32*)(o + 128) = ev ? ((p23 & 0xffffu) | (p23 << 16)) : p23;
            *(LAS u32*)(o + 128 + 1536) = p23; *(LAS u32*)(o + 128 + 2 * 1536) = p23; *(LAS u32*)(o + 128 + 3 * 1536) = p23;
        }
    }
    __syncthreads();
    STAMP(p.ws, 5);
    {
        const int G = lane >> 4, h = lane >> 5, q = (lane & 15) >> 2, pp = lane & 3;
        LAS char* ua = sm + K1_U + ch * 4160 + 2 * (32 + 32 + 16 * (G & 1) - 32 * h - 16 - 4 * q + 4 * pp);
        LAS char* ka = sm + K1_KP + wave * 1536 + 2 * (256 - 248 + 8 * (lane & 31) + 8 * h);
        f32x16 acc0, acc1;
#pragma unroll
        for (int i = 0; i < 16; i++) { acc0[i] = 0.f; acc1[i] = 0.f; }
#pragma unroll
        for (int n = 0; n < 16; n++) {
            const int B0 = 992 - 64 * n;
            const v2u lo = lds_tr(ua + 2 * (B0 - 32) + 32), hi = lds_tr(ua + 2 * (B0 - 32));
            const v4u a = {lo[0], lo[1], hi[0], hi[1]};
            acc0 = mfma_f16(a, *(LAS v4u*)(ka + 32 * n), acc0);
            acc1 = mfma_f16(a, *(LAS v4u*)(ka + 32 * n + 512), acc1);
        }
        LAS char* pw = sm + K1_XY + wave * P_WAVE + (lane & 31) * 144 + h * 16;
#pragma unroll
        for (int g = 0; g < 4; g++) *(LAS f32x4*)(pw + g * 32) = f32x4{acc0[4 * g], acc0[4 * g + 1], acc0[4 * g + 2], acc0[4 * g + 3]};
#pragma unroll
        for (int n = 0; n < 16; n++) {
            const int B0 = 2016 - 64 * n;
            const v2u lo = lds_tr(ua + 2 * (B0 - 32) + 32), hi = lds_tr(ua + 2 * (B0 - 32));
            const v4u a = {lo[0], lo[1], hi[0], hi[1]};
            acc1 = mfma_f16(a, *(LAS v4u*)(ka + 32 * n), acc1);
        }
#pragma unroll
        for (int g = 0; g < 4; g++) *(LAS f32x4*)(pw + 32 * 144 + g * 32) = f32x4{acc1[4 * g], acc1[4 * g + 1], acc1[4 * g + 2], acc1[4 * g + 3]};
    }
    STAMP(p.ws, 6);
    __syncthreads();
    {
        const int c1 = tid, c0 = tid > 0 ? tid - 1 : 0;
        const int o1 = (c1 >> 3) * 144 + (c1 & 7) * 16, o0 = (c0 >> 3) * 144 + (c0 & 7) * 16;
        const float z = tid > 0 ? 1.f : 0.f;
#pragma unroll
        for (int c = 0; c < 2; c++) {
            LAS char* pb = sm + K1_XY + c * 4 * P_WAVE;
            const f32x4 r0 = *(LAS f32x4*)(pb + o1);
            const f32x4 r1 = *(LAS f32x4*)(pb + P_WAVE + o1), l1 = *(LAS f32x4*)(pb + P_WAVE + o0) * z;
            const f32x4 r2 = *(LAS f32x4*)(pb + 2 * P_WAVE + o1), l2 = *(LAS f32x4*)(pb + 2 * P_WAVE + o0) * z;
            const f32x4 r3 = *(LAS f32x4*)(pb + 3 * P_WAVE + o1), l3 = *(LAS f32x4*)(pb + 3 * P_WAVE + o0) * z;
            const float ik = c ? ik1 : ik0;
            float y[4];
            y[0] = ((r0[0] + l1[3]) + (l2[2] + l3[1])) * ik;
            y[1] = ((r0[1] + r1[0]) + (l2[3] + l3[2])) * ik;
            y[2] = ((r0[2] + r1[1]) + (r2[0] + l3[3])) * ik;
            y[3] = ((r0[3] + r1[2]) + (r2[1] + r3[0])) * ik;
            float hv[4];
#pragma unroll
            for (int i = 0; i < 4; i++) {
                const float ud = c ? bfhi(udk[i]) : bflo(udk[i]);
                const float xv = c ? bfhi(xc[i]) : bflo(xc[i]);
                hv[i] = xv + rbf(rbf(y[i]) + ud);
            }
            *(v2u*)(p.ws + WS_HT + ((size_t)(d0 + c) * L + 4 * tid) * 2) = v2u{pk_bf16(hv[0], hv[1]), pk_bf16(hv[2], hv[3])};
        }
    }
    STAMP(p.ws, 7);
}

constexpr int K2_H = 0;
constexpr int K2_ST = D * 256;
constexpr int K2_CS = K2_ST + 4 * 128 * 8;
constexpr int K2_LDS = K2_CS + 8 * 128;

template <int MODE>
__device__ __forceinline__ f32x16 k2_tile(const v4u (&wf)[28], LAS char* a0, LAS char* a1, float& s1, float& s2) {
    f32x16 acc;
#pragma unroll
    for (int i = 0; i < 16; i++) acc[i] = 0.f;
#pragma unroll
    for (int ks = 0; ks < 28; ks++) {
        const v2u lo = lds_tr(a0 + ks * 4096), hi = lds_tr(a1 + ks * 4096);
        const v4u bfr = {lo[0], lo[1], hi[0], hi[1]};
        acc = mfma_bf16(wf[ks], bfr, acc);
        if (MODE != 0 && (ks & 1) == MODE - 1) {
#pragma unroll
            for (int i = 0; i < 4; i++) { s1 = dot2bf(bfr[i], 0x3f803f80u, s1); s2 = dot2bf(bfr[i], bfr[i], s2); }
        }
    }
    return acc;
}

__global__ __launch_bounds__(512, 2) void k2_ffn1(char* ws) {
    extern __shared__ __attribute__((aligned(16))) char smem_[];
    LAS char* sm = (LAS char*)smem_;
    const int tid = threadIdx.x, lane = tid & 63;
    const int wave = __builtin_amdgcn_readfirstlane(tid >> 6);
    const int b = blockIdx.x;
    STAMP(ws, 8);
    const int xcd = b & 7, i28 = b >> 3;
    const int tau = (xcd >> 1) * 4 + (i28 & 3), phi = (xcd & 1) * 7 + (i28 >> 2);
    {
        const int swz = ((lane >> 4) << 2) | (wave & 3);
        const char* src = ws + WS_HT + ((size_t)(4 * wave + (lane >> 4)) * L + tau * 128) * 2 + (((lane & 15) ^ swz) << 4);
#pragma unroll
        for (int i = 0; i < 14; i++)
            __builtin_amdgcn_global_load_lds((const GAS u32*)(src + (size_t)i * 32 * L * 2), (LAS u32*)(sm + K2_H + (wave + 8 * i) * 1024), 16, 0, 0);
    }
    v4u wf[28];
    {
        const v4u* wsrc = (const v4u*)(ws + WS_W1T) + (size_t)(phi * 8 + wave) * 28 * 64 + lane;
#pragma unroll
        for (int ks = 0; ks < 28; ks++) wf[ks] = wsrc[ks * 64];
    }
    const int h = lane >> 5, col = lane & 31;
    {
        float cs = 0.f;
#pragma unroll
        for (int ks = 0; ks < 28; ks++)
#pragma unroll
            for (int i = 0; i < 4; i++) cs = dot2bf(wf[ks][i], 0x3f803f80u, cs);
        cs += __shfl_xor(cs, 32);
        if (lane < 32) *(LAS float*)(sm + K2_CS + wave * 128 + lane * 4) = cs;
    }
    __syncthreads();
    STAMP(ws, 9);
    const int G1 = (lane >> 4) & 1, q = (lane & 15) >> 2, pp = lane & 3;
    const int rowoff = (8 * h + q) * 256 + 8 * (pp & 1);
    const int lowc = 2 * G1 + (pp >> 1);
    f32x4 cs4[4];
#pragma unroll
    for (int g = 0; g < 4; g++) cs4[g] = *(LAS f32x4*)(sm + K2_CS + wave * 128 + g * 32 + h * 16);
    char* actw = ws + WS_ACT + ((size_t)(tau * 4) * 112 + phi * 8 + wave) * 1024 + lane * 16;
#pragma unroll 1
    for (int it = 0; it < 4; it++) {
        const int tt = (wave + it) & 3;
        LAS char* a0 = sm + K2_H + rowoff + 16 * ((4 * (tt ^ q)) | (lowc ^ (2 * h)));
        LAS char* a1 = sm + K2_H + rowoff + 1024 + 16 * ((4 * (tt ^ q)) | (lowc ^ (2 * h + 1)));
        f32x16 acc;
        float s1 = 0.f, s2 = 0.f;
        if (it == 0) {
            if (wave < 4) acc = k2_tile<1>(wf, a0, a1, s1, s2); else acc = k2_tile<2>(wf, a0, a1, s1, s2);
            *(LAS f32x2*)(sm + K2_ST + (((wave >> 2) * 2 + h) * 128 + tt * 32 + col) * 8) = f32x2{s1, s2};
            __syncthreads();
        } else {
            acc = k2_tile<0>(wf, a0, a1, s1, s2);
        }
        LAS char* st = sm + K2_ST + (tt * 32 + col) * 8;
        const f32x2 t = (*(LAS f32x2*)(st) + *(LAS f32x2*)(st + 1024)) + (*(LAS f32x2*)(st + 2048) + *(LAS f32x2*)(st + 3072));
        const float mean = t[0] * (1.f / D);
        const float var = rbf(fmaxf(t[1] * (1.f / D) - mean * mean, 0.f));
        const float rs = 1.f / rbf(__builtin_sqrtf(rbf(var + 1e-5f)));
        const float nm = -rbf(mean);
        float o[8];
#pragma unroll
        for (int j = 0; j < 8; j++) {
            const float g = rbf(__builtin_fmaf(nm, cs4[j >> 2][j & 3], acc[j]) * rs);
            const float u = rbf(__builtin_fmaf(nm, cs4[2 + (j >> 2)][j & 3], acc[8 + j]) * rs);
            const float a = g * __builtin_fmaf(g * g, -0.10294324f, -2.3022082f);
            const float e = __builtin_amdgcn_exp2f(a);
            const float ge = rbf(g * __builtin_amdgcn_rcpf(1.f + e));
            o[j] = ge * u;
        }
        *(v4u*)(actw + (size_t)tt * 112 * 1024) = v4u{pk_bf16(o[0], o[1]), pk_bf16(o[2], o[3]), pk_bf16(o[4], o[5]), pk_bf16(o[6], o[7])};
    }
    STAMP(ws, 10);
}

constexpr int K3_LDS = 8 * 16 * 1024;

__global__ __launch_bounds__(512, 2) void k3_ffn2(char* ws, u16* out) {
    extern __shared__ __attribute__((aligned(16))) char smem_[];
    LAS char* sm = (LAS char*)smem_;
    const int tid = threadIdx.x, lane = tid & 63;
    const int wave = __builtin_amdgcn_readfirstlane(tid >> 6);
    const int b = blockIdx.x;
    STAMP(ws, 12);
    const int xcd = b & 7, i28 = b >> 3;
    const int tau = xcd * 4 + (i28 & 3), del = i28 >> 2;
    const int h = lane >> 5, col = lane & 31;
    const v4u* wsrc = (const v4u*)(ws + WS_W2T) + ((size_t)(2 * del) * 112 + wave * 14) * 64 + lane;
    const v4u* asrc = (const v4u*)(ws + WS_ACT) + ((size_t)(2 * tau) * 112 + wave * 14) * 64 + lane;
    const int td = wave >> 2, tc = (wave >> 1) & 1, half = wave & 1;
    const int dd = 64 * del + 32 * td + 16 * half + 8 * h, tok = 64 * tau + 32 * tc + col;
    u16 hr[8];
#pragma unroll
    for (int j = 0; j < 8; j++) hr[j] = *(const u16*)(ws + WS_HT + ((size_t)(dd + j) * L + tok) * 2);
    f32x16 acc[2][2];
#pragma unroll
    for (int i = 0; i < 16; i++) { acc[0][0][i] = 0.f; acc[0][1][i] = 0.f; acc[1][0][i] = 0.f; acc[1][1][i] = 0.f; }
    constexpr int WIN = 10;
    v4u w0[14], w1[14], a0[14], a1[14];
#pragma unroll
    for (int ks = 0; ks < WIN; ks++) { w0[ks] = wsrc[ks * 64]; a0[ks] = asrc[ks * 64]; w1[ks] = wsrc[(112 + ks) * 64]; a1[ks] = asrc[(112 + ks) * 64]; }
    __builtin_amdgcn_sched_barrier(0);
#pragma unroll
    for (int ks = 0; ks < 14; ks++) {
        acc[0][0] = mfma_bf16(w0[ks], a0[ks], acc[0][0]);
        acc[1][0] = mfma_bf16(w1[ks], a0[ks], acc[1][0]);
        acc[0][1] = mfma_bf16(w0[ks], a1[ks], acc[0][1]);
        acc[1][1] = mfma_bf16(w1[ks], a1[ks], acc[1][1]);
        if (ks + WIN < 14) {
            const int k2 = ks + WIN;
            w0[k2] = wsrc[k2 * 64]; a0[k2] = asrc[k2 * 64]; w1[k2] = wsrc[(112 + k2) * 64]; a1[k2] = asrc[(112 + k2) * 64];
        }
        __builtin_amdgcn_sched_barrier(0);
    }
    STAMP(ws, 13);
#pragma unroll
    for (int o = 0; o < 8; o++) {
        const f32x16& t = acc[o >> 2][(o >> 1) & 1];
        const int r = 8 * (o & 1);
        LAS char* dst = sm + (o * 8 + wave) * 2048 + lane * 16;
        *(LAS f32x4*)(dst) = f32x4{t[r], t[r + 1], t[r + 2], t[r + 3]};
        *(LAS f32x4*)(dst + 1024) = f32x4{t[r + 4], t[r + 5], t[r + 6], t[r + 7]};
    }
    __syncthreads();
    f32x4 s0 = {0, 0, 0, 0}, s1 = {0, 0, 0, 0};
#pragma unroll
    for (int s = 0; s < 8; s++) {
        LAS char* src = sm + (wave * 8 + s) * 2048 + lane * 16;
        s0 += *(LAS f32x4*)(src); s1 += *(LAS f32x4*)(src + 1024);
    }
    float o[8];
#pragma unroll
    for (int j = 0; j < 8; j++) o[j] = bflo(hr[j]) + rbf(j < 4 ? s0[j & 3] : s1[j & 3]);
    *(v4u*)(out + (size_t)tok * D + dd) = v4u{pk_bf16(o[0], o[1]), pk_bf16(o[2], o[3]), pk_bf16(o[4], o[5]), pk_bf16(o[6], o[7])};
    STAMP(ws, 14);
}

}

extern "C" void kernel_launch(void* const* d_in, const int* in_sizes, int n_in,
                              void* d_out, int out_size, void* d_ws, size_t ws_size,
                              hipStream_t stream) {
    P1 p;
    p.x = (const u16*)d_in[0]; p.Are = (const u16*)d_in[1]; p.Aim = (const u16*)d_in[2];
    p.Bre = (const u16*)d_in[3]; p.Bim = (const u16*)d_in[4]; p.Cre = (const u16*)d_in[5]; p.Cim = (const u16*)d_in[6];
    p.logdt = (const u16*)d_in[7]; p.Dskip = (const u16*)d_in[8];
    p.wg = (const u16*)d_in[10]; p.wu = (const u16*)d_in[11]; p.wd = (const u16*)d_in[12];
    p.ws = (char*)d_ws;
    (void)hipFuncSetAttribute((const void*)k1_ssm, hipFuncAttributeMaxDynamicSharedMemorySize, K1_LDS);
    (void)hipFuncSetAttribute((const void*)k2_ffn1, hipFuncAttributeMaxDynamicSharedMemorySize, K2_LDS);
    (void)hipFuncSetAttribute((const void*)k3_ffn2, hipFuncAttributeMaxDynamicSharedMemorySize, K3_LDS);
    k1_ssm<<<256, 512, K1_LDS, stream>>>(p);
    k2_ffn1<<<224, 512, K2_LDS, stream>>>((char*)d_ws);
    k3_ffn2<<<224, 512, K3_LDS, stream>>>((char*)d_ws, (u16*)d_out);
}
```

```cpp
#include <hip/hip_runtime.h>
#include <stdint.h>

namespace {

constexpr int L = 2048, D = 448, N = 256, F = 1792;

typedef unsigned short u16;
typedef unsigned int u32;
typedef unsigned long long u64;
typedef u32 v4u __attribute__((ext_vector_type(4)));
typedef u32 v2u __attribute__((ext_vector_type(2)));
typedef float f32x2 __attribute__((ext_vector_type(2)));
typedef float f32x4 __attribute__((ext_vector_type(4)));
typedef float f32x16 __attribute__((ext_vector_type(16)));
typedef _Float16 f16x2 __attribute__((ext_vector_type(2)));
typedef _Float16 f16x8 __attribute__((ext_vector_type(8)));
typedef __bf16 bf16x2 __attribute__((ext_vector_type(2)));
typedef __bf16 bf16x8 __attribute__((ext_vector_type(8)));
typedef short s16x4 __attribute__((ext_vector_type(4)));
#define GAS __attribute__((address_space(1)))
#define LAS __attribute__((address_space(3)))
#define RLX_AGENT __ATOMIC_RELAXED, __HIP_MEMORY_SCOPE_AGENT
#define SB() __builtin_amdgcn_sched_barrier(0)
#define BARRIER() asm volatile("s_waitcnt lgkmcnt(0)\n\ts_barrier" ::: "memory")
#define BARRIER_RAW() asm volatile("s_barrier" ::: "memory")
constexpr int SC1 = 16;

constexpr int WS_EPOCH = 0;
constexpr int WS_WF1 = 4096;
constexpr int WS_WF2 = WS_WF1 + 14 * 128;
constexpr int WS_HF = 8192;
constexpr int WS_AF = WS_HF + 64 * 128;
constexpr int WS_XU = 65536;
constexpr int WS_W1T = WS_XU + 224 * 32768;
constexpr int WS_W2T = WS_W1T + 112 * 28 * 1024;
constexpr int WS_HT = WS_W2T + 14 * 112 * 1024;
constexpr int WS_ACT = WS_HT + D * L * 2;
constexpr int WS_DBG = WS_ACT + 64 * 112 * 1024;
constexpr int NSTAMP = 32;
constexpr int WS_END = WS_DBG + 256 * NSTAMP * 8;
constexpr int SPIN_CAP = 1 << 16;

#define STAMP(k) do {} while (0)

__device__ __forceinline__ float bflo(u32 v) { return __builtin_bit_cast(float, v << 16); }
__device__ __forceinline__ float bfhi(u32 v) { return __builtin_bit_cast(float, v & 0xffff0000u); }
__device__ __forceinline__ u32 pk_bf16(float a, float b) { f32x2 f = {a, b}; return __builtin_bit_cast(u32, __builtin_convertvector(f, bf16x2)); }
__device__ __forceinline__ u32 pk_f16(float a, float b) { f32x2 f = {a, b}; return __builtin_bit_cast(u32, __builtin_convertvector(f, f16x2)); }
__device__ __forceinline__ float rbf(float a) { return bflo(pk_bf16(a, 0.f)); }
__device__ __forceinline__ float dot2bf(u32 a, u32 b, float c) {
    return __builtin_amdgcn_fdot2_f32_bf16(__builtin_bit_cast(bf16x2, a), __builtin_bit_cast(bf16x2, b), c, false);
}
__device__ __forceinline__ f32x16 mfma_bf16(v4u a, v4u b, f32x16 c) {
    return __builtin_amdgcn_mfma_f32_32x32x16_bf16(__builtin_bit_cast(bf16x8, a), __builtin_bit_cast(bf16x8, b), c, 0, 0, 0);
}
__device__ __forceinline__ f32x16 mfma_f16(v4u a, v4u b, f32x16 c) {
    return __builtin_amdgcn_mfma_f32_32x32x16_f16(__builtin_bit_cast(f16x8, a), __builtin_bit_cast(f16x8, b), c, 0, 0, 0);
}
__device__ __forceinline__ v2u lds_tr(LAS char* p) {
    return __builtin_bit_cast(v2u, __builtin_amdgcn_ds_read_tr16_b64_v4i16((LAS s16x4*)p));
}
__device__ __forceinline__ v4u lds_tr2(LAS char* p0, LAS char* p1) {
    const v2u lo = lds_tr(p0), hi = lds_tr(p1);
    return v4u{lo[0], lo[1], hi[0], hi[1]};
}
template <int CTRL> __device__ __forceinline__ float dpp(float v) {
    return __builtin_bit_cast(float, __builtin_amdgcn_update_dpp(0, __builtin_bit_cast(int, v), CTRL, 0xf, 0xf, true));
}
__device__ __forceinline__ float rdl(float v, int l) { return __builtin_bit_cast(float, __builtin_amdgcn_readlane(__builtin_bit_cast(int, v), l)); }
__device__ __forceinline__ float wave_sum(float v) {
    v += dpp<0xB1>(v); v += dpp<0x4E>(v); v += dpp<0x141>(v); v += dpp<0x140>(v);
    return (rdl(v, 0) + rdl(v, 16)) + (rdl(v, 32) + rdl(v, 48));
}
__device__ __forceinline__ float wave_max(float v) {
    v = fmaxf(v, dpp<0xB1>(v)); v = fmaxf(v, dpp<0x4E>(v)); v = fmaxf(v, dpp<0x141>(v)); v = fmaxf(v, dpp<0x140>(v));
    return fmaxf(fmaxf(rdl(v, 0), rdl(v, 16)), fmaxf(rdl(v, 32), rdl(v, 48)));
}
__device__ __forceinline__ float half_sum(float v) {
    const auto r = __builtin_amdgcn_permlane32_swap(__builtin_bit_cast(u32, v), __builtin_bit_cast(u32, v), false, false);
    return __builtin_bit_cast(float, (u32)r[0]) + __builtin_bit_cast(float, (u32)r[1]);
}
template <int I> struct IC { static constexpr int v = I; };
template <int B, int E, class Fn> __device__ __forceinline__ void static_for(Fn&& f) {
    if constexpr (B < E) { f(IC<B>{}); static_for<B + 1, E>(f); }
}
struct cf { float r, i; };
__device__ __forceinline__ cf cmul(cf a, cf b) { return {a.r * b.r - a.i * b.i, a.r * b.i + a.i * b.r}; }
__device__ __forceinline__ cf csq(cf a) { return {a.r * a.r - a.i * a.i, 2.f * a.r * a.i}; }
__device__ __forceinline__ v4u ldnt(const void* p) { return __builtin_nontemporal_load((const v4u*)p); }

typedef __amdgpu_buffer_rsrc_t rsrc_t;
__device__ __forceinline__ v4u ld16(rsrc_t r, int voff, int soff) { return __builtin_amdgcn_raw_buffer_load_b128(r, voff, soff, SC1); }
__device__ __forceinline__ void st16(rsrc_t r, int voff, int soff, v4u v) { __builtin_amdgcn_raw_buffer_store_b128(v, r, voff + soff, 0, SC1); }
__device__ __forceinline__ void st4(rsrc_t r, int voff, int soff, u32 v) { __builtin_amdgcn_raw_buffer_store_b32(v, r, voff, soff, SC1); }
#define WAIT_VM(n) asm volatile("s_waitcnt vmcnt(" #n ")" ::: "memory")

__device__ __forceinline__ void wait_flags(char* ws, int lane_off, u32 tag) {
    GAS u32* f = (GAS u32*)(ws + lane_off);
#pragma unroll 1
    for (int spins = 0; spins < SPIN_CAP; spins++) {
        const u32 v = __hip_atomic_load(f, RLX_AGENT);
        if (__all(v == tag)) break;
        __builtin_amdgcn_s_sleep(2);
    }
    __builtin_amdgcn_fence(__ATOMIC_ACQUIRE, "wavefront");
}
__device__ __forceinline__ void set_flag(char* ws, int off, int lane, u32 tag) {
    if (lane == 0) __hip_atomic_store((GAS u32*)(ws + off), tag, RLX_AGENT);
}
__device__ __forceinline__ void glds16(const char* gsrc, u32 lds_dst) {
    u32 keep;
    asm volatile("s_mov_b32 %0, m0\n\ts_mov_b32 m0, %2\n\ts_nop 0\n\tglobal_load_lds_dwordx4 %1, off sc1\n\ts_mov_b32 m0, %0"
                 : "=&s"(keep) : "v"(gsrc), "s"(lds_dst) : "memory");
}

struct Params {
    const u16 *x, *Are, *Aim, *Bre, *Bim, *Cre, *Cim, *logdt, *Dskip, *wg, *wu, *wd;
    char* ws;
    u16* out;
};

constexpr int XY_STRIDE = 272, XY_ROWS = 59, XY_WAVE = XY_STRIDE * XY_ROWS;
constexpr int P_WAVE = 64 * 144;
constexpr int LD_HB = 0, LD_HA = 73728;
constexpr int LD_KP = 131072;
constexpr int LD_U = LD_KP + 8 * 1536;
constexpr int LD_SB = LD_U + 2 * 4160;
constexpr int LD_LN = LD_SB + 64;
constexpr int LD_ST = LD_LN + 64;
constexpr int LD_CS = LD_ST + 4096;
constexpr int LDS_BYTES = LD_CS + 1024;

__device__ __forceinline__ void transpose_store(const v4u (&in)[8], rsrc_t rs, int voff, int cstride_lo, int cstride_hi) {
#pragma unroll
    for (int c = 0; c < 8; c++) {
        v4u o;
#pragma unroll
        for (int kp = 0; kp < 4; kp++)
            o[kp] = __builtin_amdgcn_perm(in[2 * kp + 1][c >> 1], in[2 * kp][c >> 1], (c & 1) ? 0x07060302u : 0x05040100u);
        st16(rs, voff, (c & 3) * cstride_lo + (c >> 2) * cstride_hi, o);
    }
}
__device__ __forceinline__ void w1_task_load(const Params& p, int task, int lane, v4u (&in)[8]) {
    const int t = task < 0 ? 0 : task;
    const int phi = t / 28, r = t - phi * 28, mat = r >= 14, kp = r - 14 * mat;
    const rsrc_t rw = __builtin_amdgcn_make_buffer_rsrc((void*)(mat ? p.wu : p.wg), 0, task < 0 ? 0 : D * F * 2, 0x00020000);
    const int voff = (8 * (lane >> 4) * F + (lane & 15) * 8) * 2;
#pragma unroll
    for (int k = 0; k < 8; k++) in[k] = __builtin_amdgcn_raw_buffer_load_b128(rw, voff, ((32 * kp + k) * F + phi * 128) * 2, 2);
}
__device__ __forceinline__ void w1_task_store(rsrc_t rs, int task, int lane, const v4u (&in)[8]) {
    const int phi = task / 28, r = task - phi * 28, mat = r >= 14, kp = r - 14 * mat;
    transpose_store(in, rs, WS_W1T + (phi * 8 * 28 + 2 * kp) * 1024 + (lane >> 4) * 512 + mat * 256 + (lane & 15) * 16, 28 * 1024, 4 * 28 * 1024);
}
__device__ __forceinline__ void w2_task_load(const Params& p, int task, int lane, v4u (&in)[8]) {
    const int del = task / 28, kg = task - del * 28;
    const int ks = kg * 4 + ((lane >> 3) & 3), h = lane >> 5;
    const u16* src = p.wd + (size_t)((ks >> 3) * 128 + 32 * h + (ks & 7)) * D + del * 64 + (lane & 7) * 8;
#pragma unroll
    for (int j = 0; j < 8; j++) in[j] = ldnt(src + (size_t)(8 * ((j & 3) + 8 * (j >> 2))) * D);
}
__device__ __forceinline__ void w2_task_store(rsrc_t rs, int task, int lane, const v4u (&in)[8]) {
    const int del = task / 28, kg = task - del * 28;
    const int ks = kg * 4 + ((lane >> 3) & 3), h = lane >> 5;
    transpose_store(in, rs, WS_W2T + (del * 2 * 112 + ks) * 1024 + (h * 32 + (lane & 7)) * 16, 128, 112 * 1024);
}

__global__ __launch_bounds__(512, 2) void fused_block(Params p) {
    extern __shared__ __attribute__((aligned(16))) char smem_[];
    LAS char* sm = (LAS char*)smem_;
    const int tid = threadIdx.x, lane = tid & 63;
    const int wave = __builtin_amdgcn_readfirstlane(tid >> 6);
    const int b = blockIdx.x;
    const rsrc_t rs = __builtin_amdgcn_make_buffer_rsrc(p.ws, 0, WS_END, 0x00020000);
    STAMP(0);
    const u32 tagv = __hip_atomic_load((GAS u32*)(p.ws + WS_EPOCH), RLX_AGENT) + 1u;

    const bool main_blk = b < 224;
    const int l56 = lane < 56 ? lane : 55;
    const int rbase = 1024 * ((b >> 1) & 1) + 16 * (b >> 2) + (b & 1);
    const v4u xr = ldnt(p.x + (size_t)(rbase + 2 * wave) * D + l56 * 8);
    const v4u xs = ldnt(p.x + (size_t)(rbase + 2 * (l56 & 7)) * D + 56 * wave + (l56 >> 3) * 8);

    const int xcd = b & 7, i28 = b >> 3;
    const int pair = main_blk ? xcd * 28 + i28 : 0;
    const int ch = wave >> 2, wq = wave & 3;
    const int d0 = 2 * pair, dch = d0 + ch;
    const int pi = dch * N + (tid & 255);
    const u32 r_are = __builtin_nontemporal_load(p.Are + pi), r_aim = __builtin_nontemporal_load(p.Aim + pi);
    const u32 r_bre = __builtin_nontemporal_load(p.Bre + pi), r_bim = __builtin_nontemporal_load(p.Bim + pi);
    const u32 r_cre = __builtin_nontemporal_load(p.Cre + pi), r_cim = __builtin_nontemporal_load(p.Cim + pi);
    const u32 r_ldt = p.logdt[dch];
    const u32 r_dsk = *(const u32*)(p.Dskip + d0);
    const int w1task = main_blk ? ((b < 136 && wave == 7) ? 256 + b : -1) : (b - 224) * 8 + wave;
    v4u tin[8];
    w1_task_load(p, w1task, lane, tin);
    SB();
    const u32 tag = __builtin_amdgcn_readfirstlane(tagv);
    {
        float v[8];
#pragma unroll
        for (int i = 0; i < 4; i++) { v[2 * i] = bflo(xr[i]); v[2 * i + 1] = bfhi(xr[i]); }
        float s = ((v[0] + v[1]) + (v[2] + v[3])) + ((v[4] + v[5]) + (v[6] + v[7]));
        if (lane >= 56) s = 0.f;
        const float mean = wave_sum(s) * (1.f / D);
        float q = 0.f;
#pragma unroll
        for (int i = 0; i < 8; i++) { float dd = v[i] - mean; q += dd * dd; }
        if (lane >= 56) q = 0.f;
        const float var = rbf(wave_sum(q) * (1.f / D));
        const float sd = rbf(__builtin_amdgcn_sqrtf(rbf(var + 1e-5f)));
        if (lane == 0) *(LAS f32x2*)(sm + LD_LN + wave * 8) = f32x2{rbf(mean), 1.f / sd};
    }
    *(LAS v2u*)(sm + LD_KP + wave * 1536 + lane * 8) = v2u{0, 0};
    if (tid < 32) *(LAS u32*)(sm + LD_U + (tid >> 4) * 4160 + (tid & 15) * 4) = 0;
    BARRIER();
    asm volatile("" :: "v"(r_are), "v"(r_aim), "v"(r_bre), "v"(r_bim), "v"(r_cre), "v"(r_cim), "v"(r_ldt));
    {
        const f32x2 st = *(LAS f32x2*)(sm + LD_LN + (lane & 7) * 8);
        int voff = WS_XU + ((28 * wave + 4 * (lane >> 3)) * 2048 + (b & 3) * 512 + 8 * (b >> 2) + (lane & 7)) * 16;
        if (lane >= 56) voff = 0x7f000000;
#pragma unroll
        for (int i = 0; i < 4; i++) {
            const u32 dd = pk_bf16(bflo(xs[i]) - st[0], bfhi(xs[i]) - st[0]);
            const u32 uu = pk_bf16(bflo(dd) * st[1], bfhi(dd) * st[1]);
            st16(rs, voff, i * 32768, v4u{__builtin_amdgcn_perm(uu, xs[i], 0x05040100u), tag, __builtin_amdgcn_perm(uu, xs[i], 0x07060302u), tag});
        }
    }
    STAMP(1);
    if (w1task >= 0) w1_task_store(rs, w1task, lane, tin);
    if (!main_blk) {
        const int w2task = (b - 224) * 8 + wave;
        if (w2task < 196) {
            w2_task_load(p, w2task, lane, tin);
            WAIT_VM(8);
            set_flag(p.ws, WS_WF1 + (w1task / 28) * 128 + (w1task % 28) * 4, lane, tag);
            w2_task_store(rs, w2task, lane, tin);
            WAIT_VM(0);
            set_flag(p.ws, WS_WF2 + (w2task / 28) * 128 + (w2task % 28) * 4, lane, tag);
        } else {
            WAIT_VM(0);
            set_flag(p.ws, WS_WF1 + (w1task / 28) * 128 + (w1task % 28) * 4, lane, tag);
        }
        STAMP(2);
        return;
    }

    float unscale;
    LAS char* xy = sm + wave * XY_WAVE;
    {
        const float dt = rbf(__expf(bflo(r_ldt)));
        const float ar = dt * bflo(r_are), ai = dt * bflo(r_aim);
        const float br = dt * bflo(r_bre), bi = dt * bflo(r_bim);
        const float cr = bflo(r_cre), ci = bflo(r_cim);
        cf cb = {cr * br - ci * bi, cr * bi + ci * br};
        const float a2 = ar * 1.44269504088896340736f;
        const float rh = ai * 0.15915494309189535f;
        const float rl = __builtin_fmaf(ai, 0.15915494309189535f, -rh) + ai * 6.4206382e-9f;
        auto cexp = [&](float s) -> cf {
            float e = __builtin_amdgcn_exp2f(a2 * s);
            float r = __builtin_amdgcn_fractf(rh * s) + rl * s;
            return {e * __builtin_amdgcn_cosf(r), e * __builtin_amdgcn_sinf(r)};
        };
        const float grow = fmaxf(1.f, __builtin_amdgcn_exp2f(a2 * 2048.f));
        const float mag = (fabsf(cb.r) + fabsf(cb.i)) * grow;
        const float mx = wave_max(mag);
        const float sb = wave_sum(mag);
        if (lane == 0) *(LAS float*)(sm + LD_SB + wave * 4) = sb;
        int ex = (int)((__builtin_bit_cast(u32, mx) >> 23) & 255);
        ex = min(max(ex, 30), 230);
        const float sx = __builtin_bit_cast(float, (u32)(127 + 14 + 126 - ex) << 23);
        unscale = __builtin_bit_cast(float, (u32)(127 - 14 - 126 + ex) << 23);
        cb.r *= sx; cb.i *= sx;

        const cf e1 = cexp(1.f), e16 = cexp(16.f), e256 = cexp(256.f);
        const cf e2 = csq(e1), e4 = csq(e2), e8 = csq(e4);
        const cf e32 = csq(e16), e64 = csq(e32), e128 = csq(e64);
        const cf e512 = csq(e256), e1024 = csq(e512);
        LAS char* w = xy + lane * 4;
        auto putY = [&](int q, cf v) { *(LAS u32*)(w + q * XY_STRIDE) = pk_f16(v.r, v.i); };
        auto putX = [&](int q, cf v) { *(LAS u32*)(w + (32 + q) * XY_STRIDE) = pk_f16(v.r, -v.i); };
        cf y[8];
        y[0] = {1.f, 0.f}; y[1] = e8; y[2] = e16; y[3] = cmul(e8, e16); y[4] = e32;
        y[5] = cmul(y[1], e32); y[6] = cmul(y[2], e32); y[7] = cmul(y[3], e32);
        const cf e192 = cmul(e64, e128);
#pragma unroll
        for (int q = 0; q < 8; q++) putY(q, y[q]);
        putY(8, e64);
#pragma unroll
        for (int q = 1; q < 8; q++) putY(8 + q, cmul(y[q], e64));
        putY(16, e128);
#pragma unroll
        for (int q = 1; q < 8; q++) putY(16 + q, cmul(y[q], e128));
        putY(24, e192);
#pragma unroll
        for (int q = 1; q < 8; q++) putY(24 + q, cmul(y[q], e192));
        const cf x0 = cb, x1 = cmul(cb, e1), x2 = cmul(cb, e2), x3 = cmul(x1, e2);
        const cf x4 = cmul(x0, e4), x5 = cmul(x1, e4), x6 = cmul(x2, e4), x7 = cmul(x3, e4), x8 = cmul(x0, e8);
        putX(0, x0); putX(1, x1); putX(2, x2); putX(3, x3); putX(4, x4); putX(5, x5); putX(6, x6); putX(7, x7);
        const cf x256 = cmul(x0, e256), x260 = cmul(x4, e256), x264 = cmul(x8, e256);
        putX(8, x256); putX(9, cmul(x2, e256)); putX(10, x260); putX(11, cmul(x6, e256)); putX(12, x264);
        const cf x512 = cmul(x0, e512), x520 = cmul(x8, e512), x768 = cmul(x256, e512), x776 = cmul(x264, e512);
        putX(13, x512); putX(14, cmul(x4, e512)); putX(15, x520);
        putX(16, x768); putX(17, cmul(x260, e512)); putX(18, x776);
        putX(19, cmul(x0, e1024)); putX(20, cmul(x8, e1024)); putX(21, cmul(x256, e1024)); putX(22, cmul(x264, e1024));
        putX(23, cmul(x512, e1024)); putX(24, cmul(x520, e1024)); putX(25, cmul(x768, e1024)); putX(26, cmul(x776, e1024));
    }
    STAMP(2);
    if (w1task >= 0) {
        WAIT_VM(0);
        set_flag(p.ws, WS_WF1 + (w1task / 28) * 128 + (w1task % 28) * 4, lane, tag);
    }
    const int xuoff = WS_XU + pair * 32768 + tid * 16;
    const int tau = (xcd >> 1) * 4 + (i28 & 3), phi = (xcd & 1) * 7 + (i28 >> 2);
    const int del = i28 >> 2, tau2 = 2 * tau + (xcd & 1);
    const int l28 = (lane & 31) < 28 ? (lane & 31) : 0;
    v4u gr[4];
#pragma unroll
    for (int i = 0; i < 4; i++) gr[i] = ld16(rs, xuoff, i * 8192);
    u32 wfl = __builtin_amdgcn_raw_buffer_load_b32(rs, WS_WF1 + l28 * 4, phi * 128, SC1);
    SB();
    {
        f32x16 kd;
        const int r = lane & 31, h = lane >> 5;
        const int g = r >> 3, hh = (r >> 2) & 1, i = r & 3;
        const int xi = g == 0 ? 4 * hh + i : g == 1 ? 8 + 2 * hh + i : g == 2 ? 13 + 3 * hh + i : 19 + 4 * hh + i;
        LAS char* pa = xy + (32 + xi) * XY_STRIDE + h * 16;
        LAS char* pb = xy + r * XY_STRIDE + h * 16;
#pragma unroll
        for (int i2 = 0; i2 < 16; i2++) kd[i2] = 0.f;
        v4u fa[8], fb[8];
#pragma unroll
        for (int s = 0; s < 8; s++) { fa[s] = *(LAS v4u*)(pa + s * 32); fb[s] = *(LAS v4u*)(pb + s * 32); }
        SB();
#pragma unroll
        for (int s = 0; s < 8; s++) kd = mfma_f16(fa[s], fb[s], kd);
#pragma unroll
        for (int g2 = 0; g2 < 4; g2++)
            *(LAS f32x4*)(xy + g2 * 1024 + lane * 16) = f32x4{kd[4 * g2], kd[4 * g2 + 1], kd[4 * g2 + 2], kd[4 * g2 + 3]} * unscale;
    }
    BARRIER();
    STAMP(3);
    float ik0, ik1;
    {
        const f32x4 s0 = *(LAS f32x4*)(sm + LD_SB), s1 = *(LAS f32x4*)(sm + LD_SB + 16);
        const float b0 = (s0[0] + s0[1]) + (s0[2] + s0[3]), b1 = (s1[0] + s1[1]) + (s1[2] + s1[3]);
        int e0 = (int)((__builtin_bit_cast(u32, b0) >> 23) & 255), e1 = (int)((__builtin_bit_cast(u32, b1) >> 23) & 255);
        e0 = min(max(e0, 30), 230); e1 = min(max(e1, 30), 230);
        ik0 = __builtin_bit_cast(float, (u32)(127 - 15 - 126 + e0) << 23);
        ik1 = __builtin_bit_cast(float, (u32)(127 - 15 - 126 + e1) << 23);
        const float sk = __builtin_bit_cast(float, (u32)(127 + 15 + 126 - (ch ? e1 : e0)) << 23);
        LAS char* ex = sm + (ch * 4) * XY_WAVE + wq * 1024 + lane * 16;
        const f32x4 a0 = *(LAS f32x4*)(ex), a1 = *(LAS f32x4*)(ex + XY_WAVE), a2 = *(LAS f32x4*)(ex + 2 * XY_WAVE), a3 = *(LAS f32x4*)(ex + 3 * XY_WAVE);
        const f32x4 v = ((a0 + a1) + (a2 + a3)) * sk;
        const int q = lane & 31, h = lane >> 5;
        LAS char* kp = sm + LD_KP + ch * 4 * 1536 + 512;
        const u32 p01 = pk_f16(v[0], v[1]), p23 = pk_f16(v[2], v[3]);
        if (wq == 0) {
            LAS char* o = kp + (2 * q + h) * 2;
            *(LAS u16*)(o) = (u16)p01; *(LAS u16*)(o + 1536) = (u16)(p01 >> 16);
            *(LAS u16*)(o + 2 * 1536) = (u16)p23; *(LAS u16*)(o + 3 * 1536) = (u16)(p23 >> 16);
        } else if (wq == 1) {
            LAS char* o = kp + (64 + 2 * q + h) * 2;
            *(LAS u16*)(o) = (u16)p01; *(LAS u16*)(o + 1536) = (u16)p01;
            *(LAS u16*)(o + 2 * 1536) = (u16)(p01 >> 16); *(LAS u16*)(o + 3 * 1536) = (u16)p23;
        } else if (wq == 2) {
            LAS char* o = kp + (64 * (2 + h) + 2 * q) * 2;
            *(LAS u32*)(o) = p01; *(LAS u32*)(o + 1536) = p01;
            *(LAS u32*)(o + 2 * 1536) = (p01 & 0xffffu) | (p23 << 16);
            *(LAS u32*)(o + 3 * 1536) = (p01 >> 16) | (p23 << 16);
        } else {
            LAS char* o = kp + (64 * (4 + 2 * h) + 2 * q) * 2;
            const bool ev = (q & 1) == 0;
            *(LAS u32*)(o) = ev ? ((p01 & 0xffffu) | (p01 << 16)) : p01;
            *(LAS u32*)(o + 1536) = p01; *(LAS u32*)(o + 2 * 1536) = p01; *(LAS u32*)(o + 3 * 1536) = p01;
            *(LAS u32*)(o + 128) = ev ? ((p23 & 0xffffu) | (p23 << 16)) : p23;
            *(LAS u32*)(o + 128 + 1536) = p23; *(LAS u32*)(o + 128 + 2 * 1536) = p23; *(LAS u32*)(o + 128 + 3 * 1536) = p23;
        }
    }
#pragma unroll 1
    for (int spins = 0; spins < SPIN_CAP; spins++) {
        bool ok = wfl == tag;
#pragma unroll
        for (int i = 0; i < 4; i++) ok &= (gr[i][1] == tag) & (gr[i][3] == tag);
        if (__all(ok)) break;
        __builtin_amdgcn_s_sleep(2);
#pragma unroll
        for (int i = 0; i < 4; i++) gr[i] = ld16(rs, xuoff, i * 8192);
        wfl = __builtin_amdgcn_raw_buffer_load_b32(rs, WS_WF1 + l28 * 4, phi * 128, SC1);
    }
    __builtin_amdgcn_fence(__ATOMIC_ACQUIRE, "wavefront");
    STAMP(4);
    u32 xud[2][4];
    {
#pragma unroll
        for (int c = 0; c < 2; c++) {
            const float dk = c ? bfhi(r_dsk) : bflo(r_dsk);
#pragma unroll
            for (int hf = 0; hf < 2; hf++) {
                const u32 g0 = gr[2 * hf][2 * c], g1 = gr[2 * hf + 1][2 * c];
                *(LAS u32*)(sm + LD_U + c * 4160 + 64 + hf * 2048 + tid * 4) = pk_f16(bfhi(g0), bfhi(g1));
                const u32 ud = pk_bf16(bfhi(g0) * dk, bfhi(g1) * dk);
                xud[c][2 * hf] = __builtin_amdgcn_perm(ud, g0, 0x05040100u);
                xud[c][2 * hf + 1] = __builtin_amdgcn_perm(ud, g1, 0x07060100u);
            }
        }
    }
    v4u wf[28];
    const int w1off = WS_W1T + (phi * 8 + wave) * 28 * 1024;
#pragma unroll
    for (int ks = 0; ks < 14; ks++) wf[ks] = ld16(rs, lane * 16 + (ks & 3) * 1024, w1off + (ks >> 2) * 4096);
    SB();
    BARRIER();
    if (b == 0 && tid == 0) __hip_atomic_store((GAS u32*)(p.ws + WS_EPOCH), tag, RLX_AGENT);
    STAMP(5);
    {
        const int G = lane >> 4, h = lane >> 5, q = (lane & 15) >> 2, pp = lane & 3;
        LAS char* ua = sm + LD_U + ch * 4160 + 2 * (32 + 32 + 16 * (G & 1) - 32 * h - 16 - 4 * q + 4 * pp);
        LAS char* ka = sm + LD_KP + wave * 1536 + 2 * (256 - 248 + 8 * (lane & 31) + 8 * h);
        f32x16 acc0, acc1;
#pragma unroll
        for (int i = 0; i < 16; i++) { acc0[i] = 0.f; acc1[i] = 0.f; }
        constexpr int PD = 3;
        v4u fu[32], fk0[32], fk1[16];
        auto issue = [&](int n) {
            const int B0 = n < 16 ? 992 - 64 * n : 2016 - 64 * (n - 16);
            fu[n] = lds_tr2(ua + 2 * (B0 - 32) + 32, ua + 2 * (B0 - 32));
            fk0[n] = *(LAS v4u*)(ka + 32 * (n & 15));
            if (n < 16) fk1[n] = *(LAS v4u*)(ka + 32 * n + 512);
        };
#pragma unroll
        for (int n = 0; n < PD; n++) issue(n);
        SB();
        LAS char* pw = sm + wave * P_WAVE + (lane & 31) * 144 + h * 16;
#pragma unroll
        for (int n = 0; n < 32; n++) {
            if (n + PD < 32) issue(n + PD);
            if (n < 16) {
                acc0 = mfma_f16(fu[n], fk0[n], acc0);
                acc1 = mfma_f16(fu[n], fk1[n], acc1);
            } else {
                acc1 = mfma_f16(fu[n], fk0[n], acc1);
                if (n >= 18 && n < 22) {
                    const int g = n - 18;
                    *(LAS f32x4*)(pw + g * 32) = f32x4{acc0[4 * g], acc0[4 * g + 1], acc0[4 * g + 2], acc0[4 * g + 3]};
                }
            }
            SB();
        }
#pragma unroll
        for (int g = 0; g < 4; g++) *(LAS f32x4*)(pw + 32 * 144 + g * 32) = f32x4{acc1[4 * g], acc1[4 * g + 1], acc1[4 * g + 2], acc1[4 * g + 3]};
    }
    BARRIER();
    STAMP(6);
    {
#pragma unroll
        for (int hf = 0; hf < 2; hf++) {
            const int c0 = hf * 512 + tid, c1 = max(c0 - 1, 0), c2 = max(c0 - 2, 0);
            const int o0 = (c0 >> 4) * 144 + (c0 & 15) * 8, o1 = (c1 >> 4) * 144 + (c1 & 15) * 8, o2 = (c2 >> 4) * 144 + (c2 & 15) * 8;
            const float z1 = c0 >= 1 ? 1.f : 0.f, z2 = c0 >= 2 ? 1.f : 0.f;
#pragma unroll
            for (int c = 0; c < 2; c++) {
                LAS char* pb = sm + c * 4 * P_WAVE;
                const f32x2 r0 = *(LAS f32x2*)(pb + o0);
                const float r1a = *(LAS float*)(pb + P_WAVE + o1 + 4), r1b = *(LAS float*)(pb + P_WAVE + o0);
                const f32x2 r2 = *(LAS f32x2*)(pb + 2 * P_WAVE + o1);
                const float r3a = *(LAS float*)(pb + 3 * P_WAVE + o2 + 4), r3b = *(LAS float*)(pb + 3 * P_WAVE + o1);
                const float ik = c ? ik1 : ik0;
                const float y0 = (r0[0] + (r1a + r2[0]) * z1 + r3a * z2) * ik;
                const float y1 = ((r0[1] + r1b) + (r2[1] + r3b) * z1) * ik;
                const u32 yy = pk_bf16(y0, y1);
                const u32 xa = xud[c][2 * hf], xb = xud[c][2 * hf + 1];
                const u32 ss = pk_bf16(bflo(yy) + bfhi(xa), bfhi(yy) + bfhi(xb));
                st4(rs, WS_HT + tid * 4, (d0 + c) * (L * 2) + hf * 2048, pk_bf16(bflo(ss) + bflo(xa), bfhi(ss) + bflo(xb)));
            }
        }
    }
    SB();
#pragma unroll
    for (int ks = 14; ks < 28; ks++) wf[ks] = ld16(rs, lane * 16 + (ks & 3) * 1024, w1off + (ks >> 2) * 4096);
    SB();
    WAIT_VM(14);
    set_flag(p.ws, WS_HF + (wave * 8 + ((pair >> 2) & 7)) * 128 + ((pair >> 5) * 4 + (pair & 3)) * 4, lane, tag);
    STAMP(7);
    BARRIER_RAW();

    const int h = lane >> 5, col = lane & 31;
    wait_flags(p.ws, (h ? WS_WF2 + del * 128 : WS_HF + ((tau >> 1) * 8 + wave) * 128) + l28 * 4, tag);
    STAMP(8);
    {
        const int r8 = lane >> 3;
        const char* src = p.ws + WS_HT + ((size_t)(8 * wave + r8) * L + tau * 64) * 2 + (((lane & 7) ^ ((r8 & 2) << 1)) << 4);
        const u32 lbase = (u32)(size_t)sm;
        int i = i28 >> 2;
#pragma unroll
        for (int n = 0; n < 7; n++) {
            glds16(src + (size_t)i * (64 * L * 2), lbase + LD_HA + (wave + 8 * i) * 1024);
            glds16(src + (size_t)i * (64 * L * 2) + 2048, lbase + LD_HB + (wave + 8 * i) * 1024);
            i = i == 6 ? 0 : i + 1;
        }
    }
    {
        float c0 = 0.f, c1 = 0.f;
#pragma unroll
        for (int ks = 0; ks < 28; ks++) {
            c0 = dot2bf(wf[ks][0], 0x3f803f80u, c0); c1 = dot2bf(wf[ks][1], 0x3f803f80u, c1);
            c0 = dot2bf(wf[ks][2], 0x3f803f80u, c0); c1 = dot2bf(wf[ks][3], 0x3f803f80u, c1);
        }
        const float cs = half_sum(c0 + c1);
        if (lane < 32) *(LAS float*)(sm + LD_CS + wave * 128 + lane * 4) = cs;
    }
    WAIT_VM(0);
    BARRIER();
    STAMP(9);
    {
        const int G1 = (lane >> 4) & 1, q = (lane & 15) >> 2, pp = lane & 3;
        const int lo = (8 * h + q) * 128 + 8 * (pp & 1) + 16 * (2 * G1 + (pp >> 1));
        const int sw = (q & 2) << 5;
        LAS char* at0 = sm + lo + sw;
        LAS char* at1 = sm + lo + (64 ^ sw);
        f32x4 cs4[4];
#pragma unroll
        for (int g = 0; g < 4; g++) cs4[g] = *(LAS f32x4*)(sm + LD_CS + wave * 128 + g * 32 + h * 16);
        {
            LAS char* as = ((wave & 1) ? at1 : at0) + (wave >> 1) * 7 * 2048;
#pragma unroll
            for (int hf = 0; hf < 2; hf++) {
                v4u f[7];
#pragma unroll
                for (int k = 0; k < 7; k++) f[k] = lds_tr2(as + (hf ? LD_HB : LD_HA) + k * 2048, as + (hf ? LD_HB : LD_HA) + k * 2048 + 512);
                float s1 = 0.f, s2 = 0.f, s1b = 0.f, s2b = 0.f;
#pragma unroll
                for (int k = 0; k < 7; k++) {
                    s1 = dot2bf(f[k][0], 0x3f803f80u, s1); s2 = dot2bf(f[k][0], f[k][0], s2);
                    s1b = dot2bf(f[k][1], 0x3f803f80u, s1b); s2b = dot2bf(f[k][1], f[k][1], s2b);
                    s1 = dot2bf(f[k][2], 0x3f803f80u, s1); s2 = dot2bf(f[k][2], f[k][2], s2);
                    s1b = dot2bf(f[k][3], 0x3f803f80u, s1b); s2b = dot2bf(f[k][3], f[k][3], s2b);
                }
                s1 = half_sum(s1 + s1b); s2 = half_sum(s2 + s2b);
                if (lane < 32) *(LAS f32x2*)(sm + LD_ST + (((hf * 2 + (wave & 1)) * 32 + col) * 4 + (wave >> 1)) * 8) = f32x2{s1, s2};
            }
        }
        SB();
        constexpr int PD = 4;
        v4u fr[PD + 1];
        auto issue = [&](int n) __attribute__((always_inline)) {
            const int tile = n / 28, ks = n % 28;
            LAS char* a = ((tile & 1) ? at1 : at0) + ((tile >> 1) ? LD_HB : LD_HA) + ks * 2048;
            fr[n % (PD + 1)] = lds_tr2(a, a + 512);
        };
        f32x16 acc[2];
        float nm = 0.f, rstd = 0.f;
        u32 opk[4];
        float o0 = 0.f;
        auto epi_stats = [&](int tile) __attribute__((always_inline)) {
            LAS char* st = sm + LD_ST + (tile * 32 + col) * 32;
            const f32x4 a = *(LAS f32x4*)(st), c = *(LAS f32x4*)(st + 16);
            const float mean = ((a[0] + a[2]) + (c[0] + c[2])) * (1.f / D);
            const float ex2 = ((a[1] + a[3]) + (c[1] + c[3])) * (1.f / D);
            const float var = rbf(fmaxf(ex2 - mean * mean, 0.f));
            rstd = __builtin_amdgcn_rcpf(rbf(__builtin_amdgcn_sqrtf(rbf(var + 1e-5f))));
            nm = -rbf(mean);
        };
        auto epi_pair = [&](const f32x16& a, int j) __attribute__((always_inline)) {
            const float g = __builtin_fmaf(nm, cs4[j >> 2][j & 3], a[j]) * rstd;
            const float u = __builtin_fmaf(nm, cs4[2 + (j >> 2)][j & 3], a[8 + j]) * rstd;
            const float e = __builtin_amdgcn_exp2f(g * __builtin_fmaf(g * g, -0.10294324f, -2.3022082f));
            const float o = (g * u) * __builtin_amdgcn_rcpf(1.f + e);
            if (j & 1) opk[j >> 1] = pk_bf16(o0, o); else o0 = o;
        };
        auto epi_store = [&](int tile) __attribute__((always_inline)) {
            const int tok32 = (tile >> 1) * 32 + tau * 2 + (tile & 1);
            st16(rs, WS_ACT + lane * 16, (tok32 * 112 + phi * 8 + wave) * 1024, v4u{opk[0], opk[1], opk[2], opk[3]});
        };
#pragma unroll
        for (int n = 0; n < PD; n++) issue(n);
        SB();
        static_for<0, 112>([&](auto N_) __attribute__((always_inline)) {
            constexpr int n = decltype(N_)::v;
            constexpr int tile = n / 28, ks = n % 28;
            if (n + PD < 112) issue(n + PD);
            if (ks == 0) {
                f32x16 z;
#pragma unroll
                for (int i = 0; i < 16; i++) z[i] = 0.f;
                acc[tile & 1] = mfma_bf16(wf[0], fr[n % (PD + 1)], z);
            } else {
                acc[tile & 1] = mfma_bf16(wf[ks], fr[n % (PD + 1)], acc[tile & 1]);
            }
            if (n == 20) BARRIER_RAW();
            if (tile > 0) {
                if (ks == 1) epi_stats(tile - 1);
                if (ks >= 3 && ks < 27 && (ks % 3) == 0) epi_pair(acc[(tile - 1) & 1], ks / 3 - 1);
                if (ks == 25) epi_store(tile - 1);
            }
            SB();
        });
        epi_stats(3);
#pragma unroll
        for (int j = 0; j < 8; j++) epi_pair(acc[1], j);
        epi_store(3);
    }
    SB();
    STAMP(10);
    WAIT_VM(0);
    {
        const int kap = phi * 8 + wave;
        set_flag(p.ws, WS_AF + (tau * 8 + kap / 14) * 128 + (kap % 14) * 4, lane, tag);
    }
    v4u w0[14], w1[14], a0[14], a1[14];
    int kof[14];
    {
        int i = 2 * del;
#pragma unroll
        for (int n = 0; n < 14; n++) { kof[n] = (wave * 14 + i) * 1024; i = i == 13 ? 0 : i + 1; }
    }
    const int w2v = WS_W2T + del * 2 * 112 * 1024 + lane * 16;
#pragma unroll
    for (int n = 0; n < 14; n++) { w0[n] = ld16(rs, w2v, kof[n]); w1[n] = ld16(rs, w2v + 112 * 1024, kof[n]); }
    const int tc = wave >> 2, r3 = wave & 3;
    const int dd = 64 * del + 8 * (r3 + 4 * h), tok = 1024 * tc + 32 * tau2 + col;
    v4u hr;
    {
        const int G1 = (lane >> 4) & 1, q = (lane & 15) >> 2, pp = lane & 3;
        LAS char* a = sm + (tc ? LD_HB : LD_HA) + (dd + q) * 128 + 8 * (pp & 1) + 16 * (2 * G1 + (pp >> 1)) + (((xcd & 1) << 6) ^ ((q & 2) << 5));
        hr = lds_tr2(a, a + 512);
    }
    SB();
    STAMP(11);
    wait_flags(p.ws, WS_AF + ((tau2 >> 1) * 8 + wave) * 128 + (lane < 14 ? lane : 0) * 4, tag);
    STAMP(12);
    const int av = WS_ACT + tau2 * 112 * 1024 + lane * 16;
    constexpr int WIN = 8;
#pragma unroll
    for (int n = 0; n < WIN; n++) { a0[n] = ld16(rs, av, kof[n]); a1[n] = ld16(rs, av + 32 * 112 * 1024, kof[n]); }
    SB();
    f32x16 acc[2][2];
#pragma unroll
    for (int i = 0; i < 16; i++) { acc[0][0][i] = 0.f; acc[0][1][i] = 0.f; acc[1][0][i] = 0.f; acc[1][1][i] = 0.f; }
#pragma unroll
    for (int n = 0; n < 14; n++) {
        acc[0][0] = mfma_bf16(w0[n], a0[n], acc[0][0]);
        acc[1][0] = mfma_bf16(w1[n], a0[n], acc[1][0]);
        acc[0][1] = mfma_bf16(w0[n], a1[n], acc[0][1]);
        acc[1][1] = mfma_bf16(w1[n], a1[n], acc[1][1]);
        SB();
        if (n + WIN < 14) { a0[n + WIN] = ld16(rs, av, kof[n + WIN]); a1[n + WIN] = ld16(rs, av + 32 * 112 * 1024, kof[n + WIN]); }
        SB();
    }
    STAMP(13);
    BARRIER();
#pragma unroll
    for (int o = 0; o < 8; o++) {
        const f32x16 &t0 = acc[0][o >> 2], &t1 = acc[1][o >> 2];
        const int r = o & 3;
        LAS char* dst = sm + (o * 8 + wave) * 2048 + lane * 16;
        *(LAS f32x4*)(dst) = f32x4{t0[r], t0[r + 4], t0[r + 8], t0[r + 12]};
        *(LAS f32x4*)(dst + 1024) = f32x4{t1[r], t1[r + 4], t1[r + 8], t1[r + 12]};
    }
    BARRIER();
    f32x4 s0 = {0, 0, 0, 0}, s1 = {0, 0, 0, 0};
#pragma unroll
    for (int s = 0; s < 8; s++) {
        LAS char* src = sm + (wave * 8 + s) * 2048 + lane * 16;
        s0 += *(LAS f32x4*)(src); s1 += *(LAS f32x4*)(src + 1024);
    }
    float o[8];
#pragma unroll
    for (int j = 0; j < 8; j++) o[j] = ((j & 1) ? bfhi(hr[j >> 1]) : bflo(hr[j >> 1])) + rbf(j < 4 ? s0[j & 3] : s1[j & 3]);
    *(v4u*)(p.out + (size_t)tok * D + dd) = v4u{pk_bf16(o[0], o[1]), pk_bf16(o[2], o[3]), pk_bf16(o[4], o[5]), pk_bf16(o[6], o[7])};
    STAMP(14);
}

}

extern "C" void kernel_launch(void* const* d_in, const int* in_sizes, int n_in,
                              void* d_out, int out_size, void* d_ws, size_t ws_size,
                              hipStream_t stream) {
    Params p;
    p.x = (const u16*)d_in[0]; p.Are = (const u16*)d_in[1]; p.Aim = (const u16*)d_in[2];
    p.Bre = (const u16*)d_in[3]; p.Bim = (const u16*)d_in[4]; p.Cre = (const u16*)d_in[5]; p.Cim = (const u16*)d_in[6];
    p.logdt = (const u16*)d_in[7]; p.Dskip = (const u16*)d_in[8];
    p.wg = (const u16*)d_in[10]; p.wu = (const u16*)d_in[11]; p.wd = (const u16*)d_in[12];
    p.ws = (char*)d_ws; p.out = (u16*)d_out;
    (void)hipFuncSetAttribute((const void*)fused_block, hipFuncAttributeMaxDynamicSharedMemorySize, LDS_BYTES);
    fused_block<<<256, 512, LDS_BYTES, stream>>>(p);
}
```

```cpp
#include <hip/hip_runtime.h>
#include <stdint.h>

namespace {

constexpr int L = 2048, D = 448, N = 256, F = 1792;

typedef unsigned short u16;
typedef unsigned int u32;
typedef unsigned long long u64;
typedef u32 v4u __attribute__((ext_vector_type(4)));
typedef u32 v2u __attribute__((ext_vector_type(2)));
typedef float f32x2 __attribute__((ext_vector_type(2)));
typedef float f32x4 __attribute__((ext_vector_type(4)));
typedef float f32x16 __attribute__((ext_vector_type(16)));
typedef _Float16 f16x2 __attribute__((ext_vector_type(2)));
typedef _Float16 f16x8 __attribute__((ext_vector_type(8)));
typedef __bf16 bf16x2 __attribute__((ext_vector_type(2)));
typedef __bf16 bf16x8 __attribute__((ext_vector_type(8)));
typedef short s16x4 __attribute__((ext_vector_type(4)));
#define GAS __attribute__((address_space(1)))
#define LAS __attribute__((address_space(3)))
#define RLX_AGENT __ATOMIC_RELAXED, __HIP_MEMORY_SCOPE_AGENT
#define SB() __builtin_amdgcn_sched_barrier(0)
#define PIN(x) asm volatile("" : "+v"(x))
#define BARRIER() asm volatile("s_waitcnt lgkmcnt(0)\n\ts_barrier" ::: "memory")
#define BARRIER_RAW() asm volatile("s_barrier" ::: "memory")
constexpr int SC1 = 16;

constexpr int WS_EPOCH = 0;
constexpr int WS_WF2 = 1024;
constexpr int WS_HF = 8192;
constexpr int WS_AF = WS_HF + 32 * 256;
constexpr int WS_SG = 49152;
constexpr int WS_WF1 = 65536;
constexpr int WS_ST1 = 65536 + 8192;
constexpr int WS_XX = WS_ST1 + 16384;
constexpr int WS_W1T = WS_XX + 224 * 16384;
constexpr int WS_W2T = WS_W1T + 112 * 28 * 1024;
constexpr int WS_HT = WS_W2T + 14 * 112 * 1024;
constexpr int WS_ACT = WS_HT + D * L * 2;
constexpr int WS_DBG = WS_ACT + 64 * 112 * 1024;
constexpr int NSTAMP = 32;
constexpr int WS_END = WS_DBG + 256 * NSTAMP * 8;
constexpr int SPIN_CAP = 1 << 16;
static_assert(WS_AF + 16 * 2 * 8 * 128 <= WS_SG, "flags overlap");

#define STAMP(k) do {} while (0)
#define NST 0

__device__ __forceinline__ float bflo(u32 v) { return __builtin_bit_cast(float, v << 16); }
__device__ __forceinline__ float bfhi(u32 v) { return __builtin_bit_cast(float, v & 0xffff0000u); }
__device__ __forceinline__ u32 pk_bf16(float a, float b) { f32x2 f = {a, b}; return __builtin_bit_cast(u32, __builtin_convertvector(f, bf16x2)); }
__device__ __forceinline__ u32 pk_bf16s(float a, float b) { asm("" : "+v"(a)); asm("" : "+v"(b)); return pk_bf16(a, b); }
__device__ __forceinline__ u32 pk_f16(float a, float b) { f32x2 f = {a, b}; return __builtin_bit_cast(u32, __builtin_convertvector(f, f16x2)); }
__device__ __forceinline__ float rbf(float a) { return bflo(pk_bf16(a, 0.f)); }
__device__ __forceinline__ float dot2bf(u32 a, u32 b, float c) {
    return __builtin_amdgcn_fdot2_f32_bf16(__builtin_bit_cast(bf16x2, a), __builtin_bit_cast(bf16x2, b), c, false);
}
__device__ __forceinline__ f32x16 mfma_bf16(v4u a, v4u b, f32x16 c) {
    return __builtin_amdgcn_mfma_f32_32x32x16_bf16(__builtin_bit_cast(bf16x8, a), __builtin_bit_cast(bf16x8, b), c, 0, 0, 0);
}
__device__ __forceinline__ f32x16 mfma_f16(v4u a, v4u b, f32x16 c) {
    return __builtin_amdgcn_mfma_f32_32x32x16_f16(__builtin_bit_cast(f16x8, a), __builtin_bit_cast(f16x8, b), c, 0, 0, 0);
}
__device__ __forceinline__ v2u lds_tr(LAS char* p) {
    return __builtin_bit_cast(v2u, __builtin_amdgcn_ds_read_tr16_b64_v4i16((LAS s16x4*)p));
}
__device__ __forceinline__ v4u lds_tr2(LAS char* p0, LAS char* p1) {
    const v2u lo = lds_tr(p0), hi = lds_tr(p1);
    return v4u{lo[0], lo[1], hi[0], hi[1]};
}
template <int CTRL> __device__ __forceinline__ float dpp(float v) {
    return __builtin_bit_cast(float, __builtin_amdgcn_update_dpp(0, __builtin_bit_cast(int, v), CTRL, 0xf, 0xf, true));
}
__device__ __forceinline__ float rdl(float v, int l) { return __builtin_bit_cast(float, __builtin_amdgcn_readlane(__builtin_bit_cast(int, v), l)); }
__device__ __forceinline__ float wave_sum(float v) {
    v += dpp<0xB1>(v); v += dpp<0x4E>(v); v += dpp<0x141>(v); v += dpp<0x140>(v);
    return (rdl(v, 0) + rdl(v, 16)) + (rdl(v, 32) + rdl(v, 48));
}
__device__ __forceinline__ float wave_max(float v) {
    v = fmaxf(v, dpp<0xB1>(v)); v = fmaxf(v, dpp<0x4E>(v)); v = fmaxf(v, dpp<0x141>(v)); v = fmaxf(v, dpp<0x140>(v));
    return fmaxf(fmaxf(rdl(v, 0), rdl(v, 16)), fmaxf(rdl(v, 32), rdl(v, 48)));
}
__device__ __forceinline__ float half_sum(float v) {
    const auto r = __builtin_amdgcn_permlane32_swap(__builtin_bit_cast(u32, v), __builtin_bit_cast(u32, v), false, false);
    return __builtin_bit_cast(float, (u32)r[0]) + __builtin_bit_cast(float, (u32)r[1]);
}
template <int I> struct IC { static constexpr int v = I; };
template <int B, int E, class Fn> __device__ __forceinline__ void static_for(Fn&& f) {
    if constexpr (B < E) { f(IC<B>{}); static_for<B + 1, E>(f); }
}

typedef f32x2 cf;
__device__ __forceinline__ cf rot(cf b) { return cf{-b.y, b.x}; }
__device__ __forceinline__ cf cmul(cf a, cf b) { return __builtin_elementwise_fma(a.yy, rot(b), a.xx * b); }
__device__ __forceinline__ cf cmulc(cf a, cf b) { return __builtin_elementwise_fma(a.yy, b.yx, a.xx * rot(b).yx); }
__device__ __forceinline__ u32 pkh(cf a) { return __builtin_bit_cast(u32, __builtin_convertvector(a, f16x2)); }

typedef __amdgpu_buffer_rsrc_t rsrc_t;
__device__ __forceinline__ v4u ld16(rsrc_t r, int voff, int soff) { return __builtin_amdgcn_raw_buffer_load_b128(r, voff, soff, SC1); }
__device__ __forceinline__ u32 ld4(rsrc_t r, int voff, int soff) { return __builtin_amdgcn_raw_buffer_load_b32(r, voff, soff, SC1); }
__device__ __forceinline__ void st16(rsrc_t r, int voff, v4u v) { __builtin_amdgcn_raw_buffer_store_b128(v, r, voff, 0, SC1); }
__device__ __forceinline__ void st8(rsrc_t r, int voff, int soff, v2u v) { __builtin_amdgcn_raw_buffer_store_b64(v, r, voff, soff, SC1); }
__device__ __forceinline__ void st4(rsrc_t r, int voff, int soff, u32 v) { __builtin_amdgcn_raw_buffer_store_b32(v, r, voff, soff, SC1); }
template <int N> __device__ __forceinline__ void wait_vm() { asm volatile("s_waitcnt vmcnt(%0)" :: "i"(N) : "memory"); }
constexpr int OOB = 0x7f000000;

__device__ __forceinline__ void wait_flags(rsrc_t rs, int voff, int soff, u32 v, u32 tag) {
    if (!__all(v == tag)) {
        int spins = SPIN_CAP;
#pragma unroll 1
        do {
            __builtin_amdgcn_s_sleep(1);
            v = ld4(rs, voff, soff);
        } while (!__all(v == tag) && --spins > 0);
    }
    __builtin_amdgcn_fence(__ATOMIC_ACQUIRE, "wavefront");
}
__device__ __forceinline__ void glds16(v4u srd, int voff, int soff, u32 lds_dst) {
    asm volatile("s_mov_b32 m0, %3\n\ts_nop 0\n\tbuffer_load_dwordx4 %0, %1, %2 offen sc1 lds"
                 :: "v"(voff), "s"(srd), "s"(soff), "s"(lds_dst) : "memory");
}
__device__ __forceinline__ LAS char* opaque(LAS char* q) { u32 a = (u32)(size_t)q; asm volatile("" : "+v"(a)); return (LAS char*)(size_t)a; }

struct Params {
    const u16 *x, *Are, *Aim, *Bre, *Bim, *Cre, *Cim, *logdt, *Dskip, *wg, *wu, *wd;
    char* ws;
    u16* out;
};

constexpr int XY_STRIDE = 272, XY_ROWS = 59, XY_WAVE = XY_STRIDE * XY_ROWS;
constexpr int P_WAVE = 33 * 144;
constexpr int LD_PB = 0, LD_PA = 38144;
constexpr int LD_HA = 38144, LD_HB = LD_HA + 57344;
constexpr int LD_KP = 128512;
constexpr int LD_U = LD_KP + 8 * 1536;
constexpr int LD_SB = LD_HB + 57344;
constexpr int LD_ST = LD_SB + 64;
constexpr int LD_CS = LD_ST + 4096;
constexpr int LDS_BYTES = LD_CS + 1024;
static_assert(LD_U + 2 * 4160 <= LD_SB && LDS_BYTES <= 163840, "lds map");

__device__ __forceinline__ v4u tr_piece(const v4u (&in)[8], int c) {
    v4u o;
#pragma unroll
    for (int kp = 0; kp < 4; kp++)
        o[kp] = __builtin_amdgcn_perm(in[2 * kp + 1][c >> 1], in[2 * kp][c >> 1], (c & 1) ? 0x07060302u : 0x05040100u);
    return o;
}
__device__ __forceinline__ void w1_task_load(const Params& p, int task, int lane, v2u (&in)[8]) {
    const int t = (task < 0 ? 0 : task) >> 1, c4 = task & 1;
    const int phi = t / 28, r = t - phi * 28, mat = r >= 14, kp = r - 14 * mat;
    const rsrc_t rw = __builtin_amdgcn_make_buffer_rsrc((void*)(mat ? p.wu : p.wg), 0, task < 0 ? 0 : D * F * 2, 0x00020000);
    const int voff = (8 * (lane >> 4) * F + (lane & 15) * 8) * 2;
#pragma unroll
    for (int k = 0; k < 8; k++) in[k] = __builtin_amdgcn_raw_buffer_load_b64(rw, voff + (k & 1) * F * 2, ((32 * kp + (k & 6)) * F + phi * 128 + 4 * c4) * 2, 0);
}
__device__ __forceinline__ void w1_task_store(rsrc_t rs, int task, int lane, const v2u (&in)[8]) {
    const int t = task >> 1, c4 = task & 1;
    const int phi = t / 28, r = t - phi * 28, mat = r >= 14, kp = r - 14 * mat;
    const int voff = WS_W1T + ((phi * 8 + 4 * c4) * 28 + 2 * kp) * 1024 + mat * 256 + (lane >> 4) * 512 + (lane & 15) * 16;
#pragma unroll
    for (int c = 0; c < 4; c++) {
        v4u o;
#pragma unroll
        for (int kp2 = 0; kp2 < 4; kp2++) o[kp2] = __builtin_amdgcn_perm(in[2 * kp2 + 1][c >> 1], in[2 * kp2][c >> 1], (c & 1) ? 0x07060302u : 0x05040100u);
        st16(rs, voff + c * 28 * 1024, o);
    }
}
__device__ __forceinline__ int w1_flag(int task) { const int t = task >> 1, phi = t / 28; return WS_WF1 + (phi * 2 + (task & 1)) * 128 + (t - phi * 28) * 4; }
__device__ __forceinline__ void w2_task(const Params& p, rsrc_t rs, int task, int lane, LAS char* scr) {
    const int del = task / 28, kg = task - del * 28;
    const int k4 = (lane >> 3) & 3, ks = kg * 4 + k4, h = lane >> 5, dr = lane & 7;
    const u16* src = p.wd + (size_t)((ks >> 3) * 128 + 32 * h + (ks & 7)) * D + del * 64 + dr * 8;
    v4u in[8];
#pragma unroll
    for (int j = 0; j < 8; j++) in[j] = __builtin_nontemporal_load((const v4u*)(src + (size_t)(8 * ((j & 3) + 8 * (j >> 2))) * D));
    LAS char* w = scr + ((dr >> 2) * 4 + k4) * 1024 + (32 * h + 8 * (dr & 3)) * 16;
#pragma unroll
    for (int c = 0; c < 8; c++) *(LAS v4u*)(w + c * 16) = tr_piece(in, c);
    const int voff = WS_W2T + (del * 2 * 112 + kg * 4) * 1024 + lane * 16;
#pragma unroll
    for (int f = 0; f < 8; f++) st16(rs, voff + ((f >> 2) * 112 + (f & 3)) * 1024, *(LAS v4u*)(scr + f * 1024 + lane * 16));
}

#define W1_N0 3
#define W1_DIV 2
constexpr int w1_cnt(int n) { return n <= W1_N0 ? 0 : (n - W1_N0 + W1_DIV - 1) / W1_DIV > 28 ? 28 : (n - W1_N0 + W1_DIV - 1) / W1_DIV; }
#define PRIO_A 1
#define CS_FIRST 1
#define HB_N 18
#define DELAY_B 0
#define W2_EARLY 8
#define ROT_W1 0
#define ROT_W2 1
#define AF_N 104
#define HF_N 44
constexpr int w2_cnt(int n) {
    return n <= 24 ? 0 : n <= 85 ? ((n - 24 + 3) / 4 < W2_EARLY ? (n - 24 + 3) / 4 : W2_EARLY)
         : (W2_EARLY + ((n - 85) * 2 + 2) / 3 < 28 ? W2_EARLY + ((n - 85) * 2 + 2) / 3 : 28);
}
#define W2_IN (w2_cnt(112))
#define CODE_PF 24576

__global__ __launch_bounds__(512, 2) void fused_block(Params p) {
    extern __shared__ __attribute__((aligned(16))) char smem_[];
    LAS char* sm = (LAS char*)smem_;
    const int tid = threadIdx.x, lane = tid & 63;
    const int wave = __builtin_amdgcn_readfirstlane(tid >> 6);
    const int b = blockIdx.x;
    const u64 pc0 = __builtin_amdgcn_s_getpc();
    const rsrc_t rs = __builtin_amdgcn_make_buffer_rsrc(p.ws, 0, WS_END, 0x00020000);
    asm volatile("" :: "s"(p.x), "s"(p.wd), "s"(p.ws));
    if (PRIO_A && wave >= 4) __builtin_amdgcn_s_setprio(1);
    STAMP(0);
    const u32 tagv = ld4(rs, WS_EPOCH, 0);

    const bool main_blk = b < 224;
    const int l16 = lane * 16;
    const int rbase = 16 * (b >> 1) + 2 * (b & 1);
    const rsrc_t rx = __builtin_amdgcn_make_buffer_rsrc((void*)p.x, 0, L * D * 2, 0x00020000);
    const int x2v = lane < 56 ? (lane & 3) * (4 * D * 2) + (lane >> 2) * 8 : OOB;
    const v2u xa = __builtin_amdgcn_raw_buffer_load_b64(rx, x2v, (rbase * D + 56 * wave) * 2, 2);
    const v2u xb = __builtin_amdgcn_raw_buffer_load_b64(rx, x2v, ((rbase + 1) * D + 56 * wave) * 2, 2);
    const v4u xr = __builtin_amdgcn_raw_buffer_load_b128(rx, lane < 56 ? l16 : OOB, (rbase + 4 * (wave >> 1) + (wave & 1)) * D * 2, 2);

    const int xcd = b & 7, i28 = b >> 3;
    const int pair = xcd * 28 + i28;
    const int ch = wave >> 2, wq = wave & 3;
    const int d0 = 2 * pair;
    const int psz = main_blk ? D * N * 2 : 0;
    auto ldp = [&](const u16* q) __attribute__((always_inline)) -> u32 {
        return __builtin_amdgcn_raw_buffer_load_b16(__builtin_amdgcn_make_buffer_rsrc((void*)q, 0, psz, 0x00020000), tid * 2, d0 * N * 2, 2);
    };
    const u32 r_ldt = __builtin_amdgcn_raw_buffer_load_b32(__builtin_amdgcn_make_buffer_rsrc((void*)p.logdt, 0, main_blk ? D * 2 : 0, 0x00020000), 0, d0 * 2, 0);
    const u32 r_are = ldp(p.Are), r_aim = ldp(p.Aim), r_bre = ldp(p.Bre), r_bim = ldp(p.Bim), r_cre = ldp(p.Cre), r_cim = ldp(p.Cim);
    const u32 r_dsk = __builtin_amdgcn_raw_buffer_load_b32(__builtin_amdgcn_make_buffer_rsrc((void*)p.Dskip, 0, main_blk ? D * 2 : 0, 0x00020000), 0, d0 * 2, 0);
    const u32 cpf = __builtin_amdgcn_raw_buffer_load_b32(__builtin_amdgcn_make_buffer_rsrc((void*)pc0, 0, CODE_PF, 0x00020000), lane * 64, (i28 * 8 + wave) * 4096, 0);
    const int hw = (b - 224) + 32 * wave;
    const int w1task = !main_blk ? -1 : wave < 2 ? pair * 2 + wave : (wave < 4 && pair < 168) ? 448 + pair * 2 + wave - 2 : -1;
    SB();
    const int h = lane >> 5, col = lane & 31;
    const int G1 = (lane >> 4) & 1, q4 = (lane & 15) >> 2, pp = lane & 3;
    int xxv = lane < 56 ? WS_XX + ((28 * wave + 2 * (lane >> 2)) * 2 + (b & 1)) * 8192 + (4 * (b >> 1) + (lane & 3)) * 16 : OOB;
    int flv = lane == 0 ? 0 : OOB;
    asm volatile("" : "+v"(xxv), "+v"(flv));
    LAS char* xy = sm + wave * XY_WAVE;
    LAS char* xyw = opaque(xy + lane * 4);
    LAS char* pa = opaque(xy + (32 + (int)((0x1713100d0a080400ull >> (8 * (col >> 2))) & 255) + (col & 3)) * XY_STRIDE + h * 16);
    LAS char* pb = opaque(xy + col * XY_STRIDE + h * 16);
    LAS char* exw = opaque(xy + l16);
    LAS char* exr = opaque(sm + (ch * 4) * XY_WAVE + wq * 1024 + l16);
    LAS char* kpw = opaque(sm + LD_KP + ch * 4 * 1536 + 512 + (wq < 2 ? 64 * wq + 2 * col + h : wq == 2 ? 64 * (2 + h) + 2 * col : 64 * (4 + 2 * h) + 2 * col) * 2);
    LAS char* uw = opaque(sm + LD_U + 64 + tid * 8);
    LAS char* ua = opaque(sm + LD_U + ch * 4160 + 2 * (32 + 32 + 16 * G1 - 32 * h - 16 - 4 * q4 + 4 * pp));
    LAS char* ka = opaque(sm + LD_KP + wave * 1536 + 2 * (256 - 248 + 8 * col + 8 * h));
    LAS char* pw = opaque(sm + wave * P_WAVE + (col + 1) * 144 + h * 16);
    *(LAS v2u*)(sm + LD_KP + wave * 1536 + lane * 8) = v2u{0, 0};
    if (tid < 32) *(LAS u32*)(sm + LD_U + (tid >> 4) * 4160 + (tid & 15) * 4) = 0;
    SB();
    v2u tin[8];
    w1_task_load(p, w1task, lane, tin);
    SB();
    const u32 tag = __builtin_amdgcn_readfirstlane(tagv) + 1u;
    st16(rs, xxv, v4u{xa[0], tag, xb[0], tag});
    st16(rs, xxv + 16384, v4u{xa[1], tag, xb[1], tag});
    SB();
    {
        f32x2 v[4];
#pragma unroll
        for (int i = 0; i < 4; i++) v[i] = f32x2{bflo(xr[i]), bfhi(xr[i])};
        const f32x2 s2 = (v[0] + v[1]) + (v[2] + v[3]);
        const float mean = wave_sum(s2.x + s2.y) * (1.f / D);
#pragma unroll
        for (int i = 0; i < 4; i++) v[i] -= mean;
        const f32x2 q2 = __builtin_elementwise_fma(v[0], v[0], v[1] * v[1]) + __builtin_elementwise_fma(v[2], v[2], v[3] * v[3]);
        const float var = rbf(wave_sum(lane < 56 ? q2.x + q2.y : 0.f) * (1.f / D));
        const float sd = __builtin_amdgcn_sqrtf(rbf(var + 1e-5f));
        st8(rs, flv, WS_ST1 + (((b & 1) * 512 + 4 * (b >> 1) + (wave >> 1)) * 2 + (wave & 1)) * 8, v2u{pk_bf16(mean, sd), tag});
    }
    SB();
    STAMP(1);
    const int tau = main_blk ? (xcd >> 1) * 4 + (i28 & 3) : (b & 15), phi = (xcd & 1) * 7 + (i28 >> 2);
    const int del = i28 >> 2, tau2 = 2 * tau + (xcd & 1);
    const int trl = q4 * 128 + 8 * (pp & 1) + 16 * (2 * G1 + (pp >> 1)), sw = (q4 & 2) << 5;
    const int hf_v = (lane < 56 ? lane : 0) * 4, hf_s = WS_HF + ((tau >> 2) * 4 + wq) * 256;
    const v4u srd = {(u32)(size_t)p.ws, (u32)((size_t)p.ws >> 32) & 0xffffu, (u32)WS_END, 0x00020000u};
    const int hvoff = WS_HT + ((8 * wq + (lane >> 3)) * L + tau * 64) * 2 + (((lane & 7) ^ (((lane >> 3) & 2) << 1)) << 4);
    if (!main_blk) {
        BARRIER_RAW();
        const int w2task = hw;
        if (w2task < 196) {
            w2_task(p, rs, w2task, lane, sm + LD_HB + wave * 8192);
            wait_vm<0>();
            st4(rs, flv, WS_WF2 + (w2task / 28) * 128 + (w2task % 28) * 4, tag);
        }
        asm volatile("" :: "v"(cpf), "v"(tin[7]));
        STAMP(2);
        return;
    }
    asm volatile("" :: "v"(r_are), "v"(r_aim), "v"(r_bre), "v"(r_bim), "v"(r_cre), "v"(r_cim), "v"(r_dsk), "v"(cpf), "v"(tin[7]));
    if (w1task >= 0) w1_task_store(rs, w1task, lane, tin);
    SB();

    float unscale;
    {
        const float dt = rbf(__expf(ch ? bfhi(r_ldt) : bflo(r_ldt)));
        const float ar = dt * bflo(r_are), ai = dt * bflo(r_aim);
        const float br = dt * bflo(r_bre), bi = dt * bflo(r_bim);
        const float cr = bflo(r_cre), ci = bflo(r_cim);
        cf cb = {cr * br - ci * bi, -(cr * bi + ci * br)};
        const float a2 = ar * 1.44269504088896340736f;
        const float rh = ai * 0.15915494309189535f;
        const float rl = __builtin_fmaf(ai, 0.15915494309189535f, -rh) + ai * 6.4206382e-9f;
        auto cexp = [&](float s) __attribute__((always_inline)) -> cf {
            const float e = __builtin_amdgcn_exp2f(a2 * s);
            const float r = __builtin_amdgcn_fractf(rh * s) + rl * s;
            return cf{__builtin_amdgcn_cosf(r), __builtin_amdgcn_sinf(r)} * e;
        };
        const float grow = fmaxf(1.f, __builtin_amdgcn_exp2f(a2 * 2048.f));
        const float sb = wave_sum((fabsf(cb.x) + fabsf(cb.y)) * grow);
        *(LAS float*)(sm + LD_SB + wave * 4) = sb;
        int ex = (int)((__builtin_bit_cast(u32, sb) >> 23) & 255);
        ex = min(max(ex, 30), 230);
        unscale = __builtin_bit_cast(float, (u32)(127 - 15 - 126 + ex) << 23);
        cb *= __builtin_bit_cast(float, (u32)(127 + 15 + 126 - ex) << 23);

        const cf e1 = cexp(1.f), e16 = cexp(16.f), e256 = cexp(256.f);
        const cf e2 = cmul(e1, e1), e32 = cmul(e16, e16), e512 = cmul(e256, e256), x1 = cmulc(cb, e1), x256 = cmulc(cb, e256);
        SB();
        const cf e4 = cmul(e2, e2), e64 = cmul(e32, e32), e1024 = cmul(e512, e512), x2 = cmulc(cb, e2), x3 = cmulc(x1, e2);
        const cf x512 = cmulc(cb, e512), x768 = cmulc(x256, e512), y6 = cmul(e16, e32);
        SB();
        const cf e8 = cmul(e4, e4), e128 = cmul(e64, e64);
        const cf x4 = cmulc(cb, e4), x5 = cmulc(x1, e4), x6 = cmulc(x2, e4), x7 = cmulc(x3, e4), x258 = cmulc(x2, e256);
        const cf x1024 = cmulc(cb, e1024), x1280 = cmulc(x256, e1024), x1536 = cmulc(x512, e1024), x1792 = cmulc(x768, e1024);
        SB();
        auto put = [&](int q, cf v) __attribute__((always_inline)) { *(LAS u32*)(xyw + q * XY_STRIDE) = pkh(v); };
        cf y[8];
        y[0] = cf{1.f, 0.f}; y[1] = e8; y[2] = e16; y[3] = cmul(e8, e16); y[4] = e32; y[5] = cmul(e8, e32); y[6] = y6;
        const cf e192 = cmul(e128, e64), x8 = cmulc(x4, e4), x260 = cmulc(x4, e256), x262 = cmulc(x6, e256), x516 = cmulc(x4, e512);
        put(32, cb); put(33, x1); put(34, x2); put(35, x3); put(36, x4); put(37, x5); put(38, x6); put(39, x7);
        put(40, x256); put(41, x258);
        SB();
        y[7] = cmul(y[3], e32);
        const cf x264 = cmulc(x8, e256), x520 = cmulc(x8, e512), x772 = cmulc(x260, e512), x1032 = cmulc(x8, e1024);
        cf t[8];
#pragma unroll
        for (int q = 1; q < 7; q++) t[q] = cmul(y[q], e64);
        put(42, x260); put(43, x262); put(45, x512); put(46, x516);
#pragma unroll
        for (int q = 0; q < 6; q++) put(q, y[q]);
        SB();
        t[7] = cmul(y[7], e64);
        const cf x776 = cmulc(x264, e512), x1288 = cmulc(x264, e1024), x1544 = cmulc(x520, e1024);
        cf t2[8];
#pragma unroll
        for (int q = 1; q < 8; q++) t2[q] = cmul(y[q], e128);
        put(6, y[6]); put(7, y[7]); put(8, e64);
#pragma unroll
        for (int q = 1; q < 6; q++) put(8 + q, t[q]);
        put(44, x264); put(47, x520); put(48, x768); put(49, x772); put(51, x1024); put(52, x1032);
        SB();
        const cf x1800 = cmulc(x776, e1024);
        cf t3[8];
#pragma unroll
        for (int q = 1; q < 8; q++) t3[q] = cmul(y[q], e192);
        put(14, t[6]); put(15, t[7]); put(16, e128);
#pragma unroll
        for (int q = 1; q < 8; q++) put(16 + q, t2[q]);
        put(50, x776); put(53, x1280); put(54, x1288); put(55, x1536); put(56, x1544); put(57, x1792);
        SB();
        put(24, e192);
#pragma unroll
        for (int q = 1; q < 8; q++) put(24 + q, t3[q]);
        put(58, x1800);
    }
    SB();
    STAMP(2);
    const int t16 = tid * 16;
    v4u gx[2], gs[2];
    {
        f32x16 kd;
#pragma unroll
        for (int i2 = 0; i2 < 16; i2++) kd[i2] = 0.f;
        v4u fa[8], fb[8];
#pragma unroll
        for (int s = 0; s < 8; s++) { fa[s] = *(LAS v4u*)(pa + s * 32); fb[s] = *(LAS v4u*)(pb + s * 32); }
        SB();
#pragma unroll
        for (int s = 0; s < 8; s++) kd = mfma_f16(fa[s], fb[s], kd);
        SB();
        if (w1task >= 0) { wait_vm<0>(); st4(rs, flv, w1_flag(w1task), tag); }
        SB();
#pragma unroll
        for (int g2 = 0; g2 < 4; g2++)
            *(LAS f32x4*)(exw + g2 * 1024) = f32x4{kd[4 * g2], kd[4 * g2 + 1], kd[4 * g2 + 2], kd[4 * g2 + 3]} * unscale;
    }
    BARRIER();
    __builtin_amdgcn_s_setprio(0);
    gx[0] = ld16(rs, t16, WS_XX + pair * 16384); gx[1] = ld16(rs, t16, WS_XX + pair * 16384 + 8192);
    gs[0] = ld16(rs, t16, WS_ST1); gs[1] = ld16(rs, t16, WS_ST1 + 8192);
    const int l28 = (col < 28 ? col : 0) * 4;
    SB();
    STAMP(3);
    float ik0, ik1;
    {
        const f32x4 s0 = *(LAS f32x4*)(sm + LD_SB), s1 = *(LAS f32x4*)(sm + LD_SB + 16);
        const f32x4 a0 = *(LAS f32x4*)(exr), a1 = *(LAS f32x4*)(exr + XY_WAVE), a2 = *(LAS f32x4*)(exr + 2 * XY_WAVE), a3 = *(LAS f32x4*)(exr + 3 * XY_WAVE);
        const float b0 = (s0[0] + s0[1]) + (s0[2] + s0[3]), b1 = (s1[0] + s1[1]) + (s1[2] + s1[3]);
        int e0 = (int)((__builtin_bit_cast(u32, b0) >> 23) & 255), e1 = (int)((__builtin_bit_cast(u32, b1) >> 23) & 255);
        e0 = min(max(e0, 30), 230); e1 = min(max(e1, 30), 230);
        ik0 = __builtin_bit_cast(float, (u32)(127 - 15 - 126 + e0) << 23);
        ik1 = __builtin_bit_cast(float, (u32)(127 - 15 - 126 + e1) << 23);
        const float sk = __builtin_bit_cast(float, (u32)(127 + 15 + 126 - (ch ? e1 : e0)) << 23);
        const f32x4 v = ((a0 + a1) + (a2 + a3)) * sk;
        const u32 p01 = pk_f16(v[0], v[1]), p23 = pk_f16(v[2], v[3]);
        LAS char* o = kpw;
        if (wq == 0) {
            *(LAS u16*)(o) = (u16)p01; *(LAS u16*)(o + 1536) = (u16)(p01 >> 16);
            *(LAS u16*)(o + 2 * 1536) = (u16)p23; *(LAS u16*)(o + 3 * 1536) = (u16)(p23 >> 16);
        } else if (wq == 1) {
            *(LAS u16*)(o) = (u16)p01; *(LAS u16*)(o + 1536) = (u16)p01;
            *(LAS u16*)(o + 2 * 1536) = (u16)(p01 >> 16); *(LAS u16*)(o + 3 * 1536) = (u16)p23;
        } else if (wq == 2) {
            *(LAS u32*)(o) = p01; *(LAS u32*)(o + 1536) = p01;
            *(LAS u32*)(o + 2 * 1536) = (p01 & 0xffffu) | (p23 << 16);
            *(LAS u32*)(o + 3 * 1536) = (p01 >> 16) | (p23 << 16);
        } else {
            const bool ev = (col & 1) == 0;
            *(LAS u32*)(o) = ev ? ((p01 & 0xffffu) | (p01 << 16)) : p01;
            *(LAS u32*)(o + 1536) = p01; *(LAS u32*)(o + 2 * 1536) = p01; *(LAS u32*)(o + 3 * 1536) = p01;
            *(LAS u32*)(o + 128) = ev ? ((p23 & 0xffffu) | (p23 << 16)) : p23;
            *(LAS u32*)(o + 128 + 1536) = p23; *(LAS u32*)(o + 128 + 2 * 1536) = p23; *(LAS u32*)(o + 128 + 3 * 1536) = p23;
        }
    }
    auto gr_ok = [&]() __attribute__((always_inline)) {
        const u32 a = (gx[0][1] ^ tag) | (gx[0][3] ^ tag) | (gx[1][1] ^ tag) | (gx[1][3] ^ tag);
        const u32 c = (gs[0][1] ^ tag) | (gs[0][3] ^ tag) | (gs[1][1] ^ tag) | (gs[1][3] ^ tag);
        return __all((a | c) == 0);
    };
    if (!gr_ok()) {
        int spins = SPIN_CAP;
#pragma unroll 1
        do {
            __builtin_amdgcn_s_sleep(1);
            gx[0] = ld16(rs, t16, WS_XX + pair * 16384); gx[1] = ld16(rs, t16, WS_XX + pair * 16384 + 8192);
            gs[0] = ld16(rs, t16, WS_ST1); gs[1] = ld16(rs, t16, WS_ST1 + 8192);
        } while (!gr_ok() && --spins > 0);
    }
    STAMP(4);
    const u32 wfl = ld4(rs, l28, WS_WF1 + (phi * 2 + (wave >> 2)) * 128);
    SB();
    u32 xc[4], udk[4];
    {
        float u0[4], u1[4];
#pragma unroll
        for (int i = 0; i < 4; i++) {
            xc[i] = gx[i >> 1][2 * (i & 1)];
            const u32 st = gs[i >> 1][2 * (i & 1)];
            const float m = bflo(st), r = __builtin_amdgcn_rcpf(bfhi(st));
            const u32 dd = pk_bf16(bflo(xc[i]) - m, bfhi(xc[i]) - m);
            const u32 uu = pk_bf16(bflo(dd) * r, bfhi(dd) * r);
            u0[i] = bflo(uu); u1[i] = bfhi(uu);
        }
        *(LAS v2u*)(uw) = v2u{pk_f16(u0[0], u0[1]), pk_f16(u0[2], u0[3])};
        *(LAS v2u*)(uw + 4160) = v2u{pk_f16(u1[0], u1[1]), pk_f16(u1[2], u1[3])};
        SB();
#pragma unroll
        for (int i = 0; i < 4; i++) { udk[i] = pk_bf16(u0[i] * bflo(r_dsk), u1[i] * bfhi(r_dsk)); PIN(udk[i]); PIN(xc[i]); }
    }
    v4u wf[28];
    const int w1off = WS_W1T + (phi * 8 + wave) * 28 * 1024;
    int seg7[4];
#pragma unroll
    for (int i = 0; i < 4; i++) seg7[i] = ((i + i28 * ROT_W1) & 3) * 7;
    auto w1_load = [&](int ks) __attribute__((always_inline)) {
        wf[ks] = ld16(rs, l16 + ((ks % 7) & 3) * 1024, w1off + seg7[ks / 7] * 1024 + ((ks % 7) >> 2) * 4096);
    };
    BARRIER();
    STAMP(5);
    auto conv_epilogue = [&](int pbase) __attribute__((always_inline)) {
        const int c1 = tid & 255;
        LAS char* q1 = sm + pbase + ((c1 >> 3) + 1) * 144 + (c1 & 7) * 16;
        LAS char* q0 = sm + pbase + (((c1 - 1) >> 3) + 1) * 144 + ((c1 - 1) & 7) * 16;
        f32x4 r0[2], r1[2], l3[2];
        f32x2 r2[2], l2[2];
        float l1[2], r3[2];
#pragma unroll
        for (int c = 0; c < 2; c++) {
            const int pb_ = c * 4 * P_WAVE;
            r0[c] = *(LAS f32x4*)(q1 + pb_);
            r1[c] = *(LAS f32x4*)(q1 + pb_ + P_WAVE); l1[c] = *(LAS float*)(q0 + pb_ + P_WAVE + 12);
            r2[c] = *(LAS f32x2*)(q1 + pb_ + 2 * P_WAVE); l2[c] = *(LAS f32x2*)(q0 + pb_ + 2 * P_WAVE + 8);
            r3[c] = *(LAS float*)(q1 + pb_ + 3 * P_WAVE); l3[c] = *(LAS f32x4*)(q0 + pb_ + 3 * P_WAVE);
        }
        SB();
#pragma unroll
        for (int c = 0; c < 2; c++) {
            const float ik = c ? ik1 : ik0;
            const float y0 = ((r0[c][0] + l2[c][0]) + (l1[c] + l3[c][1])) * ik;
            const float y1 = ((r0[c][1] + l2[c][1]) + (r1[c][0] + l3[c][2])) * ik;
            const float y2 = ((r0[c][2] + r2[c][0]) + (r1[c][1] + l3[c][3])) * ik;
            const float y3 = ((r0[c][3] + r2[c][1]) + (r1[c][2] + r3[c])) * ik;
            const u32 ya = pk_bf16s(y0, y1), yb = pk_bf16s(y2, y3);
            auto sel = [&](u32 v) __attribute__((always_inline)) { return c ? bfhi(v) : bflo(v); };
            const u32 sa = pk_bf16s(bflo(ya) + sel(udk[0]), bfhi(ya) + sel(udk[1])), sb2 = pk_bf16s(bflo(yb) + sel(udk[2]), bfhi(yb) + sel(udk[3]));
            st8(rs, WS_HT + tid * 8, (d0 + c) * (L * 2),
                v2u{pk_bf16s(bflo(sa) + sel(xc[0]), bfhi(sa) + sel(xc[1])), pk_bf16s(bflo(sb2) + sel(xc[2]), bfhi(sb2) + sel(xc[3]))});
        }
    };
    const int hf_set = WS_HF + (wave * 4 + ((pair >> 2) & 3)) * 256 + ((pair >> 4) * 4 + (pair & 3)) * 4;
    {
        f32x16 acc0, acc1;
#pragma unroll
        for (int i = 0; i < 16; i++) { acc0[i] = 0.f; acc1[i] = 0.f; }
        constexpr int PD = 3;
        v4u fu[PD + 1], fk[16], fk1[PD + 1];
        auto issue = [&](int n) __attribute__((always_inline)) {
            const int B0 = (n >= 16 && n < 32) ? 2016 - 64 * (n - 16) : 992 - 64 * (n & 15);
            fu[n % (PD + 1)] = lds_tr2(ua + 2 * (B0 - 32) + 32, ua + 2 * (B0 - 32));
            if (n < 16) fk[n] = *(LAS v4u*)(ka + 32 * n);
            if (n >= 32) fk1[n % (PD + 1)] = *(LAS v4u*)(ka + 32 * (n - 16));
        };
#pragma unroll
        for (int n = 0; n < PD; n++) issue(n);
        SB();
        static_for<0, 48>([&](auto N_) __attribute__((always_inline)) {
            constexpr int n = decltype(N_)::v;
            if (n + PD < 48) issue(n + PD);
            if constexpr (n < 16) acc0 = mfma_f16(fu[n % (PD + 1)], fk[n], acc0);
            else if constexpr (n < 32) acc1 = mfma_f16(fu[n % (PD + 1)], fk[n - 16], acc1);
            else acc1 = mfma_f16(fu[n % (PD + 1)], fk1[n % (PD + 1)], acc1);
            if constexpr (n >= 17 && n < 19) {
                constexpr int g = 2 * (n - 17);
                *(LAS f32x4*)(pw + LD_PA + g * 32) = f32x4{acc0[4 * g], acc0[4 * g + 1], acc0[4 * g + 2], acc0[4 * g + 3]};
                *(LAS f32x4*)(pw + LD_PA + g * 32 + 32) = f32x4{acc0[4 * g + 4], acc0[4 * g + 5], acc0[4 * g + 6], acc0[4 * g + 7]};
            }
            if (n == 19) {
                PIN(acc1);
                if (lane == 63) {
                    *(LAS f32x4*)(sm + LD_PA + wave * P_WAVE + 112) = f32x4{0.f, 0.f, 0.f, 0.f};
                    *(LAS f32x4*)(sm + LD_PB + wave * P_WAVE + 112) = f32x4{acc0[12], acc0[13], acc0[14], acc0[15]};
                }
            }
            if (n == W1_N0) { PIN(acc0); SB(); wait_flags(rs, l28, WS_WF1 + (phi * 2 + (wave >> 2)) * 128, wfl, tag); STAMP(18); }
            if (n == 20) {
                PIN(acc1);
                BARRIER();
                STAMP(19);
                if (b == 0) st4(rs, tid == 0 ? WS_EPOCH : OOB, 0, tag);
                if (wave < 4) { __builtin_amdgcn_s_setprio(2); conv_epilogue(LD_PA); __builtin_amdgcn_s_setprio(0); }
                STAMP(20);
                SB();
            }
            if (n == HF_N) {
                PIN(acc1);
                STAMP(22);
                if (wave < 4) { wait_vm<w1_cnt(HF_N) - w1_cnt(20)>(); st4(rs, flv, hf_set, tag); }
                STAMP(23);
                SB();
            }
            if constexpr (w1_cnt(n + 1) > w1_cnt(n)) w1_load(w1_cnt(n));
            SB();
        });
#pragma unroll
        for (int g = 0; g < 4; g++) *(LAS f32x4*)(pw + LD_PB + g * 32) = f32x4{acc1[4 * g], acc1[4 * g + 1], acc1[4 * g + 2], acc1[4 * g + 3]};
    }
    BARRIER();
    STAMP(6);
    int rot14[14];
    {
        int i = (2 * del + ((i28 & 3) * 7 >> 1)) * ROT_W2;
        i = i >= 14 ? i - 14 : i;
#pragma unroll
        for (int n = 0; n < 14; n++) { rot14[n] = i * 1024; i = i == 13 ? 0 : i + 1; }
    }
    auto load_h = [&](int hf) __attribute__((always_inline)) {
        const u32 lbase = (u32)(size_t)sm + (hf ? LD_HB : LD_HA) + wq * 1024;
#pragma unroll
        for (int n = 0; n < 14; n++) glds16(srd, hvoff + hf * 2048, rot14[n] * 128, lbase + rot14[n] * 4);
    };
    auto w1_rest = [&]() __attribute__((always_inline)) {
#pragma unroll
        for (int ks = w1_cnt(48); ks < 28; ks++) { w1_load(ks); SB(); }
    };
    auto colsum = [&]() __attribute__((always_inline)) {
        float c0 = 0.f, c1 = 0.f;
#pragma unroll
        for (int ks = 0; ks < 28; ks++) {
            c0 = dot2bf(wf[ks][0], 0x3f803f80u, c0); c1 = dot2bf(wf[ks][1], 0x3f803f80u, c1);
            c0 = dot2bf(wf[ks][2], 0x3f803f80u, c0); c1 = dot2bf(wf[ks][3], 0x3f803f80u, c1);
        }
        *(LAS float*)(sm + LD_CS + wave * 128 + col * 4) = half_sum(c0 + c1);
    };
    if (wave < 4) {
        w1_rest();
        const u32 w2fl = ld4(rs, l28, WS_WF2 + del * 128);
        SB();
        colsum();
        SB();
        STAMP(21);
        wait_flags(rs, hf_v, hf_s, ld4(rs, hf_v, hf_s), tag);
        wait_flags(rs, l28, WS_WF2 + del * 128, w2fl, tag);
        STAMP(7);
        load_h(0);
        SB();
        asm volatile("; A" ::: "memory");
    } else {
        conv_epilogue(LD_PB);
        SB();
        w1_rest();
        const u32 w2fl = ld4(rs, l28, WS_WF2 + del * 128);
        SB();
        wait_vm<28 - w1_cnt(48) + 1>();
        st4(rs, flv, hf_set, tag);
        SB();
        colsum();
        wait_flags(rs, l28, WS_WF2 + del * 128, w2fl, tag);
        asm volatile("; B" ::: "memory");
    }
    __builtin_amdgcn_s_waitcnt(0x0F70);
    wait_vm<0>();
    STAMP(8);
    BARRIER();
    STAMP(9);
    v4u w2[28];
    const int w2v = WS_W2T + (del * 2 * 112 + wave * 14) * 1024 + l16;
    auto w2_load = [&](int f) __attribute__((always_inline)) { w2[f] = ld16(rs, w2v + (f & 1) * 112 * 1024, rot14[f >> 1]); };
    const int kap = phi * 8 + wave;
    const int af_set = WS_AF + (tau * 16 + kap / 14) * 128 + (kap % 14) * 4;
    {
        LAS char* at0 = opaque(sm + LD_HA + 8 * h * 128 + trl + sw);
        LAS char* at1 = opaque(sm + LD_HA + 8 * h * 128 + trl + (64 ^ sw));
        LAS char* csr = opaque(sm + LD_CS + wave * 128 + h * 16);
        LAS char* as = opaque(sm + LD_HA + 8 * h * 128 + trl + (((wave & 1) << 6) ^ sw) + (wave >> 1) * 7 * 2048);
        LAS char* str = opaque(sm + LD_ST + col * 32);
        LAS char* cur;
        constexpr int PD = 4;
        v4u fr[PD + 1], sf;
        auto issue = [&](int n) __attribute__((always_inline)) {
            const int tile = n / 28, j = n % 28;
            if (j % 7 == 0) cur = opaque(((tile & 1) ? at1 : at0) + (seg7[j / 7] * 2048 + (tile >> 1) * 57344));
            LAS char* a = cur + (j % 7) * 2048;
            fr[n % (PD + 1)] = lds_tr2(a, a + 512);
        };
        f32x16 acc[2];
        float nm, k0, k1, sd2, eg, eu, ez, er, eo0;
        f32x4 cg4, cu4;
        f32x4 sta, stb;
        float s1, s2, s1b, s2b;
        u32 opk[4];
        auto stat_acc = [&](int k, int part) __attribute__((always_inline)) {
            if (k == 0 && part == 0) { s1 = 0.f; s2 = 0.f; s1b = 0.f; s2b = 0.f; }
            s1 = dot2bf(sf[2 * part], 0x3f803f80u, s1); s2 = dot2bf(sf[2 * part], sf[2 * part], s2);
            s1b = dot2bf(sf[2 * part + 1], 0x3f803f80u, s1b); s2b = dot2bf(sf[2 * part + 1], sf[2 * part + 1], s2b);
        };
        auto stat_pin = [&]() __attribute__((always_inline)) { asm volatile("" : "+v"(s1), "+v"(s2), "+v"(s1b), "+v"(s2b)); };
        auto stat_write = [&](int hf) __attribute__((always_inline)) {
            const float a = half_sum(s1 + s1b), c = half_sum(s2 + s2b);
            *(LAS f32x2*)(str + (hf * 2 + (wave & 1)) * 1024 + (wave >> 1) * 8) = f32x2{a, c};
        };
        auto epi_read = [&](int tile) __attribute__((always_inline)) { sta = *(LAS f32x4*)(str + tile * 1024); stb = *(LAS f32x4*)(str + tile * 1024 + 16); };
        auto epi_stats = [&]() __attribute__((always_inline)) {
            float m = ((sta[0] + sta[2]) + (stb[0] + stb[2])) * (1.f / D), e2 = ((sta[1] + sta[3]) + (stb[1] + stb[3])) * (1.f / D);
            asm("" : "+v"(m)); asm("" : "+v"(e2));
            const float var = rbf(fmaxf(e2 - m * m, 0.f));
            const float sd = rbf(__builtin_amdgcn_sqrtf(rbf(var + 1e-5f))), r = __builtin_amdgcn_rcpf(sd);
            nm = -rbf(m); sd2 = sd * sd; k0 = r * -2.3022082f; k1 = (r * r) * (r * -0.10294324f);
        };
        auto cs_read = [&](int g) __attribute__((always_inline)) { cg4 = *(LAS f32x4*)(csr + g * 32); cu4 = *(LAS f32x4*)(csr + 64 + g * 32); };
        auto slice = [&](const f32x16& a, int sidx) __attribute__((always_inline)) {
            const int e = sidx / 3, st = sidx % 3;
            if (st == 0) {
                if (e > 0) { float o = eu * er; asm("" : "+v"(o)); if ((e - 1) & 1) opk[(e - 1) >> 1] = pk_bf16(eo0, o); else eo0 = o; }
                if (e < 8) {
                    eg = __builtin_fmaf(nm, cg4[e & 3], a[e & 7]); eu = __builtin_fmaf(nm, cu4[e & 3], a[8 + (e & 7)]);
                    asm("" : "+v"(eg), "+v"(eu));
                }
            } else if (st == 1) { ez = __builtin_amdgcn_exp2f(eg * __builtin_fmaf(eg * eg, k1, k0)); asm("" : "+v"(ez)); }
            else { er = __builtin_amdgcn_rcpf(__builtin_fmaf(ez, sd2, sd2)); eu *= eg; asm("" : "+v"(er), "+v"(eu)); }
        };
        auto epi_four = [&](const f32x16& a, int g) __attribute__((always_inline)) {
            const f32x4 nm4 = {nm, nm, nm, nm}, sd4 = {sd2, sd2, sd2, sd2};
            const f32x4 gg = __builtin_elementwise_fma(nm4, cg4, f32x4{a[4 * g], a[4 * g + 1], a[4 * g + 2], a[4 * g + 3]});
            const f32x4 u = __builtin_elementwise_fma(nm4, cu4, f32x4{a[8 + 4 * g], a[9 + 4 * g], a[10 + 4 * g], a[11 + 4 * g]});
            const f32x4 z = gg * __builtin_elementwise_fma(gg * gg, f32x4{k1, k1, k1, k1}, f32x4{k0, k0, k0, k0});
            const f32x4 e = {__builtin_amdgcn_exp2f(z.x), __builtin_amdgcn_exp2f(z.y), __builtin_amdgcn_exp2f(z.z), __builtin_amdgcn_exp2f(z.w)};
            const f32x4 dn = __builtin_elementwise_fma(e, sd4, sd4);
            const f32x4 o = (gg * u) * f32x4{__builtin_amdgcn_rcpf(dn.x), __builtin_amdgcn_rcpf(dn.y), __builtin_amdgcn_rcpf(dn.z), __builtin_amdgcn_rcpf(dn.w)};
            opk[2 * g] = pk_bf16(o.x, o.y); opk[2 * g + 1] = pk_bf16(o.z, o.w);
        };
        auto epi_store = [&](int tile) __attribute__((always_inline)) {
            const int tok32 = (tile >> 1) * 32 + tau * 2 + (tile & 1);
            st16(rs, WS_ACT + l16 + (tok32 * 112 + kap) * 1024, v4u{opk[0], opk[1], opk[2], opk[3]});
        };
#pragma unroll
        for (int n = 0; n < PD; n++) issue(n);
        SB();
        static_for<0, 112>([&](auto N_) __attribute__((always_inline)) {
            constexpr int n = decltype(N_)::v;
            constexpr int tile = n / 28, j = n % 28;
            if (n == 50) {
                STAMP(27);
                wait_vm<w2_cnt(50)>();
                BARRIER_RAW();
                as = opaque(as + 57344);
                STAMP(28);
            }
            if (n + PD < 112) issue(n + PD);
            if (j == 0) {
                f32x16 z;
#pragma unroll
                for (int i = 0; i < 16; i++) z[i] = 0.f;
                acc[tile & 1] = mfma_bf16(wf[0], fr[n % (PD + 1)], z);
            } else {
                acc[tile & 1] = mfma_bf16(wf[j], fr[n % (PD + 1)], acc[tile & 1]);
            }
            constexpr int sk = n < 28 ? n : n - 52;
            if (sk >= 0 && sk < 21 && sk % 3 == 0) { if (sk > 0) { stat_acc(sk / 3 - 1, 1); SB(); } sf = lds_tr2(as + (sk / 3) * 2048, as + (sk / 3) * 2048 + 512); }
            if (sk >= 0 && sk < 21 && sk % 3 == 2) stat_acc(sk / 3, 0);
            if (sk == 21) stat_acc(6, 1);
            if (sk == 22) stat_write(n >= 28);
            if (n == HB_N) {
                PIN(acc[0]);
                if (HB_N >= 2 && HB_N < 22) stat_pin();
                SB();
                STAMP(24);
                if (wave < 4) { wait_flags(rs, hf_v, hf_s + 4096, ld4(rs, hf_v, hf_s + 4096), tag); STAMP(25); load_h(1); }
            }
            if (n == 27 || n == 81) { STAMP(n == 27 ? 26 : 30); BARRIER_RAW(); STAMP(n == 27 ? 29 : 31); }
            if (j == 27 && tile < 3) epi_read(tile);
            if (tile > 0) {
                if (j == 0) cs_read(0);
                if (j == 1) epi_stats();
                if (j >= 2 && j < 27) slice(acc[(tile - 1) & 1], j - 2);
                if (j == 11) { SB(); cs_read(1); }
                if (j == 27) epi_store(tile - 1);
            }
            if (n == AF_N) { wait_vm<w2_cnt(AF_N) - w2_cnt(84)>(); st4(rs, flv, af_set, tag); }
            if constexpr (w2_cnt(n + 1) > w2_cnt(n)) w2_load(w2_cnt(n));
            SB();
        });
        epi_read(3); cs_read(0);
        epi_stats();
        epi_four(acc[1], 0);
        cs_read(1);
        epi_four(acc[1], 1);
        epi_store(3);
    }
    SB();
    STAMP(10);
    const int af_v = (lane < 14 ? lane : 0) * 4, af_s = WS_AF + (tau * 16 + wave) * 128;
    u32 afl = ld4(rs, af_v, af_s);
#pragma unroll
    for (int f = W2_IN; f < 28; f++) w2_load(f);
    SB();
    const int dd = 64 * del + 8 * wave + 4 * h;
    v2u hr[2];
    {
        LAS char* a = sm + LD_HA + dd * 128 + trl + (((xcd & 1) << 6) ^ sw);
        hr[0] = lds_tr(a); hr[1] = lds_tr(a + 57344);
    }
    asm volatile("" :: "v"(afl));
    wait_vm<28 - W2_IN>();
    st4(rs, flv, af_set + 1024, tag);
    STAMP(11);
    wait_flags(rs, af_v, af_s, afl, tag);
    STAMP(12);
    const int av = WS_ACT + (tau2 * 112 + wave * 14) * 1024 + l16;
    v4u a0[14], a1[14];
#pragma unroll
    for (int n = 0; n < 14; n++) { a0[n] = ld16(rs, av, rot14[n]); SB(); }
    SB();
    BARRIER();
    f32x16 acc[2][2];
#pragma unroll
    for (int i = 0; i < 16; i++) { acc[0][0][i] = 0.f; acc[0][1][i] = 0.f; acc[1][0][i] = 0.f; acc[1][1][i] = 0.f; }
    LAS char* rw = opaque(sm + wave * 1024 + l16);
    LAS char* rr = opaque(sm + wave * 8192 + l16);
    auto red_write = [&](int hf) __attribute__((always_inline)) {
#pragma unroll
        for (int o = 0; o < 8; o++) {
            const f32x16& t = acc[o >> 2][hf];
            const int r = 4 * (o & 3);
            *(LAS f32x4*)(rw + hf * 65536 + o * 8192) = f32x4{t[r], t[r + 1], t[r + 2], t[r + 3]};
        }
    };
    auto red_finish = [&](int hf) __attribute__((always_inline)) {
        f32x4 v[8];
#pragma unroll
        for (int s = 0; s < 8; s++) v[s] = *(LAS f32x4*)(rr + hf * 65536 + s * 1024);
        const f32x4 t = ((v[0] + v[1]) + (v[2] + v[3])) + ((v[4] + v[5]) + (v[6] + v[7]));
        const u32 ta = pk_bf16(t[0], t[1]), tb = pk_bf16(t[2], t[3]);
        const int tok = 1024 * hf + 32 * tau2 + col;
        *(v2u*)(p.out + (size_t)tok * D + dd) = v2u{pk_bf16(bflo(ta) + bflo(hr[hf][0]), bfhi(ta) + bfhi(hr[hf][0])),
                                                    pk_bf16(bflo(tb) + bflo(hr[hf][1]), bfhi(tb) + bfhi(hr[hf][1]))};
    };
#pragma unroll
    for (int n = 0; n < 14; n++) {
        acc[0][0] = mfma_bf16(w2[2 * n], a0[n], acc[0][0]);
        acc[1][0] = mfma_bf16(w2[2 * n + 1], a0[n], acc[1][0]);
        SB();
    }
    PIN(acc[0][0]); PIN(acc[1][0]);
    STAMP(13);
    afl = ld4(rs, af_v, af_s + 1024);
    SB();
    red_write(0);
    SB();
    wait_flags(rs, af_v, af_s + 1024, afl, tag);
    STAMP(14);
#pragma unroll
    for (int n = 0; n < 14; n++) { a1[n] = ld16(rs, av + 32 * 112 * 1024, rot14[n]); SB(); }
    SB();
    STAMP(15);
#pragma unroll
    for (int n = 0; n < 14; n++) {
        acc[0][1] = mfma_bf16(w2[2 * n], a1[n], acc[0][1]);
        acc[1][1] = mfma_bf16(w2[2 * n + 1], a1[n], acc[1][1]);
        SB();
    }
    STAMP(16);
    red_write(1);
    BARRIER();
    red_finish(0);
    red_finish(1);
    STAMP(17);
}

}

extern "C" void kernel_launch(void* const* d_in, const int* in_sizes, int n_in,
                              void* d_out, int out_size, void* d_ws, size_t ws_size,
                              hipStream_t stream) {
    Params p;
    p.x = (const u16*)d_in[0]; p.Are = (const u16*)d_in[1]; p.Aim = (const u16*)d_in[2];
    p.Bre = (const u16*)d_in[3]; p.Bim = (const u16*)d_in[4]; p.Cre = (const u16*)d_in[5]; p.Cim = (const u16*)d_in[6];
    p.logdt = (const u16*)d_in[7]; p.Dskip = (const u16*)d_in[8];
    p.wg = (const u16*)d_in[10]; p.wu = (const u16*)d_in[11]; p.wd = (const u16*)d_in[12];
    p.ws = (char*)d_ws; p.out = (u16*)d_out;
    (void)hipFuncSetAttribute((const void*)fused_block, hipFuncAttributeMaxDynamicSharedMemorySize, LDS_BYTES);
    fused_block<<<256, 512, LDS_BYTES, stream>>>(p);
}
```

```cpp
#include <hip/hip_runtime.h>
#include <stdint.h>

namespace {

constexpr int L = 2048, D = 448, N = 256, F = 1792;

typedef unsigned short u16;
typedef unsigned int u32;
typedef unsigned long long u64;
typedef u32 v4u __attribute__((ext_vector_type(4)));
typedef u32 v2u __attribute__((ext_vector_type(2)));
typedef float f32x2 __attribute__((ext_vector_type(2)));
typedef float f32x4 __attribute__((ext_vector_type(4)));
typedef float f32x16 __attribute__((ext_vector_type(16)));
typedef _Float16 f16x2 __attribute__((ext_vector_type(2)));
typedef _Float16 f16x8 __attribute__((ext_vector_type(8)));
typedef __bf16 bf16x2 __attribute__((ext_vector_type(2)));
typedef __bf16 bf16x8 __attribute__((ext_vector_type(8)));
typedef short s16x4 __attribute__((ext_vector_type(4)));
#define GAS __attribute__((address_space(1)))
#define LAS __attribute__((address_space(3)))
#define RLX_AGENT __ATOMIC_RELAXED, __HIP_MEMORY_SCOPE_AGENT
#define SB() __builtin_amdgcn_sched_barrier(0)
#define PIN(x) asm volatile("" : "+v"(x))
#define BARRIER() asm volatile("s_waitcnt lgkmcnt(0)\n\ts_barrier" ::: "memory")
#define BARRIER_RAW() asm volatile("s_barrier" ::: "memory")
constexpr int SC1 = 16;

constexpr int WS_EPOCH = 0;
constexpr int WS_WF2 = 1024;
constexpr int WS_HF = 8192;
constexpr int WS_AF = WS_HF + 64 * 256;
constexpr int WS_WF1 = 65536;
constexpr int WS_CS = 65536 + 8192;
constexpr int WS_ST1 = WS_CS + 32768;
constexpr int WS_XX = WS_ST1 + 16384;
constexpr int WS_W1T = WS_XX + 224 * 16384;
constexpr int WS_W2T = WS_W1T + 112 * 28 * 1024;
constexpr int WS_HT = WS_W2T + 14 * 112 * 1024;
constexpr int WS_ACT = WS_HT + D * L * 2;
constexpr int WS_DBG = WS_ACT + 64 * 112 * 1024;
constexpr int NSTAMP = 32;
constexpr int WS_END = WS_DBG + 256 * NSTAMP * 8;
constexpr int SPIN_CAP = 1 << 16;
static_assert(WS_AF + 16 * 2 * 8 * 128 <= WS_WF1, "flags overlap");

#define STAMP(k) do {} while (0)
#define STAMP_FLUSH() do {} while (0)
#define STAMP_DECL() do {} while (0)

__device__ __forceinline__ float bflo(u32 v) { return __builtin_bit_cast(float, v << 16); }
__device__ __forceinline__ float bfhi(u32 v) { return __builtin_bit_cast(float, v & 0xffff0000u); }
__device__ __forceinline__ u32 pk_bf16(float a, float b) { f32x2 f = {a, b}; return __builtin_bit_cast(u32, __builtin_convertvector(f, bf16x2)); }
__device__ __forceinline__ u32 pk_bf16s(float a, float b) { asm("" : "+v"(a)); asm("" : "+v"(b)); return pk_bf16(a, b); }
__device__ __forceinline__ u32 pk_f16(float a, float b) { f32x2 f = {a, b}; return __builtin_bit_cast(u32, __builtin_convertvector(f, f16x2)); }
__device__ __forceinline__ float rbf(float a) { return bflo(pk_bf16(a, 0.f)); }
__device__ __forceinline__ float dot2bf(u32 a, u32 b, float c) {
    return __builtin_amdgcn_fdot2_f32_bf16(__builtin_bit_cast(bf16x2, a), __builtin_bit_cast(bf16x2, b), c, false);
}
__device__ __forceinline__ f32x16 mfma_bf16(v4u a, v4u b, f32x16 c) {
    return __builtin_amdgcn_mfma_f32_32x32x16_bf16(__builtin_bit_cast(bf16x8, a), __builtin_bit_cast(bf16x8, b), c, 0, 0, 0);
}
__device__ __forceinline__ f32x16 mfma_f16(v4u a, v4u b, f32x16 c) {
    return __builtin_amdgcn_mfma_f32_32x32x16_f16(__builtin_bit_cast(f16x8, a), __builtin_bit_cast(f16x8, b), c, 0, 0, 0);
}
__device__ __forceinline__ v2u lds_tr(LAS char* p) {
    return __builtin_bit_cast(v2u, __builtin_amdgcn_ds_read_tr16_b64_v4i16((LAS s16x4*)p));
}
__device__ __forceinline__ v4u lds_tr2(LAS char* p0, LAS char* p1) {
    const v2u lo = lds_tr(p0), hi = lds_tr(p1);
    return v4u{lo[0], lo[1], hi[0], hi[1]};
}
template <int CTRL> __device__ __forceinline__ float dpp(float v) {
    return __builtin_bit_cast(float, __builtin_amdgcn_update_dpp(0, __builtin_bit_cast(int, v), CTRL, 0xf, 0xf, true));
}
__device__ __forceinline__ float rdl(float v, int l) { return __builtin_bit_cast(float, __builtin_amdgcn_readlane(__builtin_bit_cast(int, v), l)); }
__device__ __forceinline__ float wave_sum(float v) {
    v += dpp<0xB1>(v); v += dpp<0x4E>(v); v += dpp<0x141>(v); v += dpp<0x140>(v);
    return (rdl(v, 0) + rdl(v, 16)) + (rdl(v, 32) + rdl(v, 48));
}
__device__ __forceinline__ float wave_max(float v) {
    v = fmaxf(v, dpp<0xB1>(v)); v = fmaxf(v, dpp<0x4E>(v)); v = fmaxf(v, dpp<0x141>(v)); v = fmaxf(v, dpp<0x140>(v));
    return fmaxf(fmaxf(rdl(v, 0), rdl(v, 16)), fmaxf(rdl(v, 32), rdl(v, 48)));
}
__device__ __forceinline__ float half_sum(float v) {
    const auto r = __builtin_amdgcn_permlane32_swap(__builtin_bit_cast(u32, v), __builtin_bit_cast(u32, v), false, false);
    return __builtin_bit_cast(float, (u32)r[0]) + __builtin_bit_cast(float, (u32)r[1]);
}
template <int I> struct IC { static constexpr int v = I; };
template <int B, int E, class Fn> __device__ __forceinline__ void static_for(Fn&& f) {
    if constexpr (B < E) { f(IC<B>{}); static_for<B + 1, E>(f); }
}

typedef f32x2 cf;
__device__ __forceinline__ cf rot(cf b) { return cf{-b.y, b.x}; }
__device__ __forceinline__ cf cmul(cf a, cf b) { return __builtin_elementwise_fma(a.yy, rot(b), a.xx * b); }
__device__ __forceinline__ cf cmulc(cf a, cf b) { return __builtin_elementwise_fma(a.yy, b.yx, a.xx * rot(b).yx); }
__device__ __forceinline__ u32 pkh(cf a) { return __builtin_bit_cast(u32, __builtin_convertvector(a, f16x2)); }

typedef __amdgpu_buffer_rsrc_t rsrc_t;
__device__ __forceinline__ v4u ld16(rsrc_t r, int voff, int soff) { return __builtin_amdgcn_raw_buffer_load_b128(r, voff, soff, SC1); }
__device__ __forceinline__ u32 ld4(rsrc_t r, int voff, int soff) { return __builtin_amdgcn_raw_buffer_load_b32(r, voff, soff, SC1); }
__device__ __forceinline__ void st16(rsrc_t r, int voff, v4u v) { __builtin_amdgcn_raw_buffer_store_b128(v, r, voff, 0, SC1); }
__device__ __forceinline__ void st8(rsrc_t r, int voff, int soff, v2u v) { __builtin_amdgcn_raw_buffer_store_b64(v, r, voff, soff, SC1); }
__device__ __forceinline__ void st4(rsrc_t r, int voff, int soff, u32 v) { __builtin_amdgcn_raw_buffer_store_b32(v, r, voff, soff, SC1); }
template <int N> __device__ __forceinline__ void wait_vm() { asm volatile("s_waitcnt vmcnt(%0)" :: "i"(N) : "memory"); }
constexpr int OOB = 0x7f000000;

__device__ __forceinline__ void wait_flags(rsrc_t rs, int voff, int soff, u32 v, u32 tag) {
    if (!__all(v == tag)) {
        int spins = SPIN_CAP;
#pragma unroll 1
        do {
            __builtin_amdgcn_s_sleep(1);
            v = ld4(rs, voff, soff);
        } while (!__all(v == tag) && --spins > 0);
    }
    __builtin_amdgcn_fence(__ATOMIC_ACQUIRE, "wavefront");
}
__device__ __forceinline__ void glds16(v4u srd, int voff, int soff, u32 lds_dst) {
    asm volatile("s_mov_b32 m0, %3\n\ts_nop 0\n\tbuffer_load_dwordx4 %0, %1, %2 offen sc1 lds"
                 :: "v"(voff), "s"(srd), "s"(soff), "s"(lds_dst) : "memory");
}
__device__ __forceinline__ LAS char* opaque(LAS char* q) { u32 a = (u32)(size_t)q; asm volatile("" : "+v"(a)); return (LAS char*)(size_t)a; }

struct Params {
    const u16 *x, *Are, *Aim, *Bre, *Bim, *Cre, *Cim, *logdt, *Dskip, *wg, *wu, *wd;
    char* ws;
    u16* out;
};

constexpr int XY_STRIDE = 272, XY_ROWS = 59, XY_WAVE = XY_STRIDE * XY_ROWS;
constexpr int P_WAVE = 33 * 144;
constexpr int LD_PB = 0, LD_PA = 38144;
constexpr int LD_HA = 38144, LD_HB = LD_HA + 57344;
constexpr int LD_XU = LD_HB;
constexpr int LD_KP = 128512;
constexpr int LD_U = LD_KP + 8 * 1536;
constexpr int LD_SB = LD_HB + 57344;
constexpr int LD_ST = LD_SB + 64;
constexpr int LD_CS = LD_ST + 4096;
constexpr int LDS_BYTES = LD_CS + 1024;
static_assert(LD_U + 2 * 4160 <= LD_SB && LDS_BYTES <= 163840, "lds map");

__device__ __forceinline__ v4u tr_piece(const v4u (&in)[8], int c) {
    v4u o;
#pragma unroll
    for (int kp = 0; kp < 4; kp++)
        o[kp] = __builtin_amdgcn_perm(in[2 * kp + 1][c >> 1], in[2 * kp][c >> 1], (c & 1) ? 0x07060302u : 0x05040100u);
    return o;
}
__device__ __forceinline__ void w1_task_load(const Params& p, int task, int lane, v2u (&in)[8]) {
    const int t = (task < 0 ? 0 : task) >> 1, c4 = task & 1;
    const int phi = t / 28, r = t - phi * 28, mat = r >= 14, kp = r - 14 * mat;
    const rsrc_t rw = __builtin_amdgcn_make_buffer_rsrc((void*)(mat ? p.wu : p.wg), 0, task < 0 ? 0 : D * F * 2, 0x00020000);
    const int voff = (8 * (lane >> 4) * F + (lane & 15) * 8) * 2;
#pragma unroll
    for (int k = 0; k < 8; k++) in[k] = __builtin_amdgcn_raw_buffer_load_b64(rw, voff + (k & 1) * F * 2, ((32 * kp + (k & 6)) * F + phi * 128 + 4 * c4) * 2, 0);
}
__device__ __forceinline__ void w1_task_store(rsrc_t rs, int task, int lane, const v2u (&in)[8]) {
    const int t = task >> 1, c4 = task & 1;
    const int phi = t / 28, r = t - phi * 28, mat = r >= 14, kp = r - 14 * mat;
    const int voff = WS_W1T + ((phi * 8 + 4 * c4) * 28 + 2 * kp) * 1024 + mat * 256 + (lane >> 4) * 512 + (lane & 15) * 16;
#pragma unroll
    for (int c = 0; c < 4; c++) {
        v4u o;
#pragma unroll
        for (int kp2 = 0; kp2 < 4; kp2++) o[kp2] = __builtin_amdgcn_perm(in[2 * kp2 + 1][c >> 1], in[2 * kp2][c >> 1], (c & 1) ? 0x07060302u : 0x05040100u);
        st16(rs, voff + c * 28 * 1024, o);
    }
}
__device__ __forceinline__ int w1_flag(int task) { const int t = task >> 1, phi = t / 28; return WS_WF1 + (phi * 2 + (task & 1)) * 128 + (t - phi * 28) * 4; }
__device__ __forceinline__ void w2_task(const Params& p, rsrc_t rs, int task, int lane, LAS char* scr) {
    const int del = task / 28, kg = task - del * 28;
    const int k4 = (lane >> 3) & 3, ks = kg * 4 + k4, h = lane >> 5, dr = lane & 7;
    const u16* src = p.wd + (size_t)((ks >> 3) * 128 + 32 * h + (ks & 7)) * D + del * 64 + dr * 8;
    v4u in[8];
#pragma unroll
    for (int j = 0; j < 8; j++) in[j] = __builtin_nontemporal_load((const v4u*)(src + (size_t)(8 * ((j & 3) + 8 * (j >> 2))) * D));
    LAS char* w = scr + ((dr >> 2) * 4 + k4) * 1024 + (32 * h + 8 * (dr & 3)) * 16;
#pragma unroll
    for (int c = 0; c < 8; c++) *(LAS v4u*)(w + c * 16) = tr_piece(in, c);
    const int voff = WS_W2T + (del * 2 * 112 + kg * 4) * 1024 + lane * 16;
#pragma unroll
    for (int f = 0; f < 8; f++) st16(rs, voff + ((f >> 2) * 112 + (f & 3)) * 1024, *(LAS v4u*)(scr + f * 1024 + lane * 16));
}

#define W1_CHK 2
#define KPRE 32
#define W1_NUM 2
#define W1_DEN 5
#define W1_BURST 4
#define PF1 0
#define PF2 1
#define GR_EARLY 3
#define CS_T 520
#define GR_T 320
#define AFB_EARLY 1
#define W1_ORDER 1
#define PRIO_C 1
constexpr int w1_cnt(int n) { return n <= W1_CHK ? 0 : W1_BURST + ((n - W1_CHK - 1) * W1_NUM) / W1_DEN > 28 ? 28 : W1_BURST + ((n - W1_CHK - 1) * W1_NUM) / W1_DEN; }
#define PRIO_A 0
#define CS_FIRST 1
#define HBP_N 10
#define W2_N0 34
#define HB_N 24
#define ROT_W1 0
#define ROT_W2 1
#define AF_N 84
#define HF_N 41
#define AFP_N 98
constexpr int g1_op(int n) {
    return (n > HB_N && n <= HB_N + 7) ? 32 + (n - HB_N - 1)
         : n == 55 ? 40 : n == 71 ? 41 : n == 111 ? 42 : n == AF_N ? 50 : n == AFP_N ? 51
         : (n >= W2_N0 && n < W2_N0 + 42 && (n & 1) == 0) ? (n - W2_N0) / 2
         : (n >= 85 && n < 99 && (n & 1) == 1) ? 21 + (n - 85) / 2
         : -1;
}
constexpr int g1_cum(int n) { int c = 0; for (int i = 0; i < n; i++) c += g1_op(i) >= 0; return c; }
static_assert(HB_N >= 23 && HB_N < 28 && HB_N + 7 < W2_N0 && (W2_N0 & 1) == 0 && W2_N0 + 28 > 52 && W2_N0 + 42 <= AF_N && (AF_N & 1) == 0 && (AFP_N & 1) == 0 && AFP_N > AF_N && AFP_N < 108, "schedule");
#define W1_MID 6
#define CODE_PF 23552

__global__ __launch_bounds__(512, 2) void fused_block(Params p) {
    extern __shared__ __attribute__((aligned(16))) char smem_[];
    LAS char* sm = (LAS char*)smem_;
    const int tid = threadIdx.x, lane = tid & 63;
    const int wave = __builtin_amdgcn_readfirstlane(tid >> 6);
    const int b = blockIdx.x;
    STAMP_DECL();
    const bool main_blk = b < 224;
    const int xcd = b & 7, i28 = b >> 3;
    const u32 cpf = __builtin_amdgcn_raw_buffer_load_b32(__builtin_amdgcn_make_buffer_rsrc((void*)__builtin_amdgcn_s_getpc(), 0, main_blk ? 0 : CODE_PF, 0x00020000),
                                                         lane < 16 ? lane * 64 : OOB, (wave * 4 + (i28 & 3)) * 1024, 0);
    const int l16 = lane * 16;
    const int rbase = 16 * (b >> 1) + 2 * (b & 1);
    int x2v = lane < 56 ? (lane & 3) * (4 * D * 2) + (lane >> 2) * 8 : OOB, xrv = lane < 56 ? l16 : OOB, t2 = tid * 2;
    int x2s = (rbase * D + 56 * wave) * 2, xrs = (rbase + 4 * (wave >> 1) + (wave & 1)) * D * 2;
    int pair = xcd * 28 + i28;
    asm volatile("" : "+v"(x2v), "+v"(xrv), "+v"(t2), "+s"(x2s), "+s"(xrs), "+s"(pair));
    SB();
    const rsrc_t rs = __builtin_amdgcn_make_buffer_rsrc(p.ws, 0, WS_END, 0x00020000);
    asm volatile("" :: "s"(p.x), "s"(p.wd), "s"(p.ws));
    if (PRIO_A && wave >= 4) __builtin_amdgcn_s_setprio(1);
    STAMP(0);
    const u32 tstart = GR_EARLY == 3 ? (u32)__builtin_amdgcn_s_memrealtime() : 0;
    const u32 tagv = ld4(rs, WS_EPOCH, 0);

    const rsrc_t rx = __builtin_amdgcn_make_buffer_rsrc((void*)p.x, 0, L * D * 2, 0x00020000);
    const v2u xa = __builtin_amdgcn_raw_buffer_load_b64(rx, x2v, x2s, 2);
    const v2u xb = __builtin_amdgcn_raw_buffer_load_b64(rx, x2v + D * 2, x2s, 2);
    const v4u xr = __builtin_amdgcn_raw_buffer_load_b128(rx, xrv, xrs, 2);
    SB();
    const int ch = wave >> 2, wq = wave & 3;
    const int d0 = 2 * pair;
    const int psz = main_blk ? D * N * 2 : 0;
    auto ldp = [&](const u16* q) __attribute__((always_inline)) -> u32 {
        return __builtin_amdgcn_raw_buffer_load_b16(__builtin_amdgcn_make_buffer_rsrc((void*)q, 0, psz, 0x00020000), t2, d0 * N * 2, 2);
    };
    const u32 r_ldt = __builtin_amdgcn_raw_buffer_load_b32(__builtin_amdgcn_make_buffer_rsrc((void*)p.logdt, 0, main_blk ? D * 2 : 0, 0x00020000), 0, d0 * 2, 0);
    const u32 r_are = ldp(p.Are), r_aim = ldp(p.Aim), r_bre = ldp(p.Bre), r_bim = ldp(p.Bim), r_cre = ldp(p.Cre), r_cim = ldp(p.Cim);
    const u32 r_dsk = __builtin_amdgcn_raw_buffer_load_b32(__builtin_amdgcn_make_buffer_rsrc((void*)p.Dskip, 0, main_blk ? D * 2 : 0, 0x00020000), 0, d0 * 2, 0);
    const int hw = (b - 224) + 32 * wave;
    const int w1task = !main_blk ? -1 : wave < 2 ? pair * 2 + wave : (wave < 4 && pair < 168) ? 448 + pair * 2 + wave - 2 : -1;
    SB();
    const int h = lane >> 5, col = lane & 31;
    const int G1 = (lane >> 4) & 1, q4 = (lane & 15) >> 2, pp = lane & 3;
    int xxv = lane < 56 ? WS_XX + ((28 * wave + 2 * (lane >> 2)) * 2 + (b & 1)) * 8192 + (4 * (b >> 1) + (lane & 3)) * 16 : OOB;
    int flv = lane == 0 ? 0 : OOB;
    asm volatile("" : "+v"(xxv), "+v"(flv));
    LAS char* xy = sm + wave * XY_WAVE;
    LAS char* xyw = opaque(xy + lane * 4);
    LAS char* pa = opaque(xy + (32 + (int)((0x1713100d0a080400ull >> (8 * (col >> 2))) & 255) + (col & 3)) * XY_STRIDE + h * 16);
    LAS char* pb = opaque(xy + col * XY_STRIDE + h * 16);
    LAS char* exw = opaque(xy + l16);
    LAS char* exr = opaque(sm + (ch * 4) * XY_WAVE + wq * 1024 + l16);
    LAS char* kpw = opaque(sm + LD_KP + ch * 4 * 1536 + 512 + (wq < 2 ? 64 * wq + 2 * col + h : wq == 2 ? 64 * (2 + h) + 2 * col : 64 * (4 + 2 * h) + 2 * col) * 2);
    LAS char* uw = opaque(sm + LD_U + 64 + tid * 8);
    LAS char* ua = opaque(sm + LD_U + ch * 4160 + 2 * (32 + 32 + 16 * G1 - 32 * h - 16 - 4 * q4 + 4 * pp));
    LAS char* ka = opaque(sm + LD_KP + wave * 1536 + 2 * (256 - 248 + 8 * col + 8 * h));
    LAS char* pw = opaque(sm + wave * P_WAVE + (col + 1) * 144 + h * 16);
    *(LAS v2u*)(sm + LD_KP + wave * 1536 + lane * 8) = v2u{0, 0};
    if (tid < 32) *(LAS u32*)(sm + LD_U + (tid >> 4) * 4160 + (tid & 15) * 4) = 0;
    SB();
    const u32 tag = __builtin_amdgcn_readfirstlane(tagv) + 1u;
    st16(rs, xxv, v4u{xa[0], tag, xb[0], tag});
    st16(rs, xxv + 16384, v4u{xa[1], tag, xb[1], tag});
    SB();
    {
        float a = 0.f, q = 0.f;
#pragma unroll
        for (int i = 0; i < 4; i++) { a = dot2bf(xr[i], 0x3f803f80u, a); q = dot2bf(xr[i], xr[i], q); }
        const float mean = wave_sum(a) * (1.f / D), ex2 = wave_sum(q) * (1.f / D);
        const float var = rbf(fmaxf(ex2 - mean * mean, 0.f));
        const float sd = __builtin_amdgcn_sqrtf(rbf(var + 1e-5f));
        st8(rs, flv, WS_ST1 + (((b & 1) * 512 + 4 * (b >> 1) + (wave >> 1)) * 2 + (wave & 1)) * 8, v2u{pk_bf16(mean, sd), tag});
    }
    SB();
    v2u tin[8];
    w1_task_load(p, w1task, lane, tin);
    SB();
    STAMP(1);
    const int tau = main_blk ? (xcd >> 1) * 4 + (i28 & 3) : (b & 15), phi = (xcd & 1) * 7 + (i28 >> 2);
    const int del = i28 >> 2, tau2 = 2 * tau + (xcd & 1);
    const int trl = q4 * 128 + 8 * (pp & 1) + 16 * (2 * G1 + (pp >> 1)), sw = (q4 & 2) << 5;
    const int hf_v = (lane < 56 ? lane : 0) * 4, hf_s = WS_HF + ((tau >> 2) * 8 + wave) * 256;
    const v4u srd = {(u32)(size_t)p.ws, (u32)((size_t)p.ws >> 32) & 0xffffu, (u32)WS_END, 0x00020000u};
    const int hvoff = WS_HT + ((8 * wave + (lane >> 3)) * L + tau * 64) * 2 + (((lane & 7) ^ (((lane >> 3) & 2) << 1)) << 4);
    if (!main_blk) {
        BARRIER_RAW();
        const int w2task = hw;
        if (w2task < 196) {
            w2_task(p, rs, w2task, lane, sm + LD_HB + wave * 8192);
            wait_vm<0>();
            st4(rs, flv, WS_WF2 + (w2task / 28) * 128 + (w2task % 28) * 4, tag);
        }
        asm volatile("" :: "v"(cpf), "v"(tin[7]));
        STAMP(2);
        const int unit = 255 - hw;
        if (unit < 112) {
            while ((u32)__builtin_amdgcn_s_memrealtime() - tstart < CS_T) __builtin_amdgcn_s_sleep(8);
            const int fv = (col < 28 ? col : 0) * 4, fs = WS_WF1 + ((unit >> 3) * 2 + ((unit >> 2) & 1)) * 128;
            wait_flags(rs, fv, fs, ld4(rs, fv, fs), tag);
            float c0 = 0.f, c1 = 0.f;
#pragma unroll 1
            for (int k0 = 0; k0 < 28; k0 += 14) {
                v4u w[14];
#pragma unroll
                for (int ks = 0; ks < 14; ks++) w[ks] = ld16(rs, l16 + (ks & 3) * 1024, WS_W1T + (unit * 28 + k0 + (ks & ~3)) * 1024);
#pragma unroll
                for (int ks = 0; ks < 14; ks++) {
                    c0 = dot2bf(w[ks][0], 0x3f803f80u, c0); c1 = dot2bf(w[ks][1], 0x3f803f80u, c1);
                    c0 = dot2bf(w[ks][2], 0x3f803f80u, c0); c1 = dot2bf(w[ks][3], 0x3f803f80u, c1);
                }
            }
            st8(rs, lane < 32 ? WS_CS + lane * 8 : OOB, unit * 256, v2u{__builtin_bit_cast(u32, half_sum(c0 + c1)), tag});
        }
        STAMP(3);
        STAMP_FLUSH();
        return;
    }
    asm volatile("" :: "v"(r_are), "v"(r_aim), "v"(r_bre), "v"(r_bim), "v"(r_cre), "v"(r_cim), "v"(r_dsk), "v"(cpf));
    SB();

    float unscale;
    {
        const float dt = rbf(__expf(ch ? bfhi(r_ldt) : bflo(r_ldt)));
        const float ar = dt * bflo(r_are), ai = dt * bflo(r_aim);
        const float br = dt * bflo(r_bre), bi = dt * bflo(r_bim);
        const float cr = bflo(r_cre), ci = bflo(r_cim);
        cf cb = {cr * br - ci * bi, -(cr * bi + ci * br)};
        const float a2 = ar * 1.44269504088896340736f;
        const float rh = ai * 0.15915494309189535f;
        const float rl = __builtin_fmaf(ai, 0.15915494309189535f, -rh) + ai * 6.4206382e-9f;
        auto cexp = [&](float s) __attribute__((always_inline)) -> cf {
            const float e = __builtin_amdgcn_exp2f(a2 * s);
            const float r = __builtin_amdgcn_fractf(rh * s) + rl * s;
            return cf{__builtin_amdgcn_cosf(r), __builtin_amdgcn_sinf(r)} * e;
        };
        const float grow = fmaxf(1.f, __builtin_amdgcn_exp2f(a2 * 2048.f));
        const float sb = wave_sum((fabsf(cb.x) + fabsf(cb.y)) * grow);
        *(LAS float*)(sm + LD_SB + wave * 4) = sb;
        int ex = (int)((__builtin_bit_cast(u32, sb) >> 23) & 255);
        ex = min(max(ex, 30), 230);
        unscale = __builtin_bit_cast(float, (u32)(127 - 15 - 126 + ex) << 23);
        cb *= __builtin_bit_cast(float, (u32)(127 + 15 + 126 - ex) << 23);

        const cf e1 = cexp(1.f), e16 = cexp(16.f), e256 = cexp(256.f);
        const cf e2 = cmul(e1, e1), e32 = cmul(e16, e16), e512 = cmul(e256, e256), x1 = cmulc(cb, e1), x256 = cmulc(cb, e256);
        SB();
        const cf e4 = cmul(e2, e2), e64 = cmul(e32, e32), e1024 = cmul(e512, e512), x2 = cmulc(cb, e2), x3 = cmulc(x1, e2);
        const cf x512 = cmulc(cb, e512), x768 = cmulc(x256, e512), y6 = cmul(e16, e32);
        SB();
        const cf e8 = cmul(e4, e4), e128 = cmul(e64, e64);
        const cf x4 = cmulc(cb, e4), x5 = cmulc(x1, e4), x6 = cmulc(x2, e4), x7 = cmulc(x3, e4), x258 = cmulc(x2, e256);
        const cf x1024 = cmulc(cb, e1024), x1280 = cmulc(x256, e1024), x1536 = cmulc(x512, e1024), x1792 = cmulc(x768, e1024);
        SB();
        auto put = [&](int q, cf v) __attribute__((always_inline)) { *(LAS u32*)(xyw + q * XY_STRIDE) = pkh(v); };
        cf y[8];
        y[0] = cf{1.f, 0.f}; y[1] = e8; y[2] = e16; y[3] = cmul(e8, e16); y[4] = e32; y[5] = cmul(e8, e32); y[6] = y6;
        const cf e192 = cmul(e128, e64), x8 = cmulc(x4, e4), x260 = cmulc(x4, e256), x262 = cmulc(x6, e256), x516 = cmulc(x4, e512);
        put(32, cb); put(33, x1); put(34, x2); put(35, x3); put(36, x4); put(37, x5); put(38, x6); put(39, x7);
        put(40, x256); put(41, x258);
        SB();
        y[7] = cmul(y[3], e32);
        const cf x264 = cmulc(x8, e256), x520 = cmulc(x8, e512), x772 = cmulc(x260, e512), x1032 = cmulc(x8, e1024);
        cf t[8];
#pragma unroll
        for (int q = 1; q < 7; q++) t[q] = cmul(y[q], e64);
        put(42, x260); put(43, x262); put(45, x512); put(46, x516);
#pragma unroll
        for (int q = 0; q < 6; q++) put(q, y[q]);
        SB();
        t[7] = cmul(y[7], e64);
        const cf x776 = cmulc(x264, e512), x1288 = cmulc(x264, e1024), x1544 = cmulc(x520, e1024);
        cf t2[8];
#pragma unroll
        for (int q = 1; q < 8; q++) t2[q] = cmul(y[q], e128);
        put(6, y[6]); put(7, y[7]); put(8, e64);
#pragma unroll
        for (int q = 1; q < 6; q++) put(8 + q, t[q]);
        put(44, x264); put(47, x520); put(48, x768); put(49, x772); put(51, x1024); put(52, x1032);
        SB();

        const cf x1800 = cmulc(x776, e1024);
        cf t3[8];
#pragma unroll
        for (int q = 1; q < 8; q++) t3[q] = cmul(y[q], e192);
        put(14, t[6]); put(15, t[7]); put(16, e128);
#pragma unroll
        for (int q = 1; q < 8; q++) put(16 + q, t2[q]);
        put(50, x776); put(53, x1280); put(54, x1288); put(55, x1536); put(56, x1544); put(57, x1792);
        SB();
        put(24, e192);
#pragma unroll
        for (int q = 1; q < 8; q++) put(24 + q, t3[q]);
        put(58, x1800);
    }
    SB();
    STAMP(2);
    asm volatile("" :: "v"(tin[7]));
    if (w1task >= 0) w1_task_store(rs, w1task, lane, tin);
    SB();
    const int t16 = tid * 16;
    v4u gx[2], gs[2];
    auto gr_load = [&]() __attribute__((always_inline)) {
        gx[0] = ld16(rs, t16, WS_XX + pair * 16384); gx[1] = ld16(rs, t16, WS_XX + pair * 16384 + 8192);
        gs[0] = ld16(rs, t16, WS_ST1); gs[1] = ld16(rs, t16, WS_ST1 + 8192);
    };
    const bool gr_first = (u32)__builtin_amdgcn_s_memrealtime() - tstart >= GR_T;
    if (gr_first) gr_load();
    SB();
    {
        f32x16 kd;
#pragma unroll
        for (int i2 = 0; i2 < 16; i2++) kd[i2] = 0.f;
        v4u fa[8], fb[8];
#pragma unroll
        for (int s = 0; s < 8; s++) { fa[s] = *(LAS v4u*)(pa + s * 32); fb[s] = *(LAS v4u*)(pb + s * 32); }
        SB();
#pragma unroll
        for (int s = 0; s < 8; s++) kd = mfma_f16(fa[s], fb[s], kd);
        SB();

#pragma unroll
        for (int g2 = 0; g2 < 4; g2++)
            *(LAS f32x4*)(exw + g2 * 1024) = f32x4{kd[4 * g2], kd[4 * g2 + 1], kd[4 * g2 + 2], kd[4 * g2 + 3]} * unscale;
    }
    if (!gr_first) {
        while ((u32)__builtin_amdgcn_s_memrealtime() - tstart < GR_T) __builtin_amdgcn_s_sleep(2);
        gr_load();
    }
    SB();
    if (w1task >= 0) { wait_vm<GR_EARLY ? 4 : 0>(); st4(rs, flv, w1_flag(w1task), tag); }
    BARRIER();
    __builtin_amdgcn_s_setprio(0);
    if (GR_EARLY == 0) gr_load();
    const int l28 = (col < 28 ? col : 0) * 4;
    SB();
    STAMP(3);
    float ik;
    {
        const f32x4 s0 = *(LAS f32x4*)(sm + LD_SB + ch * 16);
        const f32x4 a0 = *(LAS f32x4*)(exr), a1 = *(LAS f32x4*)(exr + XY_WAVE), a2 = *(LAS f32x4*)(exr + 2 * XY_WAVE), a3 = *(LAS f32x4*)(exr + 3 * XY_WAVE);
        const float b0 = (s0[0] + s0[1]) + (s0[2] + s0[3]);
        int e0 = (int)((__builtin_bit_cast(u32, b0) >> 23) & 255);
        e0 = min(max(e0, 30), 230);
        ik = __builtin_bit_cast(float, (u32)(127 - 15 - 126 + e0) << 23);
        const float sk = __builtin_bit_cast(float, (u32)(127 + 15 + 126 - e0) << 23);
        const f32x4 v = ((a0 + a1) + (a2 + a3)) * sk;
        const u32 p01 = pk_f16(v[0], v[1]), p23 = pk_f16(v[2], v[3]);
        LAS char* o = kpw;
        if (wq == 0) {
            *(LAS u16*)(o) = (u16)p01; *(LAS u16*)(o + 1536) = (u16)(p01 >> 16);
            *(LAS u16*)(o + 2 * 1536) = (u16)p23; *(LAS u16*)(o + 3 * 1536) = (u16)(p23 >> 16);
        } else if (wq == 1) {
            *(LAS u16*)(o) = (u16)p01; *(LAS u16*)(o + 1536) = (u16)p01;
            *(LAS u16*)(o + 2 * 1536) = (u16)(p01 >> 16); *(LAS u16*)(o + 3 * 1536) = (u16)p23;
        } else if (wq == 2) {
            *(LAS u32*)(o) = p01; *(LAS u32*)(o + 1536) = p01;
            *(LAS u32*)(o + 2 * 1536) = (p01 & 0xffffu) | (p23 << 16);
            *(LAS u32*)(o + 3 * 1536) = (p01 >> 16) | (p23 << 16);
        } else {
            const bool ev = (col & 1) == 0;
            *(LAS u32*)(o) = ev ? ((p01 & 0xffffu) | (p01 << 16)) : p01;
            *(LAS u32*)(o + 1536) = p01; *(LAS u32*)(o + 2 * 1536) = p01; *(LAS u32*)(o + 3 * 1536) = p01;
            *(LAS u32*)(o + 128) = ev ? ((p23 & 0xffffu) | (p23 << 16)) : p23;
            *(LAS u32*)(o + 128 + 1536) = p23; *(LAS u32*)(o + 128 + 2 * 1536) = p23; *(LAS u32*)(o + 128 + 3 * 1536) = p23;
        }
    }
    SB();
    auto gr_ok = [&]() __attribute__((always_inline)) {
        const u32 a = (gx[0][1] ^ tag) | (gx[0][3] ^ tag) | (gx[1][1] ^ tag) | (gx[1][3] ^ tag);
        const u32 c = (gs[0][1] ^ tag) | (gs[0][3] ^ tag) | (gs[1][1] ^ tag) | (gs[1][3] ^ tag);
        return __all((a | c) == 0);
    };
    if (!gr_ok()) {
        int spins = SPIN_CAP;
#pragma unroll 1
        do {
            __builtin_amdgcn_s_sleep(4);
            gr_load();
        } while (!gr_ok() && --spins > 0);
    }
    STAMP(4);
    v4u wf[28];
    const int w1off = WS_W1T + (phi * 8 + wave) * 28 * 1024;
    int seg7[4];
#pragma unroll
    for (int i = 0; i < 4; i++) seg7[i] = ((i + i28 * ROT_W1) & 3) * 7;
    auto w1_load = [&](int ks) __attribute__((always_inline)) {
        wf[ks] = ld16(rs, l16 + ((ks % 7) & 3) * 1024, w1off + seg7[ks / 7] * 1024 + ((ks % 7) >> 2) * 4096);
    };
    const int wf_s = WS_WF1 + phi * 256;
    const u32 wfl = ld4(rs, l28 + h * 128, wf_s);
    SB();
    u32 xc[4], udk[4];
    {
        float u0[4], u1[4];
#pragma unroll
        for (int i = 0; i < 4; i++) {
            xc[i] = gx[i >> 1][2 * (i & 1)];
            const u32 st = gs[i >> 1][2 * (i & 1)];
            const float m = bflo(st), r = __builtin_amdgcn_rcpf(bfhi(st));
            const u32 dd = pk_bf16(bflo(xc[i]) - m, bfhi(xc[i]) - m);
            const u32 uu = pk_bf16(bflo(dd) * r, bfhi(dd) * r);
            u0[i] = bflo(uu); u1[i] = bfhi(uu);
        }
        *(LAS v2u*)(uw) = v2u{pk_f16(u0[0], u0[1]), pk_f16(u0[2], u0[3])};
        *(LAS v2u*)(uw + 4160) = v2u{pk_f16(u1[0], u1[1]), pk_f16(u1[2], u1[3])};
#pragma unroll
        for (int i = 0; i < 4; i++) udk[i] = pk_bf16(u0[i] * bflo(r_dsk), u1[i] * bfhi(r_dsk));
    }
    BARRIER();
    STAMP(5);
    f32x4 r0, r1, l3;
    f32x2 r2, l2;
    float l1, r3;
    v4u xu;
    u32 ya, yb;
    const int c1 = tid & 255;
    LAS char* q1 = opaque(sm + ch * 4 * P_WAVE + ((c1 >> 3) + 1) * 144 + (c1 & 7) * 16);
    LAS char* q0 = opaque(sm + ch * 4 * P_WAVE + (((c1 - 1) >> 3) + 1) * 144 + ((c1 - 1) & 7) * 16);
    LAS char* xur = opaque(sm + LD_XU + ch * 8192 + c1 * 16);
    auto ce_read = [&](int hf) __attribute__((always_inline)) {
        const int pb_ = hf ? LD_PB : LD_PA;
        r0 = *(LAS f32x4*)(q1 + pb_);
        r1 = *(LAS f32x4*)(q1 + pb_ + P_WAVE); l1 = *(LAS float*)(q0 + pb_ + P_WAVE + 12);
        r2 = *(LAS f32x2*)(q1 + pb_ + 2 * P_WAVE); l2 = *(LAS f32x2*)(q0 + pb_ + 2 * P_WAVE + 8);
        r3 = *(LAS float*)(q1 + pb_ + 3 * P_WAVE); l3 = *(LAS f32x4*)(q0 + pb_ + 3 * P_WAVE);
        xu = *(LAS v4u*)(xur + hf * 4096);
    };
    auto ce_part = [&](int hf, int part) __attribute__((always_inline)) {
        if (part == 0) ya = pk_bf16s(((r0[0] + l2[0]) + (l1 + l3[1])) * ik, ((r0[1] + l2[1]) + (r1[0] + l3[2])) * ik);
        if (part == 1) yb = pk_bf16s(((r0[2] + r2[0]) + (r1[1] + l3[3])) * ik, ((r0[3] + r2[1]) + (r1[2] + r3)) * ik);
        if (part == 2) ya = pk_bf16s(bflo(ya) + bfhi(xu[0]), bfhi(ya) + bfhi(xu[1]));
        if (part == 3) yb = pk_bf16s(bflo(yb) + bfhi(xu[2]), bfhi(yb) + bfhi(xu[3]));
        if (part == 4) ya = pk_bf16s(bflo(ya) + bflo(xu[0]), bfhi(ya) + bflo(xu[1]));
        if (part == 5) {
            yb = pk_bf16s(bflo(yb) + bflo(xu[2]), bfhi(yb) + bflo(xu[3]));
            st8(rs, WS_HT + c1 * 8, (d0 + ch) * (L * 2) + hf * 2048, v2u{ya, yb});
        }
    };
    const int hf_set = WS_HF + (wq * 8 + (((d0 + ch) >> 3) & 7)) * 256 + (((d0 + ch) >> 6) * 8 + ((d0 + ch) & 7)) * 4;
    u32 pf1;
    {
        f32x16 acc0, acc1;
#pragma unroll
        for (int i = 0; i < 16; i++) { acc0[i] = 0.f; acc1[i] = 0.f; }
        constexpr int PD = 3;
        v4u fu[PD + 1], fk[16], fk1[PD + 1];
        auto issue = [&](int n) __attribute__((always_inline)) {
            const int B0 = (n >= 16 && n < 32) ? 2016 - 64 * (n - 16) : 992 - 64 * (n & 15);
            fu[n % (PD + 1)] = lds_tr2(ua + 2 * (B0 - 32) + 32, ua + 2 * (B0 - 32));
            if (n < 16) fk[n] = *(LAS v4u*)(ka + 32 * n);
            if (n >= 32) fk1[n % (PD + 1)] = *(LAS v4u*)(ka + 32 * (n - 16));
        };
#pragma unroll
        for (int n = 0; n < PD; n++) issue(n);
        SB();
        static_for<0, 48>([&](auto N_) __attribute__((always_inline)) {
            constexpr int n = decltype(N_)::v;
            if (n + PD < 48) issue(n + PD);
            if constexpr (n < 16) acc0 = mfma_f16(fu[n % (PD + 1)], fk[n], acc0);
            else if constexpr (n < 32) acc1 = mfma_f16(fu[n % (PD + 1)], fk[n - 16], acc1);
            else acc1 = mfma_f16(fu[n % (PD + 1)], fk1[n % (PD + 1)], acc1);
            if (n == 1) *(LAS v4u*)(sm + LD_XU + t16) = v4u{__builtin_amdgcn_perm(udk[0], xc[0], 0x05040100u), __builtin_amdgcn_perm(udk[1], xc[1], 0x05040100u),
                                                           __builtin_amdgcn_perm(udk[2], xc[2], 0x05040100u), __builtin_amdgcn_perm(udk[3], xc[3], 0x05040100u)};
            if (n == 2) *(LAS v4u*)(sm + LD_XU + 8192 + t16) = v4u{__builtin_amdgcn_perm(udk[0], xc[0], 0x07060302u), __builtin_amdgcn_perm(udk[1], xc[1], 0x07060302u),
                                                                  __builtin_amdgcn_perm(udk[2], xc[2], 0x07060302u), __builtin_amdgcn_perm(udk[3], xc[3], 0x07060302u)};
            if (n == W1_CHK) {
                PIN(acc0); SB();
                wait_flags(rs, l28 + h * 128, wf_s, wfl, tag);
                pf1 = PF1 ? ld4(rs, tid < 448 ? tid * 128 : OOB, WS_W1T + phi * 229376 + (i28 & 3) * 57344) : 0;
                STAMP(18);
            }
            if constexpr (n == 16 || n == 17) {
                constexpr int g = 2 * (n - 16);
                *(LAS f32x4*)(pw + LD_PA + g * 32) = f32x4{acc0[4 * g], acc0[4 * g + 1], acc0[4 * g + 2], acc0[4 * g + 3]};
                *(LAS f32x4*)(pw + LD_PA + g * 32 + 32) = f32x4{acc0[4 * g + 4], acc0[4 * g + 5], acc0[4 * g + 6], acc0[4 * g + 7]};
            }
            if (n == 17) {
                PIN(acc1);
                if (lane == 63) {
                    *(LAS f32x4*)(sm + LD_PA + wave * P_WAVE + 112) = f32x4{0.f, 0.f, 0.f, 0.f};
                    *(LAS f32x4*)(sm + LD_PB + wave * P_WAVE + 112) = f32x4{acc0[12], acc0[13], acc0[14], acc0[15]};
                }
            }
            if (n == 18) {
                BARRIER();
                if (PRIO_C) __builtin_amdgcn_s_setprio(1);
                STAMP(19);
                st4(rs, (b == 0 && tid == 0) ? WS_EPOCH : OOB, 0, tag);
                ce_read(0);
            }
            if (n >= 21 && n < 27) ce_part(0, n - 21);
            if (n == HF_N) {
                STAMP(22);
                wait_vm<w1_cnt(HF_N) - w1_cnt(26)>();
                st4(rs, flv, hf_set, tag);
                if (PRIO_C) __builtin_amdgcn_s_setprio(0);
                STAMP(23);
            }
            SB();
#pragma unroll
            for (int ks = w1_cnt(n); ks < w1_cnt(n + 1); ks++) w1_load(ks);
            SB();
        });
#pragma unroll
        for (int g = 0; g < 4; g++) *(LAS f32x4*)(pw + LD_PB + g * 32) = f32x4{acc1[4 * g], acc1[4 * g + 1], acc1[4 * g + 2], acc1[4 * g + 3]};
    }
    constexpr int W1_A = w1_cnt(48) + W1_MID > 28 ? 28 : w1_cnt(48) + W1_MID;
#pragma unroll
    for (int ks = w1_cnt(48); ks < W1_A; ks++) { w1_load(ks); SB(); }
    const int kap = phi * 8 + wave;
    const u32 w2fl = ld4(rs, l28, WS_WF2 + del * 128);
    v2u csg = __builtin_amdgcn_raw_buffer_load_b64(rs, WS_CS + col * 8, kap * 256, SC1);
    BARRIER();
    STAMP(6);
    ce_read(1);
    int rot14[14], rot7[7];
    {
        int i = (2 * del + ((i28 & 3) * 7 >> 1)) * ROT_W2;
        i = i >= 14 ? i - 14 : i;
#pragma unroll
        for (int n = 0; n < 14; n++) { rot14[n] = i * 1024; i = i == 13 ? 0 : i + 1; }
        i = del;
#pragma unroll
        for (int n = 0; n < 7; n++) { rot7[n] = i * 8192; i = i == 6 ? 0 : i + 1; }
    }
    auto load_h = [&](int hf, int n) __attribute__((always_inline)) {
        glds16(srd, hvoff + hf * 2048, rot7[n] * 32, (u32)(size_t)sm + (hf ? LD_HB : LD_HA) + wave * 1024 + rot7[n]);
    };
#pragma unroll
    for (int i = 0; i < 5; i++) ce_part(1, i);
    SB();
    u32 hfl = ld4(rs, hf_v, hf_s);
    SB();
    ce_part(1, 5);
    SB();
    STAMP(21);
#pragma unroll
    for (int ks = W1_A; ks < 28; ks++) { w1_load(ks); SB(); }
    wait_flags(rs, l28, WS_WF2 + del * 128, w2fl, tag);
    if (!__all(csg[1] == tag)) {
        int spins = SPIN_CAP;
#pragma unroll 1
        do {
            __builtin_amdgcn_s_sleep(4);
            csg = __builtin_amdgcn_raw_buffer_load_b64(rs, WS_CS + col * 8, kap * 256, SC1);
        } while (!__all(csg[1] == tag) && --spins > 0);
    }
    *(LAS u32*)(sm + LD_CS + wave * 128 + col * 4) = csg[0];
    wait_flags(rs, hf_v, hf_s, hfl, tag);
    STAMP(7);
#pragma unroll
    for (int n = 0; n < 7; n++) load_h(0, n);
    SB();
    wait_vm<7>();
    st4(rs, flv, hf_set + 8192, tag);
    SB();
    asm volatile("" :: "v"(pf1));
    wait_vm<1>();
    STAMP(8);
    BARRIER();
    STAMP(9);
    v4u w2[28];
    const int w2v = WS_W2T + (del * 2 * 112 + wave * 14) * 1024 + l16;
    auto w2_lds = [&](int f) __attribute__((always_inline)) { return f < 14 ? LD_PB + (wave * 4 + f - 10) * 1024 : LD_HA + (wave * 7 + f - 14) * 1024; };
    auto w2_load = [&](int f) __attribute__((always_inline)) {
        if (f >= 10 && f < 21) glds16(srd, w2v + (f & 1) * 112 * 1024, rot14[f >> 1], (u32)(size_t)sm + w2_lds(f));
        else w2[f] = ld16(rs, w2v + (f & 1) * 112 * 1024, rot14[f >> 1]);
    };
    const int af_v = (lane < 14 ? lane : 0) * 4, af_s = WS_AF + (tau * 16 + wave) * 128;
    const int av = WS_ACT + (tau2 * 112 + wave * 14) * 1024 + l16;
    const int dd = 64 * del + 8 * wave + 4 * h;
    LAS char* hra = sm + LD_HA + dd * 128 + trl + (((xcd & 1) << 6) ^ sw);
    v2u hr[2];
    u32 afl;
    v4u a0[14], a1[14];
    const int af_set = WS_AF + (tau * 16 + kap / 14) * 128 + (kap % 14) * 4;
    {
        LAS char* at0 = opaque(sm + LD_HA + 8 * h * 128 + trl + sw);
        LAS char* at1 = opaque(sm + LD_HA + 8 * h * 128 + trl + (64 ^ sw));
        LAS char* csr = opaque(sm + LD_CS + wave * 128 + h * 16);
        LAS char* as = opaque(sm + LD_HA + 8 * h * 128 + trl + (((wave & 1) << 6) ^ sw) + (wave >> 1) * 7 * 2048);
        LAS char* str = opaque(sm + LD_ST + col * 32);
        LAS char* cur;
        constexpr int PD = 4;
        v4u fr[PD + 1], sf, sf2;
        auto issue = [&](int n) __attribute__((always_inline)) {
            const int tile = n / 28, j = n % 28;
            if (j % 7 == 0) cur = opaque(((tile & 1) ? at1 : at0) + (seg7[j / 7] * 2048 + (tile >> 1) * 57344));
            LAS char* a = cur + (j % 7) * 2048;
            fr[n % (PD + 1)] = lds_tr2(a, a + 512);
        };
        f32x16 acc[2];
        float nm, k0, k1, sd2, eg, eu, ez, er, eo0;
        f32x4 cg4, cu4;
        f32x4 sta, stb;
        float s1, s2, s1b, s2b;
        u32 opk[4];
        auto stat_acc = [&](int k, int part, int which = 0) __attribute__((always_inline)) {
            const u32 f0 = which ? sf2[2 * part] : sf[2 * part], f1 = which ? sf2[2 * part + 1] : sf[2 * part + 1];
            if (k == 0 && part == 0) { s1 = 0.f; s2 = 0.f; s1b = 0.f; s2b = 0.f; }
            s1 = dot2bf(f0, 0x3f803f80u, s1); s2 = dot2bf(f0, f0, s2);
            s1b = dot2bf(f1, 0x3f803f80u, s1b); s2b = dot2bf(f1, f1, s2b);
        };
        auto stat_pin = [&]() __attribute__((always_inline)) { asm volatile("" : "+v"(s1), "+v"(s2), "+v"(s1b), "+v"(s2b)); };
        auto stat_write = [&](int hf) __attribute__((always_inline)) {
            const float a = half_sum(s1 + s1b), c = half_sum(s2 + s2b);
            *(LAS f32x2*)(str + (hf * 2 + (wave & 1)) * 1024 + (wave >> 1) * 8) = f32x2{a, c};
        };
        auto epi_read = [&](int tile) __attribute__((always_inline)) { sta = *(LAS f32x4*)(str + tile * 1024); stb = *(LAS f32x4*)(str + tile * 1024 + 16); };
        auto epi_stats = [&]() __attribute__((always_inline)) {
            float m = ((sta[0] + sta[2]) + (stb[0] + stb[2])) * (1.f / D), e2 = ((sta[1] + sta[3]) + (stb[1] + stb[3])) * (1.f / D);
            asm("" : "+v"(m)); asm("" : "+v"(e2));
            const float var = rbf(fmaxf(e2 - m * m, 0.f));
            const float sd = rbf(__builtin_amdgcn_sqrtf(rbf(var + 1e-5f))), r = __builtin_amdgcn_rcpf(sd);
            nm = -rbf(m); sd2 = sd * sd; k0 = r * -2.3022082f; k1 = (r * r) * (r * -0.10294324f);
        };
        auto cs_read = [&](int g) __attribute__((always_inline)) { cg4 = *(LAS f32x4*)(csr + g * 32); cu4 = *(LAS f32x4*)(csr + 64 + g * 32); };
        auto slice = [&](const f32x16& a, int sidx) __attribute__((always_inline)) {
            const int e = sidx / 3, st = sidx % 3;
            if (st == 0) {
                if (e > 0) { float o = eu * er; asm("" : "+v"(o)); if ((e - 1) & 1) opk[(e - 1) >> 1] = pk_bf16(eo0, o); else eo0 = o; }
                if (e < 8) {
                    eg = __builtin_fmaf(nm, cg4[e & 3], a[e & 7]); eu = __builtin_fmaf(nm, cu4[e & 3], a[8 + (e & 7)]);
                    asm("" : "+v"(eg), "+v"(eu));
                }
            } else if (st == 1) { ez = __builtin_amdgcn_exp2f(eg * __builtin_fmaf(eg * eg, k1, k0)); asm("" : "+v"(ez)); }
            else { er = __builtin_amdgcn_rcpf(__builtin_fmaf(ez, sd2, sd2)); eu *= eg; asm("" : "+v"(er), "+v"(eu)); }
        };
        auto epi_four = [&](const f32x16& a, int g) __attribute__((always_inline)) {
            const f32x4 nm4 = {nm, nm, nm, nm}, sd4 = {sd2, sd2, sd2, sd2};
            const f32x4 gg = __builtin_elementwise_fma(nm4, cg4, f32x4{a[4 * g], a[4 * g + 1], a[4 * g + 2], a[4 * g + 3]});
            const f32x4 u = __builtin_elementwise_fma(nm4, cu4, f32x4{a[8 + 4 * g], a[9 + 4 * g], a[10 + 4 * g], a[11 + 4 * g]});
            const f32x4 z = gg * __builtin_elementwise_fma(gg * gg, f32x4{k1, k1, k1, k1}, f32x4{k0, k0, k0, k0});
            const f32x4 e = {__builtin_amdgcn_exp2f(z.x), __builtin_amdgcn_exp2f(z.y), __builtin_amdgcn_exp2f(z.z), __builtin_amdgcn_exp2f(z.w)};
            const f32x4 dn = __builtin_elementwise_fma(e, sd4, sd4);
            const f32x4 o = (gg * u) * f32x4{__builtin_amdgcn_rcpf(dn.x), __builtin_amdgcn_rcpf(dn.y), __builtin_amdgcn_rcpf(dn.z), __builtin_amdgcn_rcpf(dn.w)};
            opk[2 * g] = pk_bf16(o.x, o.y); opk[2 * g + 1] = pk_bf16(o.z, o.w);
        };
        auto epi_store = [&](int tile) __attribute__((always_inline)) {
            const int tok32 = (tile >> 1) * 32 + tau * 2 + (tile & 1);
            st16(rs, WS_ACT + l16 + (tok32 * 112 + kap) * 1024, v4u{opk[0], opk[1], opk[2], opk[3]});
        };
#pragma unroll
        for (int n = 0; n < PD; n++) issue(n);
        SB();
        static_for<0, 112>([&](auto N_) __attribute__((always_inline)) {
            constexpr int n = decltype(N_)::v;
            constexpr int tile = n / 28, j = n % 28;
            if (n == 52) {
                STAMP(27);
                wait_vm<g1_cum(52) - g1_cum(HB_N + 8)>();
                BARRIER();
                as = opaque(as + 57344);
                STAMP(28);
            }
            if (n + PD < 112) issue(n + PD);
            if (j == 0) {
                f32x16 z;
#pragma unroll
                for (int i = 0; i < 16; i++) z[i] = 0.f;
                acc[tile & 1] = mfma_bf16(wf[0], fr[n % (PD + 1)], z);
            } else {
                acc[tile & 1] = mfma_bf16(wf[j], fr[n % (PD + 1)], acc[tile & 1]);
            }
            if constexpr (n < 28) {
                constexpr int sk = n;
                if (sk < 21 && sk % 3 == 0) { if (sk > 0) { stat_acc(sk / 3 - 1, 1); SB(); } sf = lds_tr2(as + (sk / 3) * 2048, as + (sk / 3) * 2048 + 512); }
                if (sk < 21 && sk % 3 == 2) stat_acc(sk / 3, 0);
                if (sk == 21) stat_acc(6, 1);
                if (sk == 22) stat_write(0);
            } else {
                constexpr int sk = n - 68;
                if (sk >= 2 && sk < 16) { stat_acc((sk - 2) / 2, sk & 1 ? 1 : 0, (sk / 2 - 1) & 1); SB(); }
                if (sk >= 0 && sk < 14 && (sk & 1) == 0) ((sk / 2) & 1 ? sf2 : sf) = lds_tr2(as + (sk / 2) * 2048, as + (sk / 2) * 2048 + 512);
                if (sk == 16) stat_write(1);
            }
            if (n == HBP_N) hfl = ld4(rs, hf_v, hf_s + 8192);
            if (n == HB_N) {
                PIN(acc[0]);
                SB();
                STAMP(24);
                wait_flags(rs, hf_v, hf_s + 8192, hfl, tag);
                STAMP(25);
            }
            if (n == 48) hr[0] = lds_tr(hra);
            if (n == 27 || n == 89) { STAMP(n == 27 ? 26 : 30); BARRIER_RAW(); STAMP(n == 27 ? 29 : 31); }
            if (n == 27 || n == 55) epi_read(tile);
            if (n == 89) epi_read(2);
            if (tile == 1) {
                if (j == 0) cs_read(0);
                if (j == 1) epi_stats();
                if (j >= 2 && j < 27) slice(acc[0], j - 2);
                if (j == 11) { SB(); cs_read(1); }
            }
            if (tile == 2) {
                if (j == 0) cs_read(0);
                if (j == 1) epi_stats();
                if (j >= 2 && j < 14) { slice(acc[1], 2 * (j - 2)); if (j == 6) { slice(acc[1], 9); SB(); cs_read(1); } else slice(acc[1], 2 * (j - 2) + 1); }
                if (j == 14) slice(acc[1], 24);
            }
            if (tile == 3) {
                if (j == 5) cs_read(0);
                if (j == 7) epi_stats();
                if (j >= 8 && j < 14) { slice(acc[0], 2 * (j - 8)); if (j == 12) { slice(acc[0], 9); SB(); cs_read(1); } else slice(acc[0], 2 * (j - 8) + 1); }
                if (j >= 14 && j < 27) slice(acc[0], j - 2);
            }
            constexpr int op = g1_op(n);
            if constexpr (op >= 0 && op < 28) w2_load(op);
            if constexpr (op >= 32 && op < 40) load_h(1, op - 32);
            if constexpr (op >= 40 && op < 50) epi_store(op - 40);
            if constexpr (op == 50) { wait_vm<g1_cum(AF_N) - g1_cum(72)>(); st4(rs, flv, af_set, tag); }
            if constexpr (op == 51) afl = ld4(rs, af_v, af_s);
            SB();
        });
        PIN(acc[1]);
        STAMP(10);
        wait_flags(rs, af_v, af_s, afl, tag);
        STAMP(11);
#pragma unroll
        for (int n = 0; n < 7; n++) { a0[n] = ld16(rs, av, rot14[n]); SB(); }
        epi_read(3); cs_read(0);
        epi_stats();
        epi_four(acc[1], 0);
        cs_read(1);
        epi_four(acc[1], 1);
        epi_store(3);
    }
    SB();
#pragma unroll
    for (int n = 7; n < 14; n++) { a0[n] = ld16(rs, av, rot14[n]); SB(); }
    hr[1] = lds_tr(hra + 57344);
#pragma unroll
    for (int f = 10; f < 21; f++) w2[f] = *(LAS v4u*)(sm + w2_lds(f) + l16);
    STAMP(12);
    BARRIER();
    f32x16 acc[2][2];
#pragma unroll
    for (int i = 0; i < 16; i++) { acc[0][0][i] = 0.f; acc[0][1][i] = 0.f; acc[1][0][i] = 0.f; acc[1][1][i] = 0.f; }
    LAS char* rw = opaque(sm + wave * 1024 + l16);
    LAS char* rr = opaque(sm + wave * 8192 + l16);
    auto red_write = [&](int hf) __attribute__((always_inline)) {
#pragma unroll
        for (int o = 0; o < 8; o++) {
            const f32x16& t = acc[o >> 2][hf];
            const int r = 4 * (o & 3);
            *(LAS f32x4*)(rw + hf * 65536 + o * 8192) = f32x4{t[r], t[r + 1], t[r + 2], t[r + 3]};
        }
    };
    auto red_finish = [&](int hf) __attribute__((always_inline)) {
        f32x4 v[8];
#pragma unroll
        for (int s = 0; s < 8; s++) v[s] = *(LAS f32x4*)(rr + hf * 65536 + s * 1024);
        const f32x4 t = ((v[0] + v[1]) + (v[2] + v[3])) + ((v[4] + v[5]) + (v[6] + v[7]));
        const u32 ta = pk_bf16(t[0], t[1]), tb = pk_bf16(t[2], t[3]);
        const int tok = 1024 * hf + 32 * tau2 + col;
        *(v2u*)(p.out + (size_t)tok * D + dd) = v2u{pk_bf16(bflo(ta) + bflo(hr[hf][0]), bfhi(ta) + bfhi(hr[hf][0])),
                                                    pk_bf16(bflo(tb) + bflo(hr[hf][1]), bfhi(tb) + bfhi(hr[hf][1]))};
    };
#pragma unroll
    for (int n = 0; n < 14; n++) {
        acc[0][0] = mfma_bf16(w2[2 * n], a0[n], acc[0][0]);
        acc[1][0] = mfma_bf16(w2[2 * n + 1], a0[n], acc[1][0]);
        if (n == (AFB_EARLY ? 1 : 6)) { wait_vm<7>(); st4(rs, flv, af_set + 1024, tag); }
        SB();
    }
    PIN(acc[0][0]); PIN(acc[1][0]);
    STAMP(13);
    red_write(0);
    BARRIER();
    afl = ld4(rs, af_v, af_s + 1024);
    SB();
    red_finish(0);
    SB();
    wait_flags(rs, af_v, af_s + 1024, afl, tag);
    STAMP(14);
#pragma unroll
    for (int n = 0; n < 14; n++) { a1[n] = ld16(rs, av + 32 * 112 * 1024, rot14[n]); SB(); }
    SB();
    STAMP(15);
#pragma unroll
    for (int n = 0; n < 14; n++) {
        acc[0][1] = mfma_bf16(w2[2 * n], a1[n], acc[0][1]);
        acc[1][1] = mfma_bf16(w2[2 * n + 1], a1[n], acc[1][1]);
        SB();
    }
    STAMP(16);
    red_write(1);
    BARRIER();
    red_finish(1);
    STAMP(17);
    STAMP_FLUSH();
}

}

extern "C" void kernel_launch(void* const* d_in, const int* in_sizes, int n_in,
                              void* d_out, int out_size, void* d_ws, size_t ws_size,
                              hipStream_t stream) {
    Params p;
    p.x = (const u16*)d_in[0]; p.Are = (const u16*)d_in[1]; p.Aim = (const u16*)d_in[2];
    p.Bre = (const u16*)d_in[3]; p.Bim = (const u16*)d_in[4]; p.Cre = (const u16*)d_in[5]; p.Cim = (const u16*)d_in[6];
    p.logdt = (const u16*)d_in[7]; p.Dskip = (const u16*)d_in[8];
    p.wg = (const u16*)d_in[10]; p.wu = (const u16*)d_in[11]; p.wd = (const u16*)d_in[12];
    p.ws = (char*)d_ws; p.out = (u16*)d_out;
    (void)hipFuncSetAttribute((const void*)fused_block, hipFuncAttributeMaxDynamicSharedMemorySize, LDS_BYTES);
    fused_block<<<256, 512, LDS_BYTES, stream>>>(p);
}
```

```cpp
#include <hip/hip_runtime.h>
#include <stdint.h>

namespace {

constexpr int L = 2048, D = 448, N = 256, F = 1792;

typedef unsigned short u16;
typedef unsigned int u32;
typedef unsigned long long u64;
typedef u32 v4u __attribute__((ext_vector_type(4)));
typedef u32 v2u __attribute__((ext_vector_type(2)));
typedef float f32x2 __attribute__((ext_vector_type(2)));
typedef float f32x4 __attribute__((ext_vector_type(4)));
typedef float f32x16 __attribute__((ext_vector_type(16)));
typedef _Float16 f16x2 __attribute__((ext_vector_type(2)));
typedef _Float16 f16x8 __attribute__((ext_vector_type(8)));
typedef __bf16 bf16x2 __attribute__((ext_vector_type(2)));
typedef __bf16 bf16x8 __attribute__((ext_vector_type(8)));
typedef short s16x4 __attribute__((ext_vector_type(4)));
#define GAS __attribute__((address_space(1)))
#define LAS __attribute__((address_space(3)))
#define RLX_AGENT __ATOMIC_RELAXED, __HIP_MEMORY_SCOPE_AGENT
#define SB() __builtin_amdgcn_sched_barrier(0)
#define PIN(x) asm volatile("" : "+v"(x))
#define BARRIER() asm volatile("s_waitcnt lgkmcnt(0)\n\ts_barrier" ::: "memory")
#define BARRIER_RAW() asm volatile("s_barrier" ::: "memory")
constexpr int SC1 = 16;

constexpr int WS_EPOCH = 0;
constexpr int WS_WF2 = 1024;
constexpr int WS_HF = 8192;
constexpr int WS_AF = WS_HF + 64 * 256;
constexpr int WS_WF1 = 65536;
constexpr int WS_CS = 65536 + 8192;
constexpr int WS_ST1 = WS_CS + 32768;
constexpr int WS_XX = WS_ST1 + 16384;
constexpr int WS_W1T = WS_XX + 224 * 16384;
constexpr int WS_W2T = WS_W1T + 112 * 28 * 1024;
constexpr int WS_HT = WS_W2T + 14 * 112 * 1024;
constexpr int WS_ACT = WS_HT + D * L * 2;
constexpr int WS_DBG = WS_ACT + 64 * 112 * 1024;
constexpr int NSTAMP = 32;
constexpr int WS_END = WS_DBG + 256 * NSTAMP * 8;
constexpr int SPIN_CAP = 1 << 16;
static_assert(WS_AF + 16 * 2 * 8 * 128 <= WS_WF1, "flags overlap");

#define STAMP(k) do {} while (0)
#define STAMP_FLUSH() do {} while (0)
#define STAMP_DECL() do {} while (0)

__device__ __forceinline__ float bflo(u32 v) { return __builtin_bit_cast(float, v << 16); }
__device__ __forceinline__ float bfhi(u32 v) { return __builtin_bit_cast(float, v & 0xffff0000u); }
__device__ __forceinline__ u32 pk_bf16(float a, float b) { f32x2 f = {a, b}; return __builtin_bit_cast(u32, __builtin_convertvector(f, bf16x2)); }
__device__ __forceinline__ u32 pk_bf16s(float a, float b) { asm("" : "+v"(a)); asm("" : "+v"(b)); return pk_bf16(a, b); }
__device__ __forceinline__ u32 pk_f16(float a, float b) { f32x2 f = {a, b}; return __builtin_bit_cast(u32, __builtin_convertvector(f, f16x2)); }
__device__ __forceinline__ float rbf(float a) { return bflo(pk_bf16(a, 0.f)); }
__device__ __forceinline__ float dot2bf(u32 a, u32 b, float c) {
    return __builtin_amdgcn_fdot2_f32_bf16(__builtin_bit_cast(bf16x2, a), __builtin_bit_cast(bf16x2, b), c, false);
}
__device__ __forceinline__ f32x16 mfma_bf16(v4u a, v4u b, f32x16 c) {
    return __builtin_amdgcn_mfma_f32_32x32x16_bf16(__builtin_bit_cast(bf16x8, a), __builtin_bit_cast(bf16x8, b), c, 0, 0, 0);
}
__device__ __forceinline__ f32x16 mfma_f16(v4u a, v4u b, f32x16 c) {
    return __builtin_amdgcn_mfma_f32_32x32x16_f16(__builtin_bit_cast(f16x8, a), __builtin_bit_cast(f16x8, b), c, 0, 0, 0);
}
__device__ __forceinline__ v2u lds_tr(LAS char* p) {
    return __builtin_bit_cast(v2u, __builtin_amdgcn_ds_read_tr16_b64_v4i16((LAS s16x4*)p));
}
__device__ __forceinline__ v4u lds_tr2(LAS char* p0, LAS char* p1) {
    const v2u lo = lds_tr(p0), hi = lds_tr(p1);
    return v4u{lo[0], lo[1], hi[0], hi[1]};
}
template <int CTRL> __device__ __forceinline__ float dpp(float v) {
    return __builtin_bit_cast(float, __builtin_amdgcn_update_dpp(0, __builtin_bit_cast(int, v), CTRL, 0xf, 0xf, true));
}
__device__ __forceinline__ float rdl(float v, int l) { return __builtin_bit_cast(float, __builtin_amdgcn_readlane(__builtin_bit_cast(int, v), l)); }
__device__ __forceinline__ float wave_sum(float v) {
    v += dpp<0xB1>(v); v += dpp<0x4E>(v); v += dpp<0x141>(v); v += dpp<0x140>(v);
    return (rdl(v, 0) + rdl(v, 16)) + (rdl(v, 32) + rdl(v, 48));
}
__device__ __forceinline__ float wave_max(float v) {
    v = fmaxf(v, dpp<0xB1>(v)); v = fmaxf(v, dpp<0x4E>(v)); v = fmaxf(v, dpp<0x141>(v)); v = fmaxf(v, dpp<0x140>(v));
    return fmaxf(fmaxf(rdl(v, 0), rdl(v, 16)), fmaxf(rdl(v, 32), rdl(v, 48)));
}
__device__ __forceinline__ float half_sum(float v) {
    const auto r = __builtin_amdgcn_permlane32_swap(__builtin_bit_cast(u32, v), __builtin_bit_cast(u32, v), false, false);
    return __builtin_bit_cast(float, (u32)r[0]) + __builtin_bit_cast(float, (u32)r[1]);
}
template <int I> struct IC { static constexpr int v = I; };
template <int B, int E, class Fn> __device__ __forceinline__ void static_for(Fn&& f) {
    if constexpr (B < E) { f(IC<B>{}); static_for<B + 1, E>(f); }
}

typedef f32x2 cf;
__device__ __forceinline__ cf rot(cf b) { return cf{-b.y, b.x}; }
__device__ __forceinline__ cf cmul(cf a, cf b) { return __builtin_elementwise_fma(a.yy, rot(b), a.xx * b); }
__device__ __forceinline__ cf cmulc(cf a, cf b) { return __builtin_elementwise_fma(a.yy, b.yx, a.xx * rot(b).yx); }
__device__ __forceinline__ u32 pkh(cf a) { return __builtin_bit_cast(u32, __builtin_convertvector(a, f16x2)); }

typedef __amdgpu_buffer_rsrc_t rsrc_t;
__device__ __forceinline__ v4u ld16(rsrc_t r, int voff, int soff) { return __builtin_amdgcn_raw_buffer_load_b128(r, voff, soff, SC1); }
__device__ __forceinline__ u32 ld4(rsrc_t r, int voff, int soff) { return __builtin_amdgcn_raw_buffer_load_b32(r, voff, soff, SC1); }
__device__ __forceinline__ void st16(rsrc_t r, int voff, v4u v) { __builtin_amdgcn_raw_buffer_store_b128(v, r, voff, 0, SC1); }
__device__ __forceinline__ void st8(rsrc_t r, int voff, int soff, v2u v) { __builtin_amdgcn_raw_buffer_store_b64(v, r, voff, soff, SC1); }
__device__ __forceinline__ void st4(rsrc_t r, int voff, int soff, u32 v) { __builtin_amdgcn_raw_buffer_store_b32(v, r, voff, soff, SC1); }
template <int N> __device__ __forceinline__ void wait_vm() { asm volatile("s_waitcnt vmcnt(%0)" :: "i"(N) : "memory"); }
constexpr int OOB = 0x7f000000;

__device__ __forceinline__ void wait_flags(rsrc_t rs, int voff, int soff, u32 v, u32 tag) {
    if (!__all(v == tag)) {
        int spins = SPIN_CAP;
#pragma unroll 1
        do {
            __builtin_amdgcn_s_sleep(1);
            v = ld4(rs, voff, soff);
        } while (!__all(v == tag) && --spins > 0);
    }
    __builtin_amdgcn_fence(__ATOMIC_ACQUIRE, "wavefront");
}
__device__ __forceinline__ void glds16(v4u srd, int voff, int soff, u32 lds_dst) {
    asm volatile("s_mov_b32 m0, %3\n\ts_nop 0\n\tbuffer_load_dwordx4 %0, %1, %2 offen sc1 lds"
                 :: "v"(voff), "s"(srd), "s"(soff), "s"(lds_dst) : "memory");
}
__device__ __forceinline__ LAS char* opaque(LAS char* q) { u32 a = (u32)(size_t)q; asm volatile("" : "+v"(a)); return (LAS char*)(size_t)a; }

struct Params {
    const u16 *x, *Are, *Aim, *Bre, *Bim, *Cre, *Cim, *logdt, *Dskip, *wg, *wu, *wd;
    char* ws;
    u16* out;
};

constexpr int XY_STRIDE = 272, XY_ROWS = 59, XY_WAVE = XY_STRIDE * XY_ROWS;
constexpr int P_WAVE = 33 * 144;
constexpr int LD_PB = 0, LD_PA = 38144;
constexpr int LD_HA = 38144, LD_HB = LD_HA + 57344;
constexpr int LD_XU = LD_HB;
constexpr int LD_KP = 128512;
constexpr int LD_U = LD_KP + 8 * 1536;
constexpr int LD_SB = LD_HB + 57344;
constexpr int LD_ST = LD_SB + 64;
constexpr int LD_CS = LD_ST + 4096;
constexpr int LDS_BYTES = LD_CS + 1024;
static_assert(LD_U + 2 * 4160 <= LD_SB && LDS_BYTES <= 163840, "lds map");

__device__ __forceinline__ v4u tr_piece(const v4u (&in)[8], int c) {
    v4u o;
#pragma unroll
    for (int kp = 0; kp < 4; kp++)
        o[kp] = __builtin_amdgcn_perm(in[2 * kp + 1][c >> 1], in[2 * kp][c >> 1], (c & 1) ? 0x07060302u : 0x05040100u);
    return o;
}
__device__ __forceinline__ void w1_task_load(const Params& p, int task, int lane, v2u (&in)[8]) {
    const int t = (task < 0 ? 0 : task) >> 1, c4 = task & 1;
    const int phi = t / 28, r = t - phi * 28, mat = r >= 14, kp = r - 14 * mat;
    const rsrc_t rw = __builtin_amdgcn_make_buffer_rsrc((void*)(mat ? p.wu : p.wg), 0, task < 0 ? 0 : D * F * 2, 0x00020000);
    const int voff = (8 * (lane >> 4) * F + (lane & 15) * 8) * 2;
#pragma unroll
    for (int k = 0; k < 8; k++) in[k] = __builtin_amdgcn_raw_buffer_load_b64(rw, voff + (k & 1) * F * 2, ((32 * kp + (k & 6)) * F + phi * 128 + 4 * c4) * 2, 0);
}
__device__ __forceinline__ void w1_task_store(rsrc_t rs, int task, int lane, const v2u (&in)[8]) {
    const int t = task >> 1, c4 = task & 1;
    const int phi = t / 28, r = t - phi * 28, mat = r >= 14, kp = r - 14 * mat;
    const int voff = WS_W1T + ((phi * 8 + 4 * c4) * 28 + 2 * kp) * 1024 + mat * 256 + (lane >> 4) * 512 + (lane & 15) * 16;
#pragma unroll
    for (int c = 0; c < 4; c++) {
        v4u o;
#pragma unroll
        for (int kp2 = 0; kp2 < 4; kp2++) o[kp2] = __builtin_amdgcn_perm(in[2 * kp2 + 1][c >> 1], in[2 * kp2][c >> 1], (c & 1) ? 0x07060302u : 0x05040100u);
        st16(rs, voff + c * 28 * 1024, o);
    }
}
__device__ __forceinline__ int w1_flag(int task) { const int t = task >> 1, phi = t / 28; return WS_WF1 + (phi * 2 + (task & 1)) * 128 + (t - phi * 28) * 4; }
__device__ __forceinline__ void w2_task(const Params& p, rsrc_t rs, int task, int lane, LAS char* scr) {
    const int del = task / 28, kg = task - del * 28;
    const int k4 = (lane >> 3) & 3, ks = kg * 4 + k4, h = lane >> 5, dr = lane & 7;
    const u16* src = p.wd + (size_t)((ks >> 3) * 128 + 32 * h + (ks & 7)) * D + del * 64 + dr * 8;
    v4u in[8];
#pragma unroll
    for (int j = 0; j < 8; j++) in[j] = __builtin_nontemporal_load((const v4u*)(src + (size_t)(8 * ((j & 3) + 8 * (j >> 2))) * D));
    LAS char* w = scr + ((dr >> 2) * 4 + k4) * 1024 + (32 * h + 8 * (dr & 3)) * 16;
#pragma unroll
    for (int c = 0; c < 8; c++) *(LAS v4u*)(w + c * 16) = tr_piece(in, c);
    const int voff = WS_W2T + (del * 2 * 112 + kg * 4) * 1024 + lane * 16;
#pragma unroll
    for (int f = 0; f < 8; f++) st16(rs, voff + ((f >> 2) * 112 + (f & 3)) * 1024, *(LAS v4u*)(scr + f * 1024 + lane * 16));
}

#define W1_CHK 2
#define KPRE 32
#define W1_NUM 2
#define W1_DEN 5
#define W1_BURST 4
#define PF1 0
#define PF2 1
#define GR_EARLY 3
#define CS_T 520
#define GR_T 320
#define AFB_EARLY 1
#define W1_ORDER 1
#define PRIO_C 1
constexpr int w1_cnt(int n) { return n <= W1_CHK ? 0 : W1_BURST + ((n - W1_CHK - 1) * W1_NUM) / W1_DEN > 28 ? 28 : W1_BURST + ((n - W1_CHK - 1) * W1_NUM) / W1_DEN; }
#define PRIO_A 0
#define CS_FIRST 1
#define HBP_N 10
#define W2_N0 34
#define HB_N 24
#define ROT_W1 0
#define ROT_W2 1
#define AF_N 84
#define HF_N 41
#define AFP_N 98
constexpr int g1_op(int n) {
    return (n > HB_N && n <= HB_N + 7) ? 32 + (n - HB_N - 1)
         : n == 55 ? 40 : n == 71 ? 41 : n == 111 ? 42 : n == AF_N ? 50 : n == AFP_N ? 51
         : (n >= W2_N0 && n < W2_N0 + 42 && (n & 1) == 0) ? (n - W2_N0) / 2
         : (n >= 85 && n < 99 && (n & 1) == 1) ? 21 + (n - 85) / 2
         : -1;
}
constexpr int g1_cum(int n) { int c = 0; for (int i = 0; i < n; i++) c += g1_op(i) >= 0; return c; }
static_assert(HB_N >= 23 && HB_N < 28 && HB_N + 7 < W2_N0 && (W2_N0 & 1) == 0 && W2_N0 + 28 > 52 && W2_N0 + 42 <= AF_N && (AF_N & 1) == 0 && (AFP_N & 1) == 0 && AFP_N > AF_N && AFP_N < 108, "schedule");
#define W1_MID 6
#define CODE_PF 23552

__global__ __launch_bounds__(512, 2) void fused_block(Params p) {
    extern __shared__ __attribute__((aligned(16))) char smem_[];
    LAS char* sm = (LAS char*)smem_;
    const int tid = threadIdx.x, lane = tid & 63;
    const int wave = __builtin_amdgcn_readfirstlane(tid >> 6);
    const int b = blockIdx.x;
    STAMP_DECL();
    const bool main_blk = b < 224;
    const int xcd = b & 7, i28 = b >> 3;
    u64 pc0; u32 clen;
    asm volatile("s_getpc_b64 %0\n.Lfb_code_begin:\n\ts_mov_b32 %1, .Lfb_code_end-.Lfb_code_begin" : "=s"(pc0), "=s"(clen));
    const u32 cpf = __builtin_amdgcn_raw_buffer_load_b32(__builtin_amdgcn_make_buffer_rsrc((void*)pc0, 0, main_blk ? 0 : clen, 0x00020000),
                                                         lane < 16 ? lane * 64 : OOB, (wave * 4 + (i28 & 3)) * 1024, 0);
    const int l16 = lane * 16;
    const int rbase = 16 * (b >> 1) + 2 * (b & 1);
    int x2v = lane < 56 ? (lane & 3) * (4 * D * 2) + (lane >> 2) * 8 : OOB, xrv = lane < 56 ? l16 : OOB, t2 = tid * 2;
    int x2s = (rbase * D + 56 * wave) * 2, xrs = (rbase + 4 * (wave >> 1) + (wave & 1)) * D * 2;
    int pair = xcd * 28 + i28;
    asm volatile("" : "+v"(x2v), "+v"(xrv), "+v"(t2), "+s"(x2s), "+s"(xrs), "+s"(pair));
    SB();
    const rsrc_t rs = __builtin_amdgcn_make_buffer_rsrc(p.ws, 0, WS_END, 0x00020000);
    asm volatile("" :: "s"(p.x), "s"(p.wd), "s"(p.ws));
    if (PRIO_A && wave >= 4) __builtin_amdgcn_s_setprio(1);
    STAMP(0);
    const u32 tstart = GR_EARLY == 3 ? (u32)__builtin_amdgcn_s_memrealtime() : 0;
    const u32 tagv = ld4(rs, WS_EPOCH, 0);

    const rsrc_t rx = __builtin_amdgcn_make_buffer_rsrc((void*)p.x, 0, L * D * 2, 0x00020000);
    const v2u xa = __builtin_amdgcn_raw_buffer_load_b64(rx, x2v, x2s, 2);
    const v2u xb = __builtin_amdgcn_raw_buffer_load_b64(rx, x2v + D * 2, x2s, 2);
    const v4u xr = __builtin_amdgcn_raw_buffer_load_b128(rx, xrv, xrs, 2);
    SB();
    const int ch = wave >> 2, wq = wave & 3;
    const int d0 = 2 * pair;
    const int psz = main_blk ? D * N * 2 : 0;
    auto ldp = [&](const u16* q) __attribute__((always_inline)) -> u32 {
        return __builtin_amdgcn_raw_buffer_load_b16(__builtin_amdgcn_make_buffer_rsrc((void*)q, 0, psz, 0x00020000), t2, d0 * N * 2, 2);
    };
    const u32 r_ldt = __builtin_amdgcn_raw_buffer_load_b32(__builtin_amdgcn_make_buffer_rsrc((void*)p.logdt, 0, main_blk ? D * 2 : 0, 0x00020000), 0, d0 * 2, 0);
    const u32 r_are = ldp(p.Are), r_aim = ldp(p.Aim), r_bre = ldp(p.Bre), r_bim = ldp(p.Bim), r_cre = ldp(p.Cre), r_cim = ldp(p.Cim);
    const u32 r_dsk = __builtin_amdgcn_raw_buffer_load_b32(__builtin_amdgcn_make_buffer_rsrc((void*)p.Dskip, 0, main_blk ? D * 2 : 0, 0x00020000), 0, d0 * 2, 0);
    const int hw = (b - 224) + 32 * wave;
    const int w1task = !main_blk ? -1 : wave < 2 ? pair * 2 + wave : (wave < 4 && pair < 168) ? 448 + pair * 2 + wave - 2 : -1;
    SB();
    const int h = lane >> 5, col = lane & 31;
    const int G1 = (lane >> 4) & 1, q4 = (lane & 15) >> 2, pp = lane & 3;
    int xxv = lane < 56 ? WS_XX + ((28 * wave + 2 * (lane >> 2)) * 2 + (b & 1)) * 8192 + (4 * (b >> 1) + (lane & 3)) * 16 : OOB;
    int flv = lane == 0 ? 0 : OOB;
    asm volatile("" : "+v"(xxv), "+v"(flv));
    LAS char* xy = sm + wave * XY_WAVE;
    LAS char* xyw = opaque(xy + lane * 4);
    LAS char* pa = opaque(xy + (32 + (int)((0x1713100d0a080400ull >> (8 * (col >> 2))) & 255) + (col & 3)) * XY_STRIDE + h * 16);
    LAS char* pb = opaque(xy + col * XY_STRIDE + h * 16);
    LAS char* exw = opaque(xy + l16);
    LAS char* exr = opaque(sm + (ch * 4) * XY_WAVE + wq * 1024 + l16);
    LAS char* kpw = opaque(sm + LD_KP + ch * 4 * 1536 + 512 + (wq < 2 ? 64 * wq + 2 * col + h : wq == 2 ? 64 * (2 + h) + 2 * col : 64 * (4 + 2 * h) + 2 * col) * 2);
    LAS char* uw = opaque(sm + LD_U + 64 + tid * 8);
    LAS char* ua = opaque(sm + LD_U + ch * 4160 + 2 * (32 + 32 + 16 * G1 - 32 * h - 16 - 4 * q4 + 4 * pp));
    LAS char* ka = opaque(sm + LD_KP + wave * 1536 + 2 * (256 - 248 + 8 * col + 8 * h));
    LAS char* pw = opaque(sm + wave * P_WAVE + (col + 1) * 144 + h * 16);
    *(LAS v2u*)(sm + LD_KP + wave * 1536 + lane * 8) = v2u{0, 0};
    if (tid < 32) *(LAS u32*)(sm + LD_U + (tid >> 4) * 4160 + (tid & 15) * 4) = 0;
    SB();
    const u32 tag = __builtin_amdgcn_readfirstlane(tagv) + 1u;
    st16(rs, xxv, v4u{xa[0], tag, xb[0], tag});
    st16(rs, xxv + 16384, v4u{xa[1], tag, xb[1], tag});
    SB();
    {
        float a = 0.f, q = 0.f;
#pragma unroll
        for (int i = 0; i < 4; i++) { a = dot2bf(xr[i], 0x3f803f80u, a); q = dot2bf(xr[i], xr[i], q); }
        const float mean = wave_sum(a) * (1.f / D), ex2 = wave_sum(q) * (1.f / D);
        const float var = rbf(fmaxf(ex2 - mean * mean, 0.f));
        const float sd = __builtin_amdgcn_sqrtf(rbf(var + 1e-5f));
        st8(rs, flv, WS_ST1 + (((b & 1) * 512 + 4 * (b >> 1) + (wave >> 1)) * 2 + (wave & 1)) * 8, v2u{pk_bf16(mean, sd), tag});
    }
    SB();
    v2u tin[8];
    w1_task_load(p, w1task, lane, tin);
    SB();
    STAMP(1);
    const int tau = main_blk ? (xcd >> 1) * 4 + (i28 & 3) : (b & 15), phi = (xcd & 1) * 7 + (i28 >> 2);
    const int del = i28 >> 2, tau2 = 2 * tau + (xcd & 1);
    const int trl = q4 * 128 + 8 * (pp & 1) + 16 * (2 * G1 + (pp >> 1)), sw = (q4 & 2) << 5;
    const int hf_v = (lane < 56 ? lane : 0) * 4, hf_s = WS_HF + ((tau >> 2) * 8 + wave) * 256;
    const v4u srd = {(u32)(size_t)p.ws, (u32)((size_t)p.ws >> 32) & 0xffffu, (u32)WS_END, 0x00020000u};
    const int hvoff = WS_HT + ((8 * wave + (lane >> 3)) * L + tau * 64) * 2 + (((lane & 7) ^ (((lane >> 3) & 2) << 1)) << 4);
    if (!main_blk) {
        BARRIER_RAW();
        const int w2task = hw;
        if (w2task < 196) {
            w2_task(p, rs, w2task, lane, sm + LD_HB + wave * 8192);
            wait_vm<0>();
            st4(rs, flv, WS_WF2 + (w2task / 28) * 128 + (w2task % 28) * 4, tag);
        }
        asm volatile("" :: "v"(cpf), "v"(tin[7]));
        STAMP(2);
        const int unit = 255 - hw;
        if (unit < 112) {
            while ((u32)__builtin_amdgcn_s_memrealtime() - tstart < CS_T) __builtin_amdgcn_s_sleep(8);
            const int fv = (col < 28 ? col : 0) * 4, fs = WS_WF1 + ((unit >> 3) * 2 + ((unit >> 2) & 1)) * 128;
            wait_flags(rs, fv, fs, ld4(rs, fv, fs), tag);
            float c0 = 0.f, c1 = 0.f;
#pragma unroll 1
            for (int k0 = 0; k0 < 28; k0 += 14) {
                v4u w[14];
#pragma unroll
                for (int ks = 0; ks < 14; ks++) w[ks] = ld16(rs, l16 + (ks & 3) * 1024, WS_W1T + (unit * 28 + k0 + (ks & ~3)) * 1024);
#pragma unroll
                for (int ks = 0; ks < 14; ks++) {
                    c0 = dot2bf(w[ks][0], 0x3f803f80u, c0); c1 = dot2bf(w[ks][1], 0x3f803f80u, c1);
                    c0 = dot2bf(w[ks][2], 0x3f803f80u, c0); c1 = dot2bf(w[ks][3], 0x3f803f80u, c1);
                }
            }
            st8(rs, lane < 32 ? WS_CS + lane * 8 : OOB, unit * 256, v2u{__builtin_bit_cast(u32, half_sum(c0 + c1)), tag});
        }
        STAMP(3);
        STAMP_FLUSH();
        return;
    }
    asm volatile("" :: "v"(r_are), "v"(r_aim), "v"(r_bre), "v"(r_bim), "v"(r_cre), "v"(r_cim), "v"(r_dsk), "v"(cpf));
    SB();

    float unscale;
    {
        const float dt = rbf(__expf(ch ? bfhi(r_ldt) : bflo(r_ldt)));
        const float ar = dt * bflo(r_are), ai = dt * bflo(r_aim);
        const float br = dt * bflo(r_bre), bi = dt * bflo(r_bim);
        const float cr = bflo(r_cre), ci = bflo(r_cim);
        cf cb = {cr * br - ci * bi, -(cr * bi + ci * br)};
        const float a2 = ar * 1.44269504088896340736f;
        const float rh = ai * 0.15915494309189535f;
        const float rl = __builtin_fmaf(ai, 0.15915494309189535f, -rh) + ai * 6.4206382e-9f;
        auto cexp = [&](float s) __attribute__((always_inline)) -> cf {
            const float e = __builtin_amdgcn_exp2f(a2 * s);
            const float r = __builtin_amdgcn_fractf(rh * s) + rl * s;
            return cf{__builtin_amdgcn_cosf(r), __builtin_amdgcn_sinf(r)} * e;
        };
        const float grow = fmaxf(1.f, __builtin_amdgcn_exp2f(a2 * 2048.f));
        const float sb = wave_sum((fabsf(cb.x) + fabsf(cb.y)) * grow);
        *(LAS float*)(sm + LD_SB + wave * 4) = sb;
        int ex = (int)((__builtin_bit_cast(u32, sb) >> 23) & 255);
        ex = min(max(ex, 30), 230);
        unscale = __builtin_bit_cast(float, (u32)(127 - 15 - 126 + ex) << 23);
        cb *= __builtin_bit_cast(float, (u32)(127 + 15 + 126 - ex) << 23);

        const cf e1 = cexp(1.f), e16 = cexp(16.f), e256 = cexp(256.f);
        const cf e2 = cmul(e1, e1), e32 = cmul(e16, e16), e512 = cmul(e256, e256), x1 = cmulc(cb, e1), x256 = cmulc(cb, e256);
        SB();
        const cf e4 = cmul(e2, e2), e64 = cmul(e32, e32), e1024 = cmul(e512, e512), x2 = cmulc(cb, e2), x3 = cmulc(x1, e2);
        const cf x512 = cmulc(cb, e512), x768 = cmulc(x256, e512), y6 = cmul(e16, e32);
        SB();
        const cf e8 = cmul(e4, e4), e128 = cmul(e64, e64);
        const cf x4 = cmulc(cb, e4), x5 = cmulc(x1, e4), x6 = cmulc(x2, e4), x7 = cmulc(x3, e4), x258 = cmulc(x2, e256);
        const cf x1024 = cmulc(cb, e1024), x1280 = cmulc(x256, e1024), x1536 = cmulc(x512, e1024), x1792 = cmulc(x768, e1024);
        SB();
        auto put = [&](int q, cf v) __attribute__((always_inline)) { *(LAS u32*)(xyw + q * XY_STRIDE) = pkh(v); };
        cf y[8];
        y[0] = cf{1.f, 0.f}; y[1] = e8; y[2] = e16; y[3] = cmul(e8, e16); y[4] = e32; y[5] = cmul(e8, e32); y[6] = y6;
        const cf e192 = cmul(e128, e64), x8 = cmulc(x4, e4), x260 = cmulc(x4, e256), x262 = cmulc(x6, e256), x516 = cmulc(x4, e512);
        put(32, cb); put(33, x1); put(34, x2); put(35, x3); put(36, x4); put(37, x5); put(38, x6); put(39, x7);
        put(40, x256); put(41, x258);
        SB();
        y[7] = cmul(y[3], e32);
        const cf x264 = cmulc(x8, e256), x520 = cmulc(x8, e512), x772 = cmulc(x260, e512), x1032 = cmulc(x8, e1024);
        cf t[8];
#pragma unroll
        for (int q = 1; q < 7; q++) t[q] = cmul(y[q], e64);
        put(42, x260); put(43, x262); put(45, x512); put(46, x516);
#pragma unroll
        for (int q = 0; q < 6; q++) put(q, y[q]);
        SB();
        t[7] = cmul(y[7], e64);
        const cf x776 = cmulc(x264, e512), x1288 = cmulc(x264, e1024), x1544 = cmulc(x520, e1024);
        cf t2[8];
#pragma unroll
        for (int q = 1; q < 8; q++) t2[q] = cmul(y[q], e128);
        put(6, y[6]); put(7, y[7]); put(8, e64);
#pragma unroll
        for (int q = 1; q < 6; q++) put(8 + q, t[q]);
        put(44, x264); put(47, x520); put(48, x768); put(49, x772); put(51, x1024); put(52, x1032);
        SB();

        const cf x1800 = cmulc(x776, e1024);
        cf t3[8];
#pragma unroll
        for (int q = 1; q < 8; q++) t3[q] = cmul(y[q], e192);
        put(14, t[6]); put(15, t[7]); put(16, e128);
#pragma unroll
        for (int q = 1; q < 8; q++) put(16 + q, t2[q]);
        put(50, x776); put(53, x1280); put(54, x1288); put(55, x1536); put(56, x1544); put(57, x1792);
        SB();
        put(24, e192);
#pragma unroll
        for (int q = 1; q < 8; q++) put(24 + q, t3[q]);
        put(58, x1800);
    }
    SB();
    STAMP(2);
    asm volatile("" :: "v"(tin[7]));
    if (w1task >= 0) w1_task_store(rs, w1task, lane, tin);
    SB();
    const int t16 = tid * 16;
    v4u gx[2], gs[2];
    auto gr_load = [&]() __attribute__((always_inline)) {
        gx[0] = ld16(rs, t16, WS_XX + pair * 16384); gx[1] = ld16(rs, t16, WS_XX + pair * 16384 + 8192);
        gs[0] = ld16(rs, t16, WS_ST1); gs[1] = ld16(rs, t16, WS_ST1 + 8192);
    };
    const bool gr_first = (u32)__builtin_amdgcn_s_memrealtime() - tstart >= GR_T;
    if (gr_first) gr_load();
    SB();
    {
        f32x16 kd;
#pragma unroll
        for (int i2 = 0; i2 < 16; i2++) kd[i2] = 0.f;
        v4u fa[8], fb[8];
#pragma unroll
        for (int s = 0; s < 8; s++) { fa[s] = *(LAS v4u*)(pa + s * 32); fb[s] = *(LAS v4u*)(pb + s * 32); }
        SB();
#pragma unroll
        for (int s = 0; s < 8; s++) kd = mfma_f16(fa[s], fb[s], kd);
        SB();

#pragma unroll
        for (int g2 = 0; g2 < 4; g2++)
            *(LAS f32x4*)(exw + g2 * 1024) = f32x4{kd[4 * g2], kd[4 * g2 + 1], kd[4 * g2 + 2], kd[4 * g2 + 3]} * unscale;
    }
    if (!gr_first) {
        while ((u32)__builtin_amdgcn_s_memrealtime() - tstart < GR_T) __builtin_amdgcn_s_sleep(2);
        gr_load();
    }
    SB();
    if (w1task >= 0) { wait_vm<GR_EARLY ? 4 : 0>(); st4(rs, flv, w1_flag(w1task), tag); }
    BARRIER();
    __builtin_amdgcn_s_setprio(0);
    if (GR_EARLY == 0) gr_load();
    const int l28 = (col < 28 ? col : 0) * 4;
    SB();
    STAMP(3);
    float ik;
    {
        const f32x4 s0 = *(LAS f32x4*)(sm + LD_SB + ch * 16);
        const f32x4 a0 = *(LAS f32x4*)(exr), a1 = *(LAS f32x4*)(exr + XY_WAVE), a2 = *(LAS f32x4*)(exr + 2 * XY_WAVE), a3 = *(LAS f32x4*)(exr + 3 * XY_WAVE);
        const float b0 = (s0[0] + s0[1]) + (s0[2] + s0[3]);
        int e0 = (int)((__builtin_bit_cast(u32, b0) >> 23) & 255);
        e0 = min(max(e0, 30), 230);
        ik = __builtin_bit_cast(float, (u32)(127 - 15 - 126 + e0) << 23);
        const float sk = __builtin_bit_cast(float, (u32)(127 + 15 + 126 - e0) << 23);
        const f32x4 v = ((a0 + a1) + (a2 + a3)) * sk;
        const u32 p01 = pk_f16(v[0], v[1]), p23 = pk_f16(v[2], v[3]);
        LAS char* o = kpw;
        if (wq == 0) {
            *(LAS u16*)(o) = (u16)p01; *(LAS u16*)(o + 1536) = (u16)(p01 >> 16);
            *(LAS u16*)(o + 2 * 1536) = (u16)p23; *(LAS u16*)(o + 3 * 1536) = (u16)(p23 >> 16);
        } else if (wq == 1) {
            *(LAS u16*)(o) = (u16)p01; *(LAS u16*)(o + 1536) = (u16)p01;
            *(LAS u16*)(o + 2 * 1536) = (u16)(p01 >> 16); *(LAS u16*)(o + 3 * 1536) = (u16)p23;
        } else if (wq == 2) {
            *(LAS u32*)(o) = p01; *(LAS u32*)(o + 1536) = p01;
            *(LAS u32*)(o + 2 * 1536) = (p01 & 0xffffu) | (p23 << 16);
            *(LAS u32*)(o + 3 * 1536) = (p01 >> 16) | (p23 << 16);
        } else {
            const bool ev = (col & 1) == 0;
            *(LAS u32*)(o) = ev ? ((p01 & 0xffffu) | (p01 << 16)) : p01;
            *(LAS u32*)(o + 1536) = p01; *(LAS u32*)(o + 2 * 1536) = p01; *(LAS u32*)(o + 3 * 1536) = p01;
            *(LAS u32*)(o + 128) = ev ? ((p23 & 0xffffu) | (p23 << 16)) : p23;
            *(LAS u32*)(o + 128 + 1536) = p23; *(LAS u32*)(o + 128 + 2 * 1536) = p23; *(LAS u32*)(o + 128 + 3 * 1536) = p23;
        }
    }
    SB();
    auto gr_ok = [&]() __attribute__((always_inline)) {
        const u32 a = (gx[0][1] ^ tag) | (gx[0][3] ^ tag) | (gx[1][1] ^ tag) | (gx[1][3] ^ tag);
        const u32 c = (gs[0][1] ^ tag) | (gs[0][3] ^ tag) | (gs[1][1] ^ tag) | (gs[1][3] ^ tag);
        return __all((a | c) == 0);
    };
    if (!gr_ok()) {
        int spins = SPIN_CAP;
#pragma unroll 1
        do {
            __builtin_amdgcn_s_sleep(4);
            gr_load();
        } while (!gr_ok() && --spins > 0);
    }
    STAMP(4);
    v4u wf[28];
    const int w1off = WS_W1T + (phi * 8 + wave) * 28 * 1024;
    int seg7[4];
#pragma unroll
    for (int i = 0; i < 4; i++) seg7[i] = ((i + i28 * ROT_W1) & 3) * 7;
    auto w1_load = [&](int ks) __attribute__((always_inline)) {
        wf[ks] = ld16(rs, l16 + ((ks % 7) & 3) * 1024, w1off + seg7[ks / 7] * 1024 + ((ks % 7) >> 2) * 4096);
    };
    const int wf_s = WS_WF1 + phi * 256;
    const u32 wfl = ld4(rs, l28 + h * 128, wf_s);
    SB();
    u32 xc[4], udk[4];
    {
        float u0[4], u1[4];
#pragma unroll
        for (int i = 0; i < 4; i++) {
            xc[i] = gx[i >> 1][2 * (i & 1)];
            const u32 st = gs[i >> 1][2 * (i & 1)];
            const float m = bflo(st), r = __builtin_amdgcn_rcpf(bfhi(st));
            const u32 dd = pk_bf16(bflo(xc[i]) - m, bfhi(xc[i]) - m);
            const u32 uu = pk_bf16(bflo(dd) * r, bfhi(dd) * r);
            u0[i] = bflo(uu); u1[i] = bfhi(uu);
        }
        *(LAS v2u*)(uw) = v2u{pk_f16(u0[0], u0[1]), pk_f16(u0[2], u0[3])};
        *(LAS v2u*)(uw + 4160) = v2u{pk_f16(u1[0], u1[1]), pk_f16(u1[2], u1[3])};
#pragma unroll
        for (int i = 0; i < 4; i++) udk[i] = pk_bf16(u0[i] * bflo(r_dsk), u1[i] * bfhi(r_dsk));
    }
    BARRIER();
    STAMP(5);
    f32x4 r0, r1, l3;
    f32x2 r2, l2;
    float l1, r3;
    v4u xu;
    u32 ya, yb;
    const int c1 = tid & 255;
    LAS char* q1 = opaque(sm + ch * 4 * P_WAVE + ((c1 >> 3) + 1) * 144 + (c1 & 7) * 16);
    LAS char* q0 = opaque(sm + ch * 4 * P_WAVE + (((c1 - 1) >> 3) + 1) * 144 + ((c1 - 1) & 7) * 16);
    LAS char* xur = opaque(sm + LD_XU + ch * 8192 + c1 * 16);
    auto ce_read = [&](int hf) __attribute__((always_inline)) {
        const int pb_ = hf ? LD_PB : LD_PA;
        r0 = *(LAS f32x4*)(q1 + pb_);
        r1 = *(LAS f32x4*)(q1 + pb_ + P_WAVE); l1 = *(LAS float*)(q0 + pb_ + P_WAVE + 12);
        r2 = *(LAS f32x2*)(q1 + pb_ + 2 * P_WAVE); l2 = *(LAS f32x2*)(q0 + pb_ + 2 * P_WAVE + 8);
        r3 = *(LAS float*)(q1 + pb_ + 3 * P_WAVE); l3 = *(LAS f32x4*)(q0 + pb_ + 3 * P_WAVE);
        xu = *(LAS v4u*)(xur + hf * 4096);
    };
    auto ce_part = [&](int hf, int part) __attribute__((always_inline)) {
        if (part == 0) ya = pk_bf16s(((r0[0] + l2[0]) + (l1 + l3[1])) * ik, ((r0[1] + l2[1]) + (r1[0] + l3[2])) * ik);
        if (part == 1) yb = pk_bf16s(((r0[2] + r2[0]) + (r1[1] + l3[3])) * ik, ((r0[3] + r2[1]) + (r1[2] + r3)) * ik);
        if (part == 2) ya = pk_bf16s(bflo(ya) + bfhi(xu[0]), bfhi(ya) + bfhi(xu[1]));
        if (part == 3) yb = pk_bf16s(bflo(yb) + bfhi(xu[2]), bfhi(yb) + bfhi(xu[3]));
        if (part == 4) ya = pk_bf16s(bflo(ya) + bflo(xu[0]), bfhi(ya) + bflo(xu[1]));
        if (part == 5) {
            yb = pk_bf16s(bflo(yb) + bflo(xu[2]), bfhi(yb) + bflo(xu[3]));
            st8(rs, WS_HT + c1 * 8, (d0 + ch) * (L * 2) + hf * 2048, v2u{ya, yb});
        }
    };
    const int hf_set = WS_HF + (wq * 8 + (((d0 + ch) >> 3) & 7)) * 256 + (((d0 + ch) >> 6) * 8 + ((d0 + ch) & 7)) * 4;
    u32 pf1;
    {
        f32x16 acc0, acc1;
#pragma unroll
        for (int i = 0; i < 16; i++) { acc0[i] = 0.f; acc1[i] = 0.f; }
        constexpr int PD = 3;
        v4u fu[PD + 1], fk[16], fk1[PD + 1];
        auto issue = [&](int n) __attribute__((always_inline)) {
            const int B0 = (n >= 16 && n < 32) ? 2016 - 64 * (n - 16) : 992 - 64 * (n & 15);
            fu[n % (PD + 1)] = lds_tr2(ua + 2 * (B0 - 32) + 32, ua + 2 * (B0 - 32));
            if (n < 16) fk[n] = *(LAS v4u*)(ka + 32 * n);
            if (n >= 32) fk1[n % (PD + 1)] = *(LAS v4u*)(ka + 32 * (n - 16));
        };
#pragma unroll
        for (int n = 0; n < PD; n++) issue(n);
        SB();
        static_for<0, 48>([&](auto N_) __attribute__((always_inline)) {
            constexpr int n = decltype(N_)::v;
            if (n + PD < 48) issue(n + PD);
            if constexpr (n < 16) acc0 = mfma_f16(fu[n % (PD + 1)], fk[n], acc0);
            else if constexpr (n < 32) acc1 = mfma_f16(fu[n % (PD + 1)], fk[n - 16], acc1);
            else acc1 = mfma_f16(fu[n % (PD + 1)], fk1[n % (PD + 1)], acc1);
            if (n == 1) *(LAS v4u*)(sm + LD_XU + t16) = v4u{__builtin_amdgcn_perm(udk[0], xc[0], 0x05040100u), __builtin_amdgcn_perm(udk[1], xc[1], 0x05040100u),
                                                           __builtin_amdgcn_perm(udk[2], xc[2], 0x05040100u), __builtin_amdgcn_perm(udk[3], xc[3], 0x05040100u)};
            if (n == 2) *(LAS v4u*)(sm + LD_XU + 8192 + t16) = v4u{__builtin_amdgcn_perm(udk[0], xc[0], 0x07060302u), __builtin_amdgcn_perm(udk[1], xc[1], 0x07060302u),
                                                                  __builtin_amdgcn_perm(udk[2], xc[2], 0x07060302u), __builtin_amdgcn_perm(udk[3], xc[3], 0x07060302u)};
            if (n == W1_CHK) {
                PIN(acc0); SB();
                wait_flags(rs, l28 + h * 128, wf_s, wfl, tag);
                pf1 = PF1 ? ld4(rs, tid < 448 ? tid * 128 : OOB, WS_W1T + phi * 229376 + (i28 & 3) * 57344) : 0;
                STAMP(18);
            }
            if constexpr (n == 16 || n == 17) {
                constexpr int g = 2 * (n - 16);
                *(LAS f32x4*)(pw + LD_PA + g * 32) = f32x4{acc0[4 * g], acc0[4 * g + 1], acc0[4 * g + 2], acc0[4 * g + 3]};
                *(LAS f32x4*)(pw + LD_PA + g * 32 + 32) = f32x4{acc0[4 * g + 4], acc0[4 * g + 5], acc0[4 * g + 6], acc0[4 * g + 7]};
            }
            if (n == 17) {
                PIN(acc1);
                if (lane == 63) {
                    *(LAS f32x4*)(sm + LD_PA + wave * P_WAVE + 112) = f32x4{0.f, 0.f, 0.f, 0.f};
                    *(LAS f32x4*)(sm + LD_PB + wave * P_WAVE + 112) = f32x4{acc0[12], acc0[13], acc0[14], acc0[15]};
                }
            }
            if (n == 18) {
                BARRIER();
                if (PRIO_C) __builtin_amdgcn_s_setprio(1);
                STAMP(19);
                st4(rs, (b == 0 && tid == 0) ? WS_EPOCH : OOB, 0, tag);
                ce_read(0);
            }
            if (n >= 21 && n < 27) ce_part(0, n - 21);
            if (n == HF_N) {
                STAMP(22);
                wait_vm<w1_cnt(HF_N) - w1_cnt(26)>();
                st4(rs, flv, hf_set, tag);
                if (PRIO_C) __builtin_amdgcn_s_setprio(0);
                STAMP(23);
            }
            SB();
#pragma unroll
            for (int ks = w1_cnt(n); ks < w1_cnt(n + 1); ks++) w1_load(ks);
            SB();
        });
#pragma unroll
        for (int g = 0; g < 4; g++) *(LAS f32x4*)(pw + LD_PB + g * 32) = f32x4{acc1[4 * g], acc1[4 * g + 1], acc1[4 * g + 2], acc1[4 * g + 3]};
    }
    constexpr int W1_A = w1_cnt(48) + W1_MID > 28 ? 28 : w1_cnt(48) + W1_MID;
#pragma unroll
    for (int ks = w1_cnt(48); ks < W1_A; ks++) { w1_load(ks); SB(); }
    const int kap = phi * 8 + wave;
    const u32 w2fl = ld4(rs, l28, WS_WF2 + del * 128);
    v2u csg = __builtin_amdgcn_raw_buffer_load_b64(rs, WS_CS + col * 8, kap * 256, SC1);
    BARRIER();
    STAMP(6);
    ce_read(1);
    int rot14[14], rot7[7];
    {
        int i = (2 * del + ((i28 & 3) * 7 >> 1)) * ROT_W2;
        i = i >= 14 ? i - 14 : i;
#pragma unroll
        for (int n = 0; n < 14; n++) { rot14[n] = i * 1024; i = i == 13 ? 0 : i + 1; }
        i = del;
#pragma unroll
        for (int n = 0; n < 7; n++) { rot7[n] = i * 8192; i = i == 6 ? 0 : i + 1; }
    }
    auto load_h = [&](int hf, int n) __attribute__((always_inline)) {
        glds16(srd, hvoff + hf * 2048, rot7[n] * 32, (u32)(size_t)sm + (hf ? LD_HB : LD_HA) + wave * 1024 + rot7[n]);
    };
#pragma unroll
    for (int i = 0; i < 5; i++) ce_part(1, i);
    SB();
    u32 hfl = ld4(rs, hf_v, hf_s);
    SB();
    ce_part(1, 5);
    SB();
    STAMP(21);
#pragma unroll
    for (int ks = W1_A; ks < 28; ks++) { w1_load(ks); SB(); }
    wait_flags(rs, l28, WS_WF2 + del * 128, w2fl, tag);
    if (!__all(csg[1] == tag)) {
        int spins = SPIN_CAP;
#pragma unroll 1
        do {
            __builtin_amdgcn_s_sleep(4);
            csg = __builtin_amdgcn_raw_buffer_load_b64(rs, WS_CS + col * 8, kap * 256, SC1);
        } while (!__all(csg[1] == tag) && --spins > 0);
    }
    *(LAS u32*)(sm + LD_CS + wave * 128 + col * 4) = csg[0];
    wait_flags(rs, hf_v, hf_s, hfl, tag);
    STAMP(7);
#pragma unroll
    for (int n = 0; n < 7; n++) load_h(0, n);
    SB();
    wait_vm<7>();
    st4(rs, flv, hf_set + 8192, tag);
    SB();
    asm volatile("" :: "v"(pf1));
    wait_vm<1>();
    STAMP(8);
    BARRIER();
    STAMP(9);
    v4u w2[28];
    const int w2v = WS_W2T + (del * 2 * 112 + wave * 14) * 1024 + l16;
    auto w2_lds = [&](int f) __attribute__((always_inline)) { return f < 14 ? LD_PB + (wave * 4 + f - 10) * 1024 : LD_HA + (wave * 7 + f - 14) * 1024; };
    auto w2_load = [&](int f) __attribute__((always_inline)) {
        if (f >= 10 && f < 21) glds16(srd, w2v + (f & 1) * 112 * 1024, rot14[f >> 1], (u32)(size_t)sm + w2_lds(f));
        else w2[f] = ld16(rs, w2v + (f & 1) * 112 * 1024, rot14[f >> 1]);
    };
    const int af_v = (lane < 14 ? lane : 0) * 4, af_s = WS_AF + (tau * 16 + wave) * 128;
    const int av = WS_ACT + (tau2 * 112 + wave * 14) * 1024 + l16;
    const int dd = 64 * del + 8 * wave + 4 * h;
    LAS char* hra = sm + LD_HA + dd * 128 + trl + (((xcd & 1) << 6) ^ sw);
    v2u hr[2];
    u32 afl;
    v4u a0[14], a1[14];
    const int af_set = WS_AF + (tau * 16 + kap / 14) * 128 + (kap % 14) * 4;
    {
        LAS char* at0 = opaque(sm + LD_HA + 8 * h * 128 + trl + sw);
        LAS char* at1 = opaque(sm + LD_HA + 8 * h * 128 + trl + (64 ^ sw));
        LAS char* csr = opaque(sm + LD_CS + wave * 128 + h * 16);
        LAS char* as = opaque(sm + LD_HA + 8 * h * 128 + trl + (((wave & 1) << 6) ^ sw) + (wave >> 1) * 7 * 2048);
        LAS char* str = opaque(sm + LD_ST + col * 32);
        LAS char* cur;
        constexpr int PD = 4;
        v4u fr[PD + 1], sf, sf2;
        auto issue = [&](int n) __attribute__((always_inline)) {
            const int tile = n / 28, j = n % 28;
            if (j % 7 == 0) cur = opaque(((tile & 1) ? at1 : at0) + (seg7[j / 7] * 2048 + (tile >> 1) * 57344));
            LAS char* a = cur + (j % 7) * 2048;
            fr[n % (PD + 1)] = lds_tr2(a, a + 512);
        };
        f32x16 acc[2];
        float nm, k0, k1, sd2, eg, eu, ez, er, eo0;
        f32x4 cg4, cu4;
        f32x4 sta, stb;
        float s1, s2, s1b, s2b;
        u32 opk[4];
        auto stat_acc = [&](int k, int part, int which = 0) __attribute__((always_inline)) {
            const u32 f0 = which ? sf2[2 * part] : sf[2 * part], f1 = which ? sf2[2 * part + 1] : sf[2 * part + 1];
            if (k == 0 && part == 0) { s1 = 0.f; s2 = 0.f; s1b = 0.f; s2b = 0.f; }
            s1 = dot2bf(f0, 0x3f803f80u, s1); s2 = dot2bf(f0, f0, s2);
            s1b = dot2bf(f1, 0x3f803f80u, s1b); s2b = dot2bf(f1, f1, s2b);
        };
        auto stat_pin = [&]() __attribute__((always_inline)) { asm volatile("" : "+v"(s1), "+v"(s2), "+v"(s1b), "+v"(s2b)); };
        auto stat_write = [&](int hf) __attribute__((always_inline)) {
            const float a = half_sum(s1 + s1b), c = half_sum(s2 + s2b);
            *(LAS f32x2*)(str + (hf * 2 + (wave & 1)) * 1024 + (wave >> 1) * 8) = f32x2{a, c};
        };
        auto epi_read = [&](int tile) __attribute__((always_inline)) { sta = *(LAS f32x4*)(str + tile * 1024); stb = *(LAS f32x4*)(str + tile * 1024 + 16); };
        auto epi_stats = [&]() __attribute__((always_inline)) {
            float m = ((sta[0] + sta[2]) + (stb[0] + stb[2])) * (1.f / D), e2 = ((sta[1] + sta[3]) + (stb[1] + stb[3])) * (1.f / D);
            asm("" : "+v"(m)); asm("" : "+v"(e2));
            const float var = rbf(fmaxf(e2 - m * m, 0.f));
            const float sd = rbf(__builtin_amdgcn_sqrtf(rbf(var + 1e-5f))), r = __builtin_amdgcn_rcpf(sd);
            nm = -rbf(m); sd2 = sd * sd; k0 = r * -2.3022082f; k1 = (r * r) * (r * -0.10294324f);
        };
        auto cs_read = [&](int g) __attribute__((always_inline)) { cg4 = *(LAS f32x4*)(csr + g * 32); cu4 = *(LAS f32x4*)(csr + 64 + g * 32); };
        auto slice = [&](const f32x16& a, int sidx) __attribute__((always_inline)) {
            const int e = sidx / 3, st = sidx % 3;
            if (st == 0) {
                if (e > 0) { float o = eu * er; asm("" : "+v"(o)); if ((e - 1) & 1) opk[(e - 1) >> 1] = pk_bf16(eo0, o); else eo0 = o; }
                if (e < 8) {
                    eg = __builtin_fmaf(nm, cg4[e & 3], a[e & 7]); eu = __builtin_fmaf(nm, cu4[e & 3], a[8 + (e & 7)]);
                    asm("" : "+v"(eg), "+v"(eu));
                }
            } else if (st == 1) { ez = __builtin_amdgcn_exp2f(eg * __builtin_fmaf(eg * eg, k1, k0)); asm("" : "+v"(ez)); }
            else { er = __builtin_amdgcn_rcpf(__builtin_fmaf(ez, sd2, sd2)); eu *= eg; asm("" : "+v"(er), "+v"(eu)); }
        };
        auto epi_four = [&](const f32x16& a, int g) __attribute__((always_inline)) {
            const f32x4 nm4 = {nm, nm, nm, nm}, sd4 = {sd2, sd2, sd2, sd2};
            const f32x4 gg = __builtin_elementwise_fma(nm4, cg4, f32x4{a[4 * g], a[4 * g + 1], a[4 * g + 2], a[4 * g + 3]});
            const f32x4 u = __builtin_elementwise_fma(nm4, cu4, f32x4{a[8 + 4 * g], a[9 + 4 * g], a[10 + 4 * g], a[11 + 4 * g]});
            const f32x4 z = gg * __builtin_elementwise_fma(gg * gg, f32x4{k1, k1, k1, k1}, f32x4{k0, k0, k0, k0});
            const f32x4 e = {__builtin_amdgcn_exp2f(z.x), __builtin_amdgcn_exp2f(z.y), __builtin_amdgcn_exp2f(z.z), __builtin_amdgcn_exp2f(z.w)};
            const f32x4 dn = __builtin_elementwise_fma(e, sd4, sd4);
            const f32x4 o = (gg * u) * f32x4{__builtin_amdgcn_rcpf(dn.x), __builtin_amdgcn_rcpf(dn.y), __builtin_amdgcn_rcpf(dn.z), __builtin_amdgcn_rcpf(dn.w)};
            opk[2 * g] = pk_bf16(o.x, o.y); opk[2 * g + 1] = pk_bf16(o.z, o.w);
        };
        auto epi_store = [&](int tile) __attribute__((always_inline)) {
            const int tok32 = (tile >> 1) * 32 + tau * 2 + (tile & 1);
            st16(rs, WS_ACT + l16 + (tok32 * 112 + kap) * 1024, v4u{opk[0], opk[1], opk[2], opk[3]});
        };
#pragma unroll
        for (int n = 0; n < PD; n++) issue(n);
        SB();
        static_for<0, 112>([&](auto N_) __attribute__((always_inline)) {
            constexpr int n = decltype(N_)::v;
            constexpr int tile = n / 28, j = n % 28;
            if (n == 52) {
                STAMP(27);
                wait_vm<g1_cum(52) - g1_cum(HB_N + 8)>();
                BARRIER();
                as = opaque(as + 57344);
                STAMP(28);
            }
            if (n + PD < 112) issue(n + PD);
            if (j == 0) {
                f32x16 z;
#pragma unroll
                for (int i = 0; i < 16; i++) z[i] = 0.f;
                acc[tile & 1] = mfma_bf16(wf[0], fr[n % (PD + 1)], z);
            } else {
                acc[tile & 1] = mfma_bf16(wf[j], fr[n % (PD + 1)], acc[tile & 1]);
            }
            if constexpr (n < 28) {
                constexpr int sk = n;
                if (sk < 21 && sk % 3 == 0) { if (sk > 0) { stat_acc(sk / 3 - 1, 1); SB(); } sf = lds_tr2(as + (sk / 3) * 2048, as + (sk / 3) * 2048 + 512); }
                if (sk < 21 && sk % 3 == 2) stat_acc(sk / 3, 0);
                if (sk == 21) stat_acc(6, 1);
                if (sk == 22) stat_write(0);
            } else {
                constexpr int sk = n - 68;
                if (sk >= 2 && sk < 16) { stat_acc((sk - 2) / 2, sk & 1 ? 1 : 0, (sk / 2 - 1) & 1); SB(); }
                if (sk >= 0 && sk < 14 && (sk & 1) == 0) ((sk / 2) & 1 ? sf2 : sf) = lds_tr2(as + (sk / 2) * 2048, as + (sk / 2) * 2048 + 512);
                if (sk == 16) stat_write(1);
            }
            if (n == HBP_N) hfl = ld4(rs, hf_v, hf_s + 8192);
            if (n == HB_N) {
                PIN(acc[0]);
                SB();
                STAMP(24);
                wait_flags(rs, hf_v, hf_s + 8192, hfl, tag);
                STAMP(25);
            }
            if (n == 48) hr[0] = lds_tr(hra);
            if (n == 27 || n == 89) { STAMP(n == 27 ? 26 : 30); BARRIER_RAW(); STAMP(n == 27 ? 29 : 31); }
            if (n == 27 || n == 55) epi_read(tile);
            if (n == 89) epi_read(2);
            if (tile == 1) {
                if (j == 0) cs_read(0);
                if (j == 1) epi_stats();
                if (j >= 2 && j < 27) slice(acc[0], j - 2);
                if (j == 11) { SB(); cs_read(1); }
            }
            if (tile == 2) {
                if (j == 0) cs_read(0);
                if (j == 1) epi_stats();
                if (j >= 2 && j < 14) { slice(acc[1], 2 * (j - 2)); if (j == 6) { slice(acc[1], 9); SB(); cs_read(1); } else slice(acc[1], 2 * (j - 2) + 1); }
                if (j == 14) slice(acc[1], 24);
            }
            if (tile == 3) {
                if (j == 5) cs_read(0);
                if (j == 7) epi_stats();
                if (j >= 8 && j < 14) { slice(acc[0], 2 * (j - 8)); if (j == 12) { slice(acc[0], 9); SB(); cs_read(1); } else slice(acc[0], 2 * (j - 8) + 1); }
                if (j >= 14 && j < 27) slice(acc[0], j - 2);
            }
            constexpr int op = g1_op(n);
            if constexpr (op >= 0 && op < 28) w2_load(op);
            if constexpr (op >= 32 && op < 40) load_h(1, op - 32);
            if constexpr (op >= 40 && op < 50) epi_store(op - 40);
            if constexpr (op == 50) { wait_vm<g1_cum(AF_N) - g1_cum(72)>(); st4(rs, flv, af_set, tag); }
            if constexpr (op == 51) afl = ld4(rs, af_v, af_s);
            SB();
        });
        PIN(acc[1]);
        STAMP(10);
        wait_flags(rs, af_v, af_s, afl, tag);
        STAMP(11);
#pragma unroll
        for (int n = 0; n < 7; n++) { a0[n] = ld16(rs, av, rot14[n]); SB(); }
        epi_read(3); cs_read(0);
        epi_stats();
        epi_four(acc[1], 0);
        cs_read(1);
        epi_four(acc[1], 1);
        epi_store(3);
    }
    SB();
#pragma unroll
    for (int n = 7; n < 14; n++) { a0[n] = ld16(rs, av, rot14[n]); SB(); }
    hr[1] = lds_tr(hra + 57344);
#pragma unroll
    for (int f = 10; f < 21; f++) w2[f] = *(LAS v4u*)(sm + w2_lds(f) + l16);
    STAMP(12);
    BARRIER();
    f32x16 acc[2][2];
#pragma unroll
    for (int i = 0; i < 16; i++) { acc[0][0][i] = 0.f; acc[0][1][i] = 0.f; acc[1][0][i] = 0.f; acc[1][1][i] = 0.f; }
    LAS char* rw = opaque(sm + wave * 1024 + l16);
    LAS char* rr = opaque(sm + wave * 8192 + l16);
    auto red_write = [&](int hf) __attribute__((always_inline)) {
#pragma unroll
        for (int o = 0; o < 8; o++) {
            const f32x16& t = acc[o >> 2][hf];
            const int r = 4 * (o & 3);
            *(LAS f32x4*)(rw + hf * 65536 + o * 8192) = f32x4{t[r], t[r + 1], t[r + 2], t[r + 3]};
        }
    };
    auto red_finish = [&](int hf) __attribute__((always_inline)) {
        f32x4 v[8];
#pragma unroll
        for (int s = 0; s < 8; s++) v[s] = *(LAS f32x4*)(rr + hf * 65536 + s * 1024);
        const f32x4 t = ((v[0] + v[1]) + (v[2] + v[3])) + ((v[4] + v[5]) + (v[6] + v[7]));
        const u32 ta = pk_bf16(t[0], t[1]), tb = pk_bf16(t[2], t[3]);
        const int tok = 1024 * hf + 32 * tau2 + col;
        *(v2u*)(p.out + (size_t)tok * D + dd) = v2u{pk_bf16(bflo(ta) + bflo(hr[hf][0]), bfhi(ta) + bfhi(hr[hf][0])),
                                                    pk_bf16(bflo(tb) + bflo(hr[hf][1]), bfhi(tb) + bfhi(hr[hf][1]))};
    };
#pragma unroll
    for (int n = 0; n < 14; n++) {
        acc[0][0] = mfma_bf16(w2[2 * n], a0[n], acc[0][0]);
        acc[1][0] = mfma_bf16(w2[2 * n + 1], a0[n], acc[1][0]);
        if (n == (AFB_EARLY ? 1 : 6)) { wait_vm<7>(); st4(rs, flv, af_set + 1024, tag); }
        SB();
    }
    PIN(acc[0][0]); PIN(acc[1][0]);
    STAMP(13);
    red_write(0);
    BARRIER();
    afl = ld4(rs, af_v, af_s + 1024);
    SB();
    red_finish(0);
    SB();
    wait_flags(rs, af_v, af_s + 1024, afl, tag);
    STAMP(14);
#pragma unroll
    for (int n = 0; n < 14; n++) { a1[n] = ld16(rs, av + 32 * 112 * 1024, rot14[n]); SB(); }
    SB();
    STAMP(15);
#pragma unroll
    for (int n = 0; n < 14; n++) {
        acc[0][1] = mfma_bf16(w2[2 * n], a1[n], acc[0][1]);
        acc[1][1] = mfma_bf16(w2[2 * n + 1], a1[n], acc[1][1]);
        SB();
    }
    STAMP(16);
    red_write(1);
    BARRIER();
    red_finish(1);
    STAMP(17);
    STAMP_FLUSH();
    asm volatile(".Lfb_code_end:");
}

}

extern "C" void kernel_launch(void* const* d_in, const int* in_sizes, int n_in,
                              void* d_out, int out_size, void* d_ws, size_t ws_size,
                              hipStream_t stream) {
    Params p;
    p.x = (const u16*)d_in[0]; p.Are = (const u16*)d_in[1]; p.Aim = (const u16*)d_in[2];
    p.Bre = (const u16*)d_in[3]; p.Bim = (const u16*)d_in[4]; p.Cre = (const u16*)d_in[5]; p.Cim = (const u16*)d_in[6];
    p.logdt = (const u16*)d_in[7]; p.Dskip = (const u16*)d_in[8];
    p.wg = (const u16*)d_in[10]; p.wu = (const u16*)d_in[11]; p.wd = (const u16*)d_in[12];
    p.ws = (char*)d_ws; p.out = (u16*)d_out;
    (void)hipFuncSetAttribute((const void*)fused_block, hipFuncAttributeMaxDynamicSharedMemorySize, LDS_BYTES);
    fused_block<<<256, 512, LDS_BYTES, stream>>>(p);
}
```

```cpp
#include <hip/hip_runtime.h>
#include <stdint.h>

namespace {

constexpr int L = 2048, D = 448, N = 256, F = 1792;

typedef unsigned short u16;
typedef unsigned int u32;
typedef unsigned long long u64;
typedef u32 v4u __attribute__((ext_vector_type(4)));
typedef u32 v2u __attribute__((ext_vector_type(2)));
typedef float f32x2 __attribute__((ext_vector_type(2)));
typedef float f32x4 __attribute__((ext_vector_type(4)));
typedef float f32x16 __attribute__((ext_vector_type(16)));
typedef _Float16 f16x2 __attribute__((ext_vector_type(2)));
typedef _Float16 f16x8 __attribute__((ext_vector_type(8)));
typedef __bf16 bf16x2 __attribute__((ext_vector_type(2)));
typedef __bf16 bf16x8 __attribute__((ext_vector_type(8)));
typedef short s16x4 __attribute__((ext_vector_type(4)));
#define GAS __attribute__((address_space(1)))
#define LAS __attribute__((address_space(3)))
#define RLX_AGENT __ATOMIC_RELAXED, __HIP_MEMORY_SCOPE_AGENT
#define SB() __builtin_amdgcn_sched_barrier(0)
#define PIN(x) asm volatile("" : "+v"(x))
#define BARRIER() asm volatile("s_waitcnt lgkmcnt(0)\n\ts_barrier" ::: "memory")
#define BARRIER_RAW() asm volatile("s_barrier" ::: "memory")
constexpr int SC1 = 16;

constexpr int WS_EPOCH = 0;
constexpr int WS_WF2 = 1024;
constexpr int WS_TR = 4096;
constexpr int WS_HF = 8192;
constexpr int WS_AF = WS_HF + 64 * 256;
constexpr int WS_SG = 57344;
constexpr int WS_WF1 = 65536;
constexpr int WS_CS = 65536 + 8192;
constexpr int WS_ST1 = WS_CS + 32768;
constexpr int WS_XX = WS_ST1 + 16384;
constexpr int WS_W1T = WS_XX + 224 * 16384;
constexpr int WS_W2T = WS_W1T + 112 * 28 * 1024;
constexpr int WS_HT = WS_W2T + 14 * 112 * 1024;
constexpr int WS_ACT = WS_HT + D * L * 2;
constexpr int WS_DBG = WS_ACT + 64 * 112 * 1024;
constexpr int NSTAMP = 32;
constexpr int WS_END = WS_DBG + 256 * NSTAMP * 8;
constexpr int SPIN_CAP = 1 << 16;
static_assert(WS_AF + 16 * 2 * 8 * 128 <= WS_SG, "flags overlap");

#define STAMP(k) do { if ((k) == 0) { u64 t_; asm volatile("s_memrealtime %0\n\ts_waitcnt lgkmcnt(0)" : "=s"(t_) :: "memory"); stv[0] = (u32)t_; } } while (0)
#define STAMP_FLUSH() do { if (main_blk && xcd == 1) while ((u32)__builtin_amdgcn_s_memrealtime() - stv[0] < 3000) __builtin_amdgcn_s_sleep(1); } while (0)
#define STAMP_DECL() u32 stv[2]

__device__ __forceinline__ float bflo(u32 v) { return __builtin_bit_cast(float, v << 16); }
__device__ __forceinline__ float bfhi(u32 v) { return __builtin_bit_cast(float, v & 0xffff0000u); }
__device__ __forceinline__ u32 pk_bf16(float a, float b) { f32x2 f = {a, b}; return __builtin_bit_cast(u32, __builtin_convertvector(f, bf16x2)); }
__device__ __forceinline__ u32 pk_bf16s(float a, float b) { asm("" : "+v"(a)); asm("" : "+v"(b)); return pk_bf16(a, b); }
__device__ __forceinline__ u32 pk_f16(float a, float b) { f32x2 f = {a, b}; return __builtin_bit_cast(u32, __builtin_convertvector(f, f16x2)); }
__device__ __forceinline__ float rbf(float a) { return bflo(pk_bf16(a, 0.f)); }
__device__ __forceinline__ float dot2bf(u32 a, u32 b, float c) {
    return __builtin_amdgcn_fdot2_f32_bf16(__builtin_bit_cast(bf16x2, a), __builtin_bit_cast(bf16x2, b), c, false);
}
__device__ __forceinline__ f32x16 mfma_bf16(v4u a, v4u b, f32x16 c) {
    return __builtin_amdgcn_mfma_f32_32x32x16_bf16(__builtin_bit_cast(bf16x8, a), __builtin_bit_cast(bf16x8, b), c, 0, 0, 0);
}
__device__ __forceinline__ f32x16 mfma_f16(v4u a, v4u b, f32x16 c) {
    return __builtin_amdgcn_mfma_f32_32x32x16_f16(__builtin_bit_cast(f16x8, a), __builtin_bit_cast(f16x8, b), c, 0, 0, 0);
}
__device__ __forceinline__ v2u lds_tr(LAS char* p) {
    return __builtin_bit_cast(v2u, __builtin_amdgcn_ds_read_tr16_b64_v4i16((LAS s16x4*)p));
}
__device__ __forceinline__ v4u lds_tr2(LAS char* p0, LAS char* p1) {
    const v2u lo = lds_tr(p0), hi = lds_tr(p1);
    return v4u{lo[0], lo[1], hi[0], hi[1]};
}
template <int CTRL> __device__ __forceinline__ float dpp(float v) {
    return __builtin_bit_cast(float, __builtin_amdgcn_update_dpp(0, __builtin_bit_cast(int, v), CTRL, 0xf, 0xf, true));
}
__device__ __forceinline__ float rdl(float v, int l) { return __builtin_bit_cast(float, __builtin_amdgcn_readlane(__builtin_bit_cast(int, v), l)); }
__device__ __forceinline__ float wave_sum(float v) {
    v += dpp<0xB1>(v); v += dpp<0x4E>(v); v += dpp<0x141>(v); v += dpp<0x140>(v);
    return (rdl(v, 0) + rdl(v, 16)) + (rdl(v, 32) + rdl(v, 48));
}
__device__ __forceinline__ float wave_max(float v) {
    v = fmaxf(v, dpp<0xB1>(v)); v = fmaxf(v, dpp<0x4E>(v)); v = fmaxf(v, dpp<0x141>(v)); v = fmaxf(v, dpp<0x140>(v));
    return fmaxf(fmaxf(rdl(v, 0), rdl(v, 16)), fmaxf(rdl(v, 32), rdl(v, 48)));
}
__device__ __forceinline__ float half_sum(float v) {
    const auto r = __builtin_amdgcn_permlane32_swap(__builtin_bit_cast(u32, v), __builtin_bit_cast(u32, v), false, false);
    return __builtin_bit_cast(float, (u32)r[0]) + __builtin_bit_cast(float, (u32)r[1]);
}
template <int I> struct IC { static constexpr int v = I; };
template <int B, int E, class Fn> __device__ __forceinline__ void static_for(Fn&& f) {
    if constexpr (B < E) { f(IC<B>{}); static_for<B + 1, E>(f); }
}

typedef f32x2 cf;
__device__ __forceinline__ cf rot(cf b) { return cf{-b.y, b.x}; }
__device__ __forceinline__ cf cmul(cf a, cf b) { return __builtin_elementwise_fma(a.yy, rot(b), a.xx * b); }
__device__ __forceinline__ cf cmulc(cf a, cf b) { return __builtin_elementwise_fma(a.yy, b.yx, a.xx * rot(b).yx); }
__device__ __forceinline__ u32 pkh(cf a) { return __builtin_bit_cast(u32, __builtin_convertvector(a, f16x2)); }

typedef __amdgpu_buffer_rsrc_t rsrc_t;
__device__ __forceinline__ v4u ld16(rsrc_t r, int voff, int soff) { return __builtin_amdgcn_raw_buffer_load_b128(r, voff, soff, SC1); }
__device__ __forceinline__ u32 ld4(rsrc_t r, int voff, int soff) { return __builtin_amdgcn_raw_buffer_load_b32(r, voff, soff, SC1); }
__device__ __forceinline__ void st16(rsrc_t r, int voff, v4u v) { __builtin_amdgcn_raw_buffer_store_b128(v, r, voff, 0, SC1); }
__device__ __forceinline__ void st8(rsrc_t r, int voff, int soff, v2u v) { __builtin_amdgcn_raw_buffer_store_b64(v, r, voff, soff, SC1); }
__device__ __forceinline__ void st4(rsrc_t r, int voff, int soff, u32 v) { __builtin_amdgcn_raw_buffer_store_b32(v, r, voff, soff, SC1); }
template <int N> __device__ __forceinline__ void wait_vm() { asm volatile("s_waitcnt vmcnt(%0)" :: "i"(N) : "memory"); }
constexpr int OOB = 0x7f000000;

__device__ __forceinline__ void wait_flags(rsrc_t rs, int voff, int soff, u32 v, u32 tag) {
    if (!__all(v == tag)) {
        int spins = SPIN_CAP;
#pragma unroll 1
        do {
            __builtin_amdgcn_s_sleep(1);
            v = ld4(rs, voff, soff);
        } while (!__all(v == tag) && --spins > 0);
    }
    __builtin_amdgcn_fence(__ATOMIC_ACQUIRE, "wavefront");
}
__device__ __forceinline__ void glds16(v4u srd, int voff, int soff, u32 lds_dst) {
    asm volatile("s_mov_b32 m0, %3\n\ts_nop 0\n\tbuffer_load_dwordx4 %0, %1, %2 offen sc1 lds"
                 :: "v"(voff), "s"(srd), "s"(soff), "s"(lds_dst) : "memory");
}
__device__ __forceinline__ LAS char* opaque(LAS char* q) { u32 a = (u32)(size_t)q; asm volatile("" : "+v"(a)); return (LAS char*)(size_t)a; }

struct Params {
    const u16 *x, *Are, *Aim, *Bre, *Bim, *Cre, *Cim, *logdt, *Dskip, *wg, *wu, *wd;
    char* ws;
    u16* out;
};

constexpr int XY_STRIDE = 272, XY_ROWS = 59, XY_WAVE = XY_STRIDE * XY_ROWS;
constexpr int P_WAVE = 33 * 144;
constexpr int LD_PB = 0, LD_PA = 38144;
constexpr int LD_HA = 38144, LD_HB = LD_HA + 57344;
constexpr int LD_XU = LD_HB;
constexpr int LD_KP = 128512;
constexpr int LD_U = LD_KP + 8 * 1536;
constexpr int LD_SB = LD_HB + 57344;
constexpr int LD_ST = LD_SB + 64;
constexpr int LD_CS = LD_ST + 4096;
constexpr int LDS_BYTES = LD_CS + 1024;
static_assert(LD_U + 2 * 4160 <= LD_SB && LDS_BYTES <= 163840, "lds map");

__device__ __forceinline__ v4u tr_piece(const v4u (&in)[8], int c) {
    v4u o;
#pragma unroll
    for (int kp = 0; kp < 4; kp++)
        o[kp] = __builtin_amdgcn_perm(in[2 * kp + 1][c >> 1], in[2 * kp][c >> 1], (c & 1) ? 0x07060302u : 0x05040100u);
    return o;
}
__device__ __forceinline__ void w1_task_load(const Params& p, int task, int lane, v2u (&in)[8]) {
    const int t = (task < 0 ? 0 : task) >> 1, c4 = task & 1;
    const int phi = t / 28, r = t - phi * 28, mat = r >= 14, kp = r - 14 * mat;
    const rsrc_t rw = __builtin_amdgcn_make_buffer_rsrc((void*)(mat ? p.wu : p.wg), 0, task < 0 ? 0 : D * F * 2, 0x00020000);
    const int voff = (8 * (lane >> 4) * F + (lane & 15) * 8) * 2;
#pragma unroll
    for (int k = 0; k < 8; k++) in[k] = __builtin_amdgcn_raw_buffer_load_b64(rw, voff + (k & 1) * F * 2, ((32 * kp + (k & 6)) * F + phi * 128 + 4 * c4) * 2, 0);
}
__device__ __forceinline__ void w1_task_store(rsrc_t rs, int task, int lane, const v2u (&in)[8]) {
    const int t = task >> 1, c4 = task & 1;
    const int phi = t / 28, r = t - phi * 28, mat = r >= 14, kp = r - 14 * mat;
    const int voff = WS_W1T + ((phi * 8 + 4 * c4) * 28 + 2 * kp) * 1024 + mat * 256 + (lane >> 4) * 512 + (lane & 15) * 16;
#pragma unroll
    for (int c = 0; c < 4; c++) {
        v4u o;
#pragma unroll
        for (int kp2 = 0; kp2 < 4; kp2++) o[kp2] = __builtin_amdgcn_perm(in[2 * kp2 + 1][c >> 1], in[2 * kp2][c >> 1], (c & 1) ? 0x07060302u : 0x05040100u);
        st16(rs, voff + c * 28 * 1024, o);
    }
}
__device__ __forceinline__ int w1_flag(int task) { const int t = task >> 1, phi = t / 28; return WS_WF1 + (phi * 2 + (task & 1)) * 128 + (t - phi * 28) * 4; }
__device__ __forceinline__ void w2_task(const Params& p, rsrc_t rs, int task, int lane, LAS char* scr) {
    const int del = task / 28, kg = task - del * 28;
    const int k4 = (lane >> 3) & 3, ks = kg * 4 + k4, h = lane >> 5, dr = lane & 7;
    const u16* src = p.wd + (size_t)((ks >> 3) * 128 + 32 * h + (ks & 7)) * D + del * 64 + dr * 8;
    v4u in[8];
#pragma unroll
    for (int j = 0; j < 8; j++) in[j] = __builtin_nontemporal_load((const v4u*)(src + (size_t)(8 * ((j & 3) + 8 * (j >> 2))) * D));
    LAS char* w = scr + ((dr >> 2) * 4 + k4) * 1024 + (32 * h + 8 * (dr & 3)) * 16;
#pragma unroll
    for (int c = 0; c < 8; c++) *(LAS v4u*)(w + c * 16) = tr_piece(in, c);
    const int voff = WS_W2T + (del * 2 * 112 + kg * 4) * 1024 + lane * 16;
#pragma unroll
    for (int f = 0; f < 8; f++) st16(rs, voff + ((f >> 2) * 112 + (f & 3)) * 1024, *(LAS v4u*)(scr + f * 1024 + lane * 16));
}

#define W1_CHK 10
#define KPRE 32
#define W1_NUM 1
#define W1_DEN 2
#define W1_BURST 4
#define PF1 0
#define PF2 1
#define GR_EARLY 3
#define CS_T 520
#define GR_T 320
#define AFB_EARLY 1
#define W1_ORDER 1
#define PRIO_C 1
constexpr int w1_cnt(int n) { return n <= W1_CHK ? 0 : W1_BURST + ((n - W1_CHK - 1) * W1_NUM) / W1_DEN > 28 ? 28 : W1_BURST + ((n - W1_CHK - 1) * W1_NUM) / W1_DEN; }
#define PRIO_A 0
#define CS_FIRST 1
#define HBP_N 10
#define W2_N0 34
#define PRIO_G 1
#define BAR_AF 1
#define OPT 0
#define AFBP_N 13
#define TAIL 0
#define ROT_H 0
#define HF_OPT 0
#define HFL_EARLY 0
#define WFL_N 0
#define SG_N 73
#define A0_LATE 0
#define MFMA_FIRST 1
#define HB_N 24
#define ROT_W1 0
#define ROT_W2 0
#define AF_N 82
#define HF_N 41
#define AFP_N 102
#define SCHED 1
constexpr int g1_op(int n) {
    return (n > HB_N && n <= HB_N + 7) ? 32 + (n - HB_N - 1)
         : n == 55 ? 40 : n == 71 ? 41 : n == 111 ? 42 : n == AF_N ? 50 : n == AFP_N ? 51 : n == 33 ? 52 : n == SG_N ? 53
         : (n >= 1 && n < 21 && (n & 1) == 1) ? (n - 1) / 2
         : (n >= 36 && n < 52 && (n & 3) == 0) ? 10 + (n - 36) / 4
         : (n >= 54 && n < 82 && (n & 3) == 2) ? 14 + (n - 54) / 4
         : (n >= 85 && n < 99 && (n & 1) == 1) ? 21 + (n - 85) / 2
         : -1;
}
constexpr bool g1_all() { int m = 0; for (int i = 0; i < 112; i++) if (g1_op(i) >= 0 && g1_op(i) < 28) m |= 1 << g1_op(i); return m == (1 << 28) - 1; }
static_assert(g1_all(), "every W2 fragment is requested");
constexpr int g1_cum(int n) { int c = 0; for (int i = 0; i < n; i++) c += g1_op(i) >= 0; return c; }
static_assert(HB_N >= 23 && HB_N < 28 && HB_N + 7 < W2_N0 && (W2_N0 & 1) == 0 && W2_N0 + 28 > 52 && W2_N0 + 42 <= AF_N && AF_N > 72 && (AF_N & 1) == 0 && (AFP_N & 1) == 0 && AFP_N > AF_N && AFP_N < 108, "schedule");
#define W1_MID 6
#define CODE_PF 23552

__global__ __launch_bounds__(512, 2) void fused_block(Params p) {
    extern __shared__ __attribute__((aligned(16))) char smem_[];
    LAS char* sm = (LAS char*)smem_;
    const int tid = threadIdx.x, lane = tid & 63;
    const int wave = __builtin_amdgcn_readfirstlane(tid >> 6);
    const int b = blockIdx.x;
    STAMP_DECL();
    STAMP(20);
    SB();
    const bool main_blk = b < 224;
    const int xcd = b & 7, i28 = b >> 3;
    u64 pc0; u32 clen;
    asm volatile("s_getpc_b64 %0\n.Lfb_code_begin:\n\ts_mov_b32 %1, .Lfb_code_end-.Lfb_code_begin" : "=s"(pc0), "=s"(clen));
    const u32 cpf = __builtin_amdgcn_raw_buffer_load_b32(__builtin_amdgcn_make_buffer_rsrc((void*)pc0, 0, main_blk ? 0 : clen, 0x00020000),
                                                         lane < 16 ? lane * 64 : OOB, (wave * 4 + (i28 & 3)) * 1024, 0);
    const int l16 = lane * 16;
    const int rbase = 16 * (b >> 1) + 2 * (b & 1);
    int x2v = lane < 56 ? (lane & 3) * (4 * D * 2) + (lane >> 2) * 8 : OOB, xrv = lane < 56 ? l16 : OOB, t2 = tid * 2;
    int x2s = (rbase * D + 56 * wave) * 2, xrs = (rbase + 4 * (wave >> 1) + (wave & 1)) * D * 2;
    int pair = xcd * 28 + i28;
    asm volatile("" : "+v"(x2v), "+v"(xrv), "+v"(t2), "+s"(x2s), "+s"(xrs), "+s"(pair));
    SB();
    const rsrc_t rs = __builtin_amdgcn_make_buffer_rsrc(p.ws, 0, WS_END, 0x00020000);
    asm volatile("" :: "s"(p.x), "s"(p.wd), "s"(p.ws));
    if (PRIO_A && wave >= 4) __builtin_amdgcn_s_setprio(1);
    STAMP(0);
    const u32 tstart = GR_EARLY == 3 ? (u32)__builtin_amdgcn_s_memrealtime() : 0;
    const u32 tagv = ld4(rs, WS_EPOCH, 0);

    const rsrc_t rx = __builtin_amdgcn_make_buffer_rsrc((void*)p.x, 0, L * D * 2, 0x00020000);
    const v2u xa = __builtin_amdgcn_raw_buffer_load_b64(rx, x2v, x2s, 2);
    const v2u xb = __builtin_amdgcn_raw_buffer_load_b64(rx, x2v + D * 2, x2s, 2);
    const v4u xr = __builtin_amdgcn_raw_buffer_load_b128(rx, xrv, xrs, 2);
    SB();
    const int ch = wave >> 2, wq = wave & 3;
    const int d0 = 2 * pair;
    const int psz = main_blk ? D * N * 2 : 0;
    auto ldp = [&](const u16* q) __attribute__((always_inline)) -> u32 {
        return __builtin_amdgcn_raw_buffer_load_b16(__builtin_amdgcn_make_buffer_rsrc((void*)q, 0, psz, 0x00020000), t2, d0 * N * 2, 2);
    };
    const u32 r_ldt = __builtin_amdgcn_raw_buffer_load_b32(__builtin_amdgcn_make_buffer_rsrc((void*)p.logdt, 0, main_blk ? D * 2 : 0, 0x00020000), 0, d0 * 2, 0);
    const u32 r_are = ldp(p.Are), r_aim = ldp(p.Aim), r_bre = ldp(p.Bre), r_bim = ldp(p.Bim), r_cre = ldp(p.Cre), r_cim = ldp(p.Cim);
    const u32 r_dsk = __builtin_amdgcn_raw_buffer_load_b32(__builtin_amdgcn_make_buffer_rsrc((void*)p.Dskip, 0, main_blk ? D * 2 : 0, 0x00020000), 0, d0 * 2, 0);
    const int hw = (b - 224) + 32 * wave;
    const int w1task = !main_blk ? -1 : wave < 2 ? pair * 2 + wave : (wave < 4 && pair < 168) ? 448 + pair * 2 + wave - 2 : -1;
    SB();
    const int h = lane >> 5, col = lane & 31;
    const int G1 = (lane >> 4) & 1, q4 = (lane & 15) >> 2, pp = lane & 3;
    int xxv = lane < 56 ? WS_XX + ((28 * wave + 2 * (lane >> 2)) * 2 + (b & 1)) * 8192 + (4 * (b >> 1) + (lane & 3)) * 16 : OOB;
    int flv = lane == 0 ? 0 : OOB;
    asm volatile("" : "+v"(xxv), "+v"(flv));
    LAS char* xy = sm + wave * XY_WAVE;
    LAS char* xyw = opaque(xy + lane * 4);
    LAS char* pa = opaque(xy + (32 + (int)((0x1713100d0a080400ull >> (8 * (col >> 2))) & 255) + (col & 3)) * XY_STRIDE + h * 16);
    LAS char* pb = opaque(xy + col * XY_STRIDE + h * 16);
    LAS char* exw = opaque(xy + l16);
    LAS char* exr = opaque(sm + (ch * 4) * XY_WAVE + wq * 1024 + l16);
    LAS char* kpw = opaque(sm + LD_KP + ch * 4 * 1536 + 512 + (wq < 2 ? 64 * wq + 2 * col + h : wq == 2 ? 64 * (2 + h) + 2 * col : 64 * (4 + 2 * h) + 2 * col) * 2);
    LAS char* uw = opaque(sm + LD_U + 64 + tid * 8);
    LAS char* ua = opaque(sm + LD_U + ch * 4160 + 2 * (32 + 32 + 16 * G1 - 32 * h - 16 - 4 * q4 + 4 * pp));
    LAS char* ka = opaque(sm + LD_KP + wave * 1536 + 2 * (256 - 248 + 8 * col + 8 * h));
    LAS char* pw = opaque(sm + wave * P_WAVE + (col + 1) * 144 + h * 16);
    *(LAS v2u*)(sm + LD_KP + wave * 1536 + lane * 8) = v2u{0, 0};
    if (tid < 32) *(LAS u32*)(sm + LD_U + (tid >> 4) * 4160 + (tid & 15) * 4) = 0;
    SB();
    const u32 tag = __builtin_amdgcn_readfirstlane(tagv) + 1u;
    st16(rs, xxv, v4u{xa[0], tag, xb[0], tag});
    st16(rs, xxv + 16384, v4u{xa[1], tag, xb[1], tag});
    SB();
    {
        float a = 0.f, q = 0.f;
#pragma unroll
        for (int i = 0; i < 4; i++) { a = dot2bf(xr[i], 0x3f803f80u, a); q = dot2bf(xr[i], xr[i], q); }
        const float mean = wave_sum(a) * (1.f / D), ex2 = wave_sum(q) * (1.f / D);
        const float var = rbf(fmaxf(ex2 - mean * mean, 0.f));
        const float sd = __builtin_amdgcn_sqrtf(rbf(var + 1e-5f));
        st8(rs, flv, WS_ST1 + (((b & 1) * 512 + 4 * (b >> 1) + (wave >> 1)) * 2 + (wave & 1)) * 8, v2u{pk_bf16(mean, sd), tag});
    }
    SB();
    v2u tin[8];
    w1_task_load(p, w1task, lane, tin);
    SB();
    STAMP(1);
    const int tau = main_blk ? (xcd >> 1) * 4 + (i28 & 3) : (b & 15), phi = (xcd & 1) * 7 + (i28 >> 2);
    const int del = i28 >> 2, tau2 = 2 * tau + (xcd & 1);
    const int trl = q4 * 128 + 8 * (pp & 1) + 16 * (2 * G1 + (pp >> 1)), sw = (q4 & 2) << 5;
    const int hf_v = (lane < 56 ? lane : 0) * 4, hf_s = WS_HF + ((tau >> 2) * 8 + wave) * 256;
    const v4u srd = {(u32)(size_t)p.ws, (u32)((size_t)p.ws >> 32) & 0xffffu, (u32)WS_END, 0x00020000u};
    const int hvoff = WS_HT + ((8 * wave + (lane >> 3)) * L + tau * 64) * 2 + (((lane & 7) ^ (((lane >> 3) & 2) << 1)) << 4);
    if (!main_blk) {
        BARRIER_RAW();
        const int w2task = hw;
        if (w2task < 196) {
            w2_task(p, rs, w2task, lane, sm + LD_HB + wave * 8192);
            wait_vm<0>();
            st4(rs, flv, WS_WF2 + (w2task / 28) * 128 + (w2task % 28) * 4, tag);
        }
        asm volatile("" :: "v"(cpf), "v"(tin[7]));
        STAMP(2);
        const int unit = 255 - hw;
        if (unit < 112) {
            while ((u32)__builtin_amdgcn_s_memrealtime() - tstart < CS_T) __builtin_amdgcn_s_sleep(8);
            const int fv = (col < 28 ? col : 0) * 4, fs = WS_WF1 + ((unit >> 3) * 2 + ((unit >> 2) & 1)) * 128;
            wait_flags(rs, fv, fs, ld4(rs, fv, fs), tag);
            float c0 = 0.f, c1 = 0.f;
#pragma unroll 1
            for (int k0 = 0; k0 < 28; k0 += 14) {
                v4u w[14];
#pragma unroll
                for (int ks = 0; ks < 14; ks++) w[ks] = ld16(rs, l16 + (ks & 3) * 1024, WS_W1T + (unit * 28 + k0 + (ks & ~3)) * 1024);
#pragma unroll
                for (int ks = 0; ks < 14; ks++) {
                    c0 = dot2bf(w[ks][0], 0x3f803f80u, c0); c1 = dot2bf(w[ks][1], 0x3f803f80u, c1);
                    c0 = dot2bf(w[ks][2], 0x3f803f80u, c0); c1 = dot2bf(w[ks][3], 0x3f803f80u, c1);
                }
            }
            st8(rs, lane < 32 ? WS_CS + lane * 8 : OOB, unit * 256, v2u{__builtin_bit_cast(u32, half_sum(c0 + c1)), tag});
        }
        STAMP(3);
        {
            const int tl = (b >> 4) & 1;
            if (wave == 0) {
                int spins = SPIN_CAP;
#pragma unroll 1
                while (ld4(rs, 0, WS_TR + (b - 224) * 128) != tag && --spins > 0) __builtin_amdgcn_s_sleep(4);
            }
            BARRIER();
            if (wave < 7) {
                const int gv = WS_HT + ((lane >> 2) * L + 1024 + tau * 64 + tl * 32) * 2 + (lane & 3) * 16;
                LAS char* as = sm + 4096 + wave * 4096 + (8 * h + q4) * 64 + G1 * 32 + pp * 8;
#pragma unroll
                for (int n = 0; n < 4; n++) glds16(srd, gv, (wave * 4 + n) * 65536, (u32)(size_t)sm + 4096 + wave * 4096 + n * 1024);
                wait_vm<0>();
                v4u f[4];
#pragma unroll
                for (int k = 0; k < 4; k++) f[k] = lds_tr2(as + k * 1024, as + k * 1024 + 256);
                float s1 = 0.f, s2 = 0.f, s1b = 0.f, s2b = 0.f;
#pragma unroll
                for (int k = 0; k < 4; k++) {
                    s1 = dot2bf(f[k][0], 0x3f803f80u, s1); s2 = dot2bf(f[k][0], f[k][0], s2);
                    s1b = dot2bf(f[k][1], 0x3f803f80u, s1b); s2b = dot2bf(f[k][1], f[k][1], s2b);
                    s1 = dot2bf(f[k][2], 0x3f803f80u, s1); s2 = dot2bf(f[k][2], f[k][2], s2);
                    s1b = dot2bf(f[k][3], 0x3f803f80u, s1b); s2b = dot2bf(f[k][3], f[k][3], s2b);
                }
                *(LAS f32x2*)(sm + wave * 512 + lane * 8) = f32x2{s1 + s1b, s2 + s2b};
            }
            BARRIER();
            if (wave == 0) {
                f32x2 t = *(LAS f32x2*)(sm + lane * 8);
#pragma unroll
                for (int k = 1; k < 7; k++) t += *(LAS f32x2*)(sm + k * 512 + lane * 8);
                const float mean = half_sum(t.x) * (1.f / D), ex2 = half_sum(t.y) * (1.f / D);
                const float var = rbf(fmaxf(ex2 - mean * mean, 0.f));
                st8(rs, lane < 32 ? WS_SG + col * 16 + tl * 8 : OOB, tau * 512, v2u{pk_bf16(mean, __builtin_amdgcn_sqrtf(rbf(var + 1e-5f))), tag});
            }
        }
        STAMP(4);
        STAMP_FLUSH();
        return;
    }
    asm volatile("" :: "v"(r_are), "v"(r_aim), "v"(r_bre), "v"(r_bim), "v"(r_cre), "v"(r_cim), "v"(r_dsk), "v"(cpf));
    SB();

    float unscale;
    {
        const float dt = rbf(__expf(ch ? bfhi(r_ldt) : bflo(r_ldt)));
        const float ar = dt * bflo(r_are), ai = dt * bflo(r_aim);
        const float br = dt * bflo(r_bre), bi = dt * bflo(r_bim);
        const float cr = bflo(r_cre), ci = bflo(r_cim);
        cf cb = {cr * br - ci * bi, -(cr * bi + ci * br)};
        const float a2 = ar * 1.44269504088896340736f;
        const float rh = ai * 0.15915494309189535f;
        const float rl = __builtin_fmaf(ai, 0.15915494309189535f, -rh) + ai * 6.4206382e-9f;
        auto cexp = [&](float s) __attribute__((always_inline)) -> cf {
            const float e = __builtin_amdgcn_exp2f(a2 * s);
            const float r = __builtin_amdgcn_fractf(rh * s) + rl * s;
            return cf{__builtin_amdgcn_cosf(r), __builtin_amdgcn_sinf(r)} * e;
        };
        const float grow = fmaxf(1.f, __builtin_amdgcn_exp2f(a2 * 2048.f));
        const float sb = wave_sum((fabsf(cb.x) + fabsf(cb.y)) * grow);
        *(LAS float*)(sm + LD_SB + wave * 4) = sb;
        int ex = (int)((__builtin_bit_cast(u32, sb) >> 23) & 255);
        ex = min(max(ex, 30), 230);
        unscale = __builtin_bit_cast(float, (u32)(127 - 15 - 126 + ex) << 23);
        cb *= __builtin_bit_cast(float, (u32)(127 + 15 + 126 - ex) << 23);

        const cf e1 = cexp(1.f), e16 = cexp(16.f), e256 = cexp(256.f);
        const cf e2 = cmul(e1, e1), e32 = cmul(e16, e16), e512 = cmul(e256, e256), x1 = cmulc(cb, e1), x256 = cmulc(cb, e256);
        SB();
        const cf e4 = cmul(e2, e2), e64 = cmul(e32, e32), e1024 = cmul(e512, e512), x2 = cmulc(cb, e2), x3 = cmulc(x1, e2);
        const cf x512 = cmulc(cb, e512), x768 = cmulc(x256, e512), y6 = cmul(e16, e32);
        SB();
        const cf e8 = cmul(e4, e4), e128 = cmul(e64, e64);
        const cf x4 = cmulc(cb, e4), x5 = cmulc(x1, e4), x6 = cmulc(x2, e4), x7 = cmulc(x3, e4), x258 = cmulc(x2, e256);
        const cf x1024 = cmulc(cb, e1024), x1280 = cmulc(x256, e1024), x1536 = cmulc(x512, e1024), x1792 = cmulc(x768, e1024);
        SB();
        auto put = [&](int q, cf v) __attribute__((always_inline)) { *(LAS u32*)(xyw + q * XY_STRIDE) = pkh(v); };
        cf y[8];
        y[0] = cf{1.f, 0.f}; y[1] = e8; y[2] = e16; y[3] = cmul(e8, e16); y[4] = e32; y[5] = cmul(e8, e32); y[6] = y6;
        const cf e192 = cmul(e128, e64), x8 = cmulc(x4, e4), x260 = cmulc(x4, e256), x262 = cmulc(x6, e256), x516 = cmulc(x4, e512);
        put(32, cb); put(33, x1); put(34, x2); put(35, x3); put(36, x4); put(37, x5); put(38, x6); put(39, x7);
        put(40, x256); put(41, x258);
        SB();
        y[7] = cmul(y[3], e32);
        const cf x264 = cmulc(x8, e256), x520 = cmulc(x8, e512), x772 = cmulc(x260, e512), x1032 = cmulc(x8, e1024);
        cf t[8];
#pragma unroll
        for (int q = 1; q < 7; q++) t[q] = cmul(y[q], e64);
        put(42, x260); put(43, x262); put(45, x512); put(46, x516);
#pragma unroll
        for (int q = 0; q < 6; q++) put(q, y[q]);
        SB();
        t[7] = cmul(y[7], e64);
        const cf x776 = cmulc(x264, e512), x1288 = cmulc(x264, e1024), x1544 = cmulc(x520, e1024);
        cf t2[8];
#pragma unroll
        for (int q = 1; q < 8; q++) t2[q] = cmul(y[q], e128);
        put(6, y[6]); put(7, y[7]); put(8, e64);
#pragma unroll
        for (int q = 1; q < 6; q++) put(8 + q, t[q]);
        put(44, x264); put(47, x520); put(48, x768); put(49, x772); put(51, x1024); put(52, x1032);
        SB();

        const cf x1800 = cmulc(x776, e1024);
        cf t3[8];
#pragma unroll
        for (int q = 1; q < 8; q++) t3[q] = cmul(y[q], e192);
        put(14, t[6]); put(15, t[7]); put(16, e128);
#pragma unroll
        for (int q = 1; q < 8; q++) put(16 + q, t2[q]);
        put(50, x776); put(53, x1280); put(54, x1288); put(55, x1536); put(56, x1544); put(57, x1792);
        SB();
        put(24, e192);
#pragma unroll
        for (int q = 1; q < 8; q++) put(24 + q, t3[q]);
        put(58, x1800);
    }
    SB();
    STAMP(2);
    asm volatile("" :: "v"(tin[7]));
    if (w1task >= 0) w1_task_store(rs, w1task, lane, tin);
    SB();
    const int t16 = tid * 16;
    v4u gx[2], gs[2];
    auto gr_load = [&]() __attribute__((always_inline)) {
        gx[0] = ld16(rs, t16, WS_XX + pair * 16384); gx[1] = ld16(rs, t16, WS_XX + pair * 16384 + 8192);
        gs[0] = ld16(rs, t16, WS_ST1); gs[1] = ld16(rs, t16, WS_ST1 + 8192);
    };
    const bool gr_first = (u32)__builtin_amdgcn_s_memrealtime() - tstart >= GR_T;
    if (gr_first) gr_load();
    SB();
    {
        f32x16 kd;
#pragma unroll
        for (int i2 = 0; i2 < 16; i2++) kd[i2] = 0.f;
        v4u fa[8], fb[8];
#pragma unroll
        for (int s = 0; s < 8; s++) { fa[s] = *(LAS v4u*)(pa + s * 32); fb[s] = *(LAS v4u*)(pb + s * 32); }
        SB();
#pragma unroll
        for (int s = 0; s < 8; s++) kd = mfma_f16(fa[s], fb[s], kd);
        SB();

#pragma unroll
        for (int g2 = 0; g2 < 4; g2++)
            *(LAS f32x4*)(exw + g2 * 1024) = f32x4{kd[4 * g2], kd[4 * g2 + 1], kd[4 * g2 + 2], kd[4 * g2 + 3]} * unscale;
    }
    if (w1task >= 0) { wait_vm<0>(); st4(rs, flv, w1_flag(w1task), tag); }
    if (!gr_first) {
        while ((u32)__builtin_amdgcn_s_memrealtime() - tstart < GR_T) __builtin_amdgcn_s_sleep(2);
        gr_load();
    }
    SB();
    BARRIER();
    __builtin_amdgcn_s_setprio(0);
    if (GR_EARLY == 0) gr_load();
    const int l28 = (col < 28 ? col : 0) * 4;
    SB();
    STAMP(3);
    float ik;
    {
        const f32x4 s0 = *(LAS f32x4*)(sm + LD_SB + ch * 16);
        const f32x4 a0 = *(LAS f32x4*)(exr), a1 = *(LAS f32x4*)(exr + XY_WAVE), a2 = *(LAS f32x4*)(exr + 2 * XY_WAVE), a3 = *(LAS f32x4*)(exr + 3 * XY_WAVE);
        const float b0 = (s0[0] + s0[1]) + (s0[2] + s0[3]);
        int e0 = (int)((__builtin_bit_cast(u32, b0) >> 23) & 255);
        e0 = min(max(e0, 30), 230);
        ik = __builtin_bit_cast(float, (u32)(127 - 15 - 126 + e0) << 23);
        const float sk = __builtin_bit_cast(float, (u32)(127 + 15 + 126 - e0) << 23);
        const f32x4 v = ((a0 + a1) + (a2 + a3)) * sk;
        const u32 p01 = pk_f16(v[0], v[1]), p23 = pk_f16(v[2], v[3]);
        LAS char* o = kpw;
        if (wq == 0) {
            *(LAS u16*)(o) = (u16)p01; *(LAS u16*)(o + 1536) = (u16)(p01 >> 16);
            *(LAS u16*)(o + 2 * 1536) = (u16)p23; *(LAS u16*)(o + 3 * 1536) = (u16)(p23 >> 16);
        } else if (wq == 1) {
            *(LAS u16*)(o) = (u16)p01; *(LAS u16*)(o + 1536) = (u16)p01;
            *(LAS u16*)(o + 2 * 1536) = (u16)(p01 >> 16); *(LAS u16*)(o + 3 * 1536) = (u16)p23;
        } else if (wq == 2) {
            *(LAS u32*)(o) = p01; *(LAS u32*)(o + 1536) = p01;
            *(LAS u32*)(o + 2 * 1536) = (p01 & 0xffffu) | (p23 << 16);
            *(LAS u32*)(o + 3 * 1536) = (p01 >> 16) | (p23 << 16);
        } else {
            const bool ev = (col & 1) == 0;
            *(LAS u32*)(o) = ev ? ((p01 & 0xffffu) | (p01 << 16)) : p01;
            *(LAS u32*)(o + 1536) = p01; *(LAS u32*)(o + 2 * 1536) = p01; *(LAS u32*)(o + 3 * 1536) = p01;
            *(LAS u32*)(o + 128) = ev ? ((p23 & 0xffffu) | (p23 << 16)) : p23;
            *(LAS u32*)(o + 128 + 1536) = p23; *(LAS u32*)(o + 128 + 2 * 1536) = p23; *(LAS u32*)(o + 128 + 3 * 1536) = p23;
        }
    }
    SB();
    auto gr_ok = [&]() __attribute__((always_inline)) {
        const u32 a = (gx[0][1] ^ tag) | (gx[0][3] ^ tag) | (gx[1][1] ^ tag) | (gx[1][3] ^ tag);
        const u32 c = (gs[0][1] ^ tag) | (gs[0][3] ^ tag) | (gs[1][1] ^ tag) | (gs[1][3] ^ tag);
        return __all((a | c) == 0);
    };
    if (!gr_ok()) {
        int spins = SPIN_CAP;
#pragma unroll 1
        do {
            __builtin_amdgcn_s_sleep(4);
            gr_load();
        } while (!gr_ok() && --spins > 0);
    }
    STAMP(4);
    v4u wf[28];
    const int w1off = WS_W1T + (phi * 8 + wave) * 28 * 1024;
    int seg7[4];
#pragma unroll
    for (int i = 0; i < 4; i++) seg7[i] = ((i + i28 * ROT_W1) & 3) * 7;
    auto w1_load = [&](int ks) __attribute__((always_inline)) {
        wf[ks] = ld16(rs, l16 + ((ks % 7) & 3) * 1024, w1off + seg7[ks / 7] * 1024 + ((ks % 7) >> 2) * 4096);
    };
    const int wf_s = WS_WF1 + phi * 256;
    u32 wfl;
    if (WFL_N < 0) wfl = ld4(rs, l28 + h * 128, wf_s);
    SB();
    u32 xc[4], udk[4];
    {
        float u0[4], u1[4];
#pragma unroll
        for (int i = 0; i < 4; i++) {
            xc[i] = gx[i >> 1][2 * (i & 1)];
            const u32 st = gs[i >> 1][2 * (i & 1)];
            const float m = bflo(st), r = __builtin_amdgcn_rcpf(bfhi(st));
            const u32 dd = pk_bf16(bflo(xc[i]) - m, bfhi(xc[i]) - m);
            const u32 uu = pk_bf16(bflo(dd) * r, bfhi(dd) * r);
            u0[i] = bflo(uu); u1[i] = bfhi(uu);
        }
        *(LAS v2u*)(uw) = v2u{pk_f16(u0[0], u0[1]), pk_f16(u0[2], u0[3])};
        *(LAS v2u*)(uw + 4160) = v2u{pk_f16(u1[0], u1[1]), pk_f16(u1[2], u1[3])};
#pragma unroll
        for (int i = 0; i < 4; i++) udk[i] = pk_bf16(u0[i] * bflo(r_dsk), u1[i] * bfhi(r_dsk));
    }
    BARRIER();
    STAMP(5);
    f32x4 r0, r1, l3;
    f32x2 r2, l2;
    float l1, r3;
    v4u xu;
    u32 ya, yb;
    const int c1 = tid & 255;
    LAS char* q1 = opaque(sm + ch * 4 * P_WAVE + ((c1 >> 3) + 1) * 144 + (c1 & 7) * 16);
    LAS char* q0 = opaque(sm + ch * 4 * P_WAVE + (((c1 - 1) >> 3) + 1) * 144 + ((c1 - 1) & 7) * 16);
    LAS char* xur = opaque(sm + LD_XU + ch * 8192 + c1 * 16);
    auto ce_read = [&](int hf) __attribute__((always_inline)) {
        const int pb_ = hf ? LD_PB : LD_PA;
        r0 = *(LAS f32x4*)(q1 + pb_);
        r1 = *(LAS f32x4*)(q1 + pb_ + P_WAVE); l1 = *(LAS float*)(q0 + pb_ + P_WAVE + 12);
        r2 = *(LAS f32x2*)(q1 + pb_ + 2 * P_WAVE); l2 = *(LAS f32x2*)(q0 + pb_ + 2 * P_WAVE + 8);
        r3 = *(LAS float*)(q1 + pb_ + 3 * P_WAVE); l3 = *(LAS f32x4*)(q0 + pb_ + 3 * P_WAVE);
        xu = *(LAS v4u*)(xur + hf * 4096);
    };
    auto ce_part = [&](int hf, int part) __attribute__((always_inline)) {
        if (part == 0) ya = pk_bf16s(((r0[0] + l2[0]) + (l1 + l3[1])) * ik, ((r0[1] + l2[1]) + (r1[0] + l3[2])) * ik);
        if (part == 1) yb = pk_bf16s(((r0[2] + r2[0]) + (r1[1] + l3[3])) * ik, ((r0[3] + r2[1]) + (r1[2] + r3)) * ik);
        if (part == 2) ya = pk_bf16s(bflo(ya) + bfhi(xu[0]), bfhi(ya) + bfhi(xu[1]));
        if (part == 3) yb = pk_bf16s(bflo(yb) + bfhi(xu[2]), bfhi(yb) + bfhi(xu[3]));
        if (part == 4) ya = pk_bf16s(bflo(ya) + bflo(xu[0]), bfhi(ya) + bflo(xu[1]));
        if (part == 5) {
            yb = pk_bf16s(bflo(yb) + bflo(xu[2]), bfhi(yb) + bflo(xu[3]));
            st8(rs, WS_HT + c1 * 8, (d0 + ch) * (L * 2) + hf * 2048, v2u{ya, yb});
        }
    };
    const int hf_set = WS_HF + (wq * 8 + (((d0 + ch) >> 3) & 7)) * 256 + (((d0 + ch) >> 6) * 8 + ((d0 + ch) & 7)) * 4;
    u32 pf1;
    {
        f32x16 acc0, acc1;
#pragma unroll
        for (int i = 0; i < 16; i++) { acc0[i] = 0.f; acc1[i] = 0.f; }
        constexpr int PD = 3;
        v4u fu[PD + 1], fk[16], fk1[PD + 1];
        auto issue = [&](int n) __attribute__((always_inline)) {
            const int B0 = (n >= 16 && n < 32) ? 2016 - 64 * (n - 16) : 992 - 64 * (n & 15);
            fu[n % (PD + 1)] = lds_tr2(ua + 2 * (B0 - 32) + 32, ua + 2 * (B0 - 32));
            if (n < 16) fk[n] = *(LAS v4u*)(ka + 32 * n);
            if (n >= 32) fk1[n % (PD + 1)] = *(LAS v4u*)(ka + 32 * (n - 16));
        };
#pragma unroll
        for (int n = 0; n < PD; n++) issue(n);
        SB();
        static_for<0, 48>([&](auto N_) __attribute__((always_inline)) {
            constexpr int n = decltype(N_)::v;
            if (n + PD < 48) issue(n + PD);
            if constexpr (n < 16) acc0 = mfma_f16(fu[n % (PD + 1)], fk[n], acc0);
            else if constexpr (n < 32) acc1 = mfma_f16(fu[n % (PD + 1)], fk[n - 16], acc1);
            else acc1 = mfma_f16(fu[n % (PD + 1)], fk1[n % (PD + 1)], acc1);
            if (n == 1) *(LAS v4u*)(sm + LD_XU + t16) = v4u{__builtin_amdgcn_perm(udk[0], xc[0], 0x05040100u), __builtin_amdgcn_perm(udk[1], xc[1], 0x05040100u),
                                                           __builtin_amdgcn_perm(udk[2], xc[2], 0x05040100u), __builtin_amdgcn_perm(udk[3], xc[3], 0x05040100u)};
            if (n == 2) *(LAS v4u*)(sm + LD_XU + 8192 + t16) = v4u{__builtin_amdgcn_perm(udk[0], xc[0], 0x07060302u), __builtin_amdgcn_perm(udk[1], xc[1], 0x07060302u),
                                                                  __builtin_amdgcn_perm(udk[2], xc[2], 0x07060302u), __builtin_amdgcn_perm(udk[3], xc[3], 0x07060302u)};
            if (n == WFL_N) wfl = ld4(rs, l28 + h * 128, wf_s);
            if (n == W1_CHK) {
                PIN(acc0); SB();
                wait_flags(rs, l28 + h * 128, wf_s, wfl, tag);
                pf1 = PF1 ? ld4(rs, tid < 448 ? tid * 128 : OOB, WS_W1T + phi * 229376 + (i28 & 3) * 57344) : 0;
                STAMP(18);
            }
            if constexpr (n == 16 || n == 17) {
                constexpr int g = 2 * (n - 16);
                *(LAS f32x4*)(pw + LD_PA + g * 32) = f32x4{acc0[4 * g], acc0[4 * g + 1], acc0[4 * g + 2], acc0[4 * g + 3]};
                *(LAS f32x4*)(pw + LD_PA + g * 32 + 32) = f32x4{acc0[4 * g + 4], acc0[4 * g + 5], acc0[4 * g + 6], acc0[4 * g + 7]};
            }
            if (n == 17) {
                PIN(acc1);
                if (lane == 63) {
                    *(LAS f32x4*)(sm + LD_PA + wave * P_WAVE + 112) = f32x4{0.f, 0.f, 0.f, 0.f};
                    *(LAS f32x4*)(sm + LD_PB + wave * P_WAVE + 112) = f32x4{acc0[12], acc0[13], acc0[14], acc0[15]};
                }
            }
            if (n == 18) {
                BARRIER();
                if (PRIO_C) __builtin_amdgcn_s_setprio(1);
                STAMP(19);
                st4(rs, (b == 0 && tid == 0) ? WS_EPOCH : OOB, 0, tag);
                ce_read(0);
            }
            if (n >= 21 && n < 27) ce_part(0, n - 21);
            if (n == HF_N) {
                STAMP(22);
                if (!HF_OPT) wait_vm<w1_cnt(HF_N) - w1_cnt(26)>();
                st4(rs, flv, hf_set, tag);
                if (PRIO_C) __builtin_amdgcn_s_setprio(0);
                STAMP(23);
            }
            SB();
#pragma unroll
            for (int ks = w1_cnt(n); ks < w1_cnt(n + 1); ks++) w1_load(ks);
            SB();
        });
#pragma unroll
        for (int g = 0; g < 4; g++) *(LAS f32x4*)(pw + LD_PB + g * 32) = f32x4{acc1[4 * g], acc1[4 * g + 1], acc1[4 * g + 2], acc1[4 * g + 3]};
    }
    constexpr int W1_A = w1_cnt(48) + W1_MID > 28 ? 28 : w1_cnt(48) + W1_MID;
#pragma unroll
    for (int ks = w1_cnt(48); ks < W1_A; ks++) { w1_load(ks); SB(); }
    const int kap = phi * 8 + wave;
    const u32 w2fl = ld4(rs, l28, WS_WF2 + del * 128);
    v2u csg = __builtin_amdgcn_raw_buffer_load_b64(rs, WS_CS + col * 8, kap * 256, SC1);
    u32 hfl;
    if (HFL_EARLY) hfl = ld4(rs, hf_v, hf_s);
    BARRIER();
    STAMP(6);
    ce_read(1);
    int rot14[14], rot7[7];
    {
        int i = (2 * del + ((i28 & 3) * 7 >> 1)) * ROT_W2;
        i = i >= 14 ? i - 14 : i;
#pragma unroll
        for (int n = 0; n < 14; n++) { rot14[n] = i * 1024; i = i == 13 ? 0 : i + 1; }
        i = del * ROT_H;
#pragma unroll
        for (int n = 0; n < 7; n++) { rot7[n] = i * 8192; i = i == 6 ? 0 : i + 1; }
    }
    auto load_h = [&](int hf, int n) __attribute__((always_inline)) {
        glds16(srd, hvoff + hf * 2048, rot7[n] * 32, (u32)(size_t)sm + (hf ? LD_HB : LD_HA) + wave * 1024 + rot7[n]);
    };
#pragma unroll
    for (int i = 0; i < 5; i++) ce_part(1, i);
    SB();
    if (!HFL_EARLY) hfl = ld4(rs, hf_v, hf_s);
    SB();
    ce_part(1, 5);
    SB();
    STAMP(21);
#pragma unroll
    for (int ks = W1_A; ks < 28; ks++) { w1_load(ks); SB(); }
    wait_flags(rs, l28, WS_WF2 + del * 128, w2fl, tag);
    if (!__all(csg[1] == tag)) {
        int spins = SPIN_CAP;
#pragma unroll 1
        do {
            __builtin_amdgcn_s_sleep(4);
            csg = __builtin_amdgcn_raw_buffer_load_b64(rs, WS_CS + col * 8, kap * 256, SC1);
        } while (!__all(csg[1] == tag) && --spins > 0);
    }
    *(LAS u32*)(sm + LD_CS + wave * 128 + col * 4) = csg[0];
    wait_flags(rs, hf_v, hf_s, hfl, tag);
    STAMP(7);
#pragma unroll
    for (int n = 0; n < 7; n++) load_h(0, n);
    SB();
    wait_vm<7>();
    st4(rs, flv, hf_set + 8192, tag);
    SB();
    asm volatile("" :: "v"(pf1));
    wait_vm<1>();
    STAMP(8);
    BARRIER();
    STAMP(9);
    v4u w2[28];
    const int w2v = WS_W2T + (del * 2 * 112 + wave * 14) * 1024 + l16;
    auto w2_lds = [&](int f) __attribute__((always_inline)) { return f < 14 ? LD_PB + (wave * 4 + f - 10) * 1024 : LD_HA + (wave * 7 + f - 14) * 1024; };
    auto w2_load = [&](int f) __attribute__((always_inline)) {
        if (f >= 10 && f < 21) glds16(srd, w2v + (f & 1) * 112 * 1024, rot14[f >> 1], (u32)(size_t)sm + w2_lds(f));
        else w2[f] = ld16(rs, w2v + (f & 1) * 112 * 1024, rot14[f >> 1]);
    };
    const int af_v = (lane < 14 ? lane : 0) * 4, af_s = WS_AF + (tau * 16 + wave) * 128;
    const int av = WS_ACT + (tau2 * 112 + wave * 14) * 1024 + l16;
    const int dd = 64 * del + 8 * wave + 4 * h;
    LAS char* hra = sm + LD_HA + dd * 128 + trl + (((xcd & 1) << 6) ^ sw);
    v2u hr[2];
    u32 afl;
    v4u a0[14], a1[14];
    const int af_set = WS_AF + (tau * 16 + kap / 14) * 128 + (kap % 14) * 4;
    {
        LAS char* at0 = opaque(sm + LD_HA + 8 * h * 128 + trl + sw);
        LAS char* at1 = opaque(sm + LD_HA + 8 * h * 128 + trl + (64 ^ sw));
        LAS char* csr = opaque(sm + LD_CS + wave * 128 + h * 16);
        LAS char* as = opaque(sm + LD_HA + 8 * h * 128 + trl + (((wave & 1) << 6) ^ sw) + (wave >> 1) * 7 * 2048);
        LAS char* str = opaque(sm + LD_ST + col * 32);
        LAS char* cur;
        constexpr int PD = 4;
        v4u fr[PD + 1], sf, sf2;
        auto issue = [&](int n) __attribute__((always_inline)) {
            const int tile = n / 28, j = n % 28;
            if (j % 7 == 0) cur = opaque(((tile & 1) ? at1 : at0) + (seg7[j / 7] * 2048 + (tile >> 1) * 57344));
            LAS char* a = cur + (j % 7) * 2048;
            fr[n % (PD + 1)] = lds_tr2(a, a + 512);
        };
        f32x16 acc[2];
        float nm, k0, k1, sd2, eg, eu, ez, er, eo0;
        f32x4 cg4, cu4;
        f32x4 sta, stb;
        float s1, s2, s1b, s2b;
        u32 opk[4];
        auto stat_acc = [&](int k, int part, int which = 0) __attribute__((always_inline)) {
            const u32 f0 = which ? sf2[2 * part] : sf[2 * part], f1 = which ? sf2[2 * part + 1] : sf[2 * part + 1];
            if (k == 0 && part == 0) { s1 = 0.f; s2 = 0.f; s1b = 0.f; s2b = 0.f; }
            s1 = dot2bf(f0, 0x3f803f80u, s1); s2 = dot2bf(f0, f0, s2);
            s1b = dot2bf(f1, 0x3f803f80u, s1b); s2b = dot2bf(f1, f1, s2b);
        };
        auto stat_pin = [&]() __attribute__((always_inline)) { asm volatile("" : "+v"(s1), "+v"(s2), "+v"(s1b), "+v"(s2b)); };
        auto stat_write = [&](int hf) __attribute__((always_inline)) {
            const float a = half_sum(s1 + s1b), c = half_sum(s2 + s2b);
            *(LAS f32x2*)(str + (hf * 2 + (wave & 1)) * 1024 + (wave >> 1) * 8) = f32x2{a, c};
        };
        auto epi_read = [&](int tile) __attribute__((always_inline)) { sta = *(LAS f32x4*)(str + tile * 1024); stb = *(LAS f32x4*)(str + tile * 1024 + 16); };
        auto epi_stats = [&]() __attribute__((always_inline)) {
            float m = ((sta[0] + sta[2]) + (stb[0] + stb[2])) * (1.f / D), e2 = ((sta[1] + sta[3]) + (stb[1] + stb[3])) * (1.f / D);
            asm("" : "+v"(m)); asm("" : "+v"(e2));
            const float var = rbf(fmaxf(e2 - m * m, 0.f));
            const float sd = rbf(__builtin_amdgcn_sqrtf(rbf(var + 1e-5f))), r = __builtin_amdgcn_rcpf(sd);
            nm = -rbf(m); sd2 = sd * sd; k0 = r * -2.3022082f; k1 = (r * r) * (r * -0.10294324f);
        };
        v4u sg;
        auto epi_stats2 = [&](u32 st) __attribute__((always_inline)) {
            const float sd = bfhi(st), r = __builtin_amdgcn_rcpf(sd);
            nm = -bflo(st); sd2 = sd * sd; k0 = r * -2.3022082f; k1 = (r * r) * (r * -0.10294324f);
        };
        auto cs_read = [&](int g) __attribute__((always_inline)) { cg4 = *(LAS f32x4*)(csr + g * 32); cu4 = *(LAS f32x4*)(csr + 64 + g * 32); };
        auto slice = [&](const f32x16& a, int sidx) __attribute__((always_inline)) {
            const int e = sidx / 3, st = sidx % 3;
            if (st == 0) {
                if (e > 0) { float o = eu * er; asm("" : "+v"(o)); if ((e - 1) & 1) opk[(e - 1) >> 1] = pk_bf16(eo0, o); else eo0 = o; }
                if (e < 8) {
                    eg = __builtin_fmaf(nm, cg4[e & 3], a[e & 7]); eu = __builtin_fmaf(nm, cu4[e & 3], a[8 + (e & 7)]);
                    asm("" : "+v"(eg), "+v"(eu));
                }
            } else if (st == 1) { ez = __builtin_amdgcn_exp2f(eg * __builtin_fmaf(eg * eg, k1, k0)); asm("" : "+v"(ez)); }
            else { er = __builtin_amdgcn_rcpf(__builtin_fmaf(ez, sd2, sd2)); eu *= eg; asm("" : "+v"(er), "+v"(eu)); }
        };
        auto epi_four = [&](const f32x16& a, int g) __attribute__((always_inline)) {
            const f32x4 nm4 = {nm, nm, nm, nm}, sd4 = {sd2, sd2, sd2, sd2};
            const f32x4 gg = __builtin_elementwise_fma(nm4, cg4, f32x4{a[4 * g], a[4 * g + 1], a[4 * g + 2], a[4 * g + 3]});
            const f32x4 u = __builtin_elementwise_fma(nm4, cu4, f32x4{a[8 + 4 * g], a[9 + 4 * g], a[10 + 4 * g], a[11 + 4 * g]});
            const f32x4 z = gg * __builtin_elementwise_fma(gg * gg, f32x4{k1, k1, k1, k1}, f32x4{k0, k0, k0, k0});
            const f32x4 e = {__builtin_amdgcn_exp2f(z.x), __builtin_amdgcn_exp2f(z.y), __builtin_amdgcn_exp2f(z.z), __builtin_amdgcn_exp2f(z.w)};
            const f32x4 dn = __builtin_elementwise_fma(e, sd4, sd4);
            const f32x4 o = (gg * u) * f32x4{__builtin_amdgcn_rcpf(dn.x), __builtin_amdgcn_rcpf(dn.y), __builtin_amdgcn_rcpf(dn.z), __builtin_amdgcn_rcpf(dn.w)};
            opk[2 * g] = pk_bf16(o.x, o.y); opk[2 * g + 1] = pk_bf16(o.z, o.w);
        };
        auto epi_store = [&](int tile) __attribute__((always_inline)) {
            const int tok32 = (tile >> 1) * 32 + tau * 2 + (tile & 1);
            st16(rs, WS_ACT + l16 + (tok32 * 112 + kap) * 1024, v4u{opk[0], opk[1], opk[2], opk[3]});
        };
#pragma unroll
        for (int n = 0; n < PD; n++) issue(n);
        SB();
        static_for<0, 112>([&](auto N_) __attribute__((always_inline)) {
            constexpr int n = decltype(N_)::v;
            constexpr int tile = n / 28, j = n % 28;
            if (n == 52) {
                STAMP(27);
                wait_vm<g1_cum(52) - g1_cum(HB_N + 8)>();
                BARRIER();
                if (PRIO_G) __builtin_amdgcn_s_setprio(1);
                STAMP(28);
            }
            if (n + PD < 112) issue(n + PD);
            if (j == 0) {
                f32x16 z;
#pragma unroll
                for (int i = 0; i < 16; i++) z[i] = 0.f;
                acc[tile & 1] = mfma_bf16(wf[0], fr[n % (PD + 1)], z);
            } else {
                acc[tile & 1] = mfma_bf16(wf[j], fr[n % (PD + 1)], acc[tile & 1]);
            }
            if (MFMA_FIRST) SB();
            if constexpr (n < 28) {
                constexpr int sk = n;
                if (sk < 21 && sk % 3 == 0) { if (sk > 0) { stat_acc(sk / 3 - 1, 1); SB(); } sf = lds_tr2(as + (sk / 3) * 2048, as + (sk / 3) * 2048 + 512); }
                if (sk < 21 && sk % 3 == 2) stat_acc(sk / 3, 0);
                if (sk == 21) stat_acc(6, 1);
                if (sk == 22) stat_write(0);
            }
            if (n == HBP_N) hfl = ld4(rs, hf_v, hf_s + 8192);
            if (n == HB_N) {
                PIN(acc[0]);
                SB();
                STAMP(24);
                wait_flags(rs, hf_v, hf_s + 8192, hfl, tag);
                STAMP(25);
            }
            if (n == 48) hr[0] = lds_tr(hra);
            if (n == 27) { STAMP(26); BARRIER_RAW(); STAMP(29); }
            if (n == 27 || n == 55) epi_read(tile);
            if (n == 84) {
                PIN(acc[0]); PIN(acc[1]);
                SB();
                STAMP(30);
                if (!__all(sg[1] == tag && sg[3] == tag)) {
                    int spins = SPIN_CAP;
#pragma unroll 1
                    do {
                        __builtin_amdgcn_s_sleep(2);
                        sg = ld16(rs, WS_SG + col * 16, tau * 512);
                    } while (!__all(sg[1] == tag && sg[3] == tag) && --spins > 0);
                }
                STAMP(31);
            }
            if (tile == 1) {
                if (j == 0) cs_read(0);
                if (j == 1) epi_stats();
                if (j >= 2 && j < 27) slice(acc[0], j - 2);
                if (j == 11) { SB(); cs_read(1); }
            }
            if (tile == 2) {
                if (j == 0) cs_read(0);
                if (j == 1) epi_stats();
                if (j >= 2 && j < 14) { slice(acc[1], 2 * (j - 2)); if (j == 6) { slice(acc[1], 9); SB(); cs_read(1); } else slice(acc[1], 2 * (j - 2) + 1); }
                if (j == 14) slice(acc[1], 24);
            }
            if (tile == 3) {
                if (j == 0) cs_read(0);
                if (j == 1) epi_stats2(sg[0]);
                if (j >= 2 && j < 27) slice(acc[0], j - 2);
                if (j == 11) { SB(); cs_read(1); }
            }
            constexpr int op = g1_op(n);
            if constexpr (op >= 0 && op < 28) w2_load(op);
            if constexpr (op >= 32 && op < 40) load_h(1, op - 32);
            if constexpr (op >= 40 && op < 50) epi_store(op - 40);
            if constexpr (op == 50) { wait_vm<g1_cum(AF_N) - g1_cum(72)>(); st4(rs, flv, af_set, tag); if (PRIO_G) __builtin_amdgcn_s_setprio(0); if (BAR_AF) BARRIER_RAW(); }
            if constexpr (op == 51) afl = ld4(rs, af_v, af_s);
            if constexpr (op == 52) st4(rs, (tid == 0 && del == 0) ? 0 : OOB, WS_TR + ((xcd & 1) * 16 + tau) * 128, tag);
            if constexpr (op == 53) sg = ld16(rs, WS_SG + col * 16, tau * 512);
            SB();
        });
        PIN(acc[1]);
        STAMP(10);
        wait_flags(rs, af_v, af_s, afl, tag);
        STAMP(11);
#pragma unroll
        for (int n = 0; n < (A0_LATE ? 0 : 7); n++) { a0[n] = ld16(rs, av, rot14[n]); SB(); }
        cs_read(0);
        epi_stats2(sg[2]);
        epi_four(acc[1], 0);
        cs_read(1);
        epi_four(acc[1], 1);
        epi_store(3);
        if (OPT) { SB(); st4(rs, flv, af_set + 1024, tag); }
    }
    SB();
#pragma unroll
    for (int n = (A0_LATE ? 0 : 7); n < 14; n++) { a0[n] = ld16(rs, av, rot14[n]); SB(); }
    hr[1] = lds_tr(hra + 57344);
#pragma unroll
    for (int f = 10; f < 21; f++) w2[f] = *(LAS v4u*)(sm + w2_lds(f) + l16);
    STAMP(12);
    BARRIER();
    f32x16 acc[2][2];
#pragma unroll
    for (int i = 0; i < 16; i++) { acc[0][0][i] = 0.f; acc[0][1][i] = 0.f; acc[1][0][i] = 0.f; acc[1][1][i] = 0.f; }
    LAS char* rw = opaque(sm + wave * 1024 + l16);
    LAS char* rr = opaque(sm + wave * 8192 + l16);
    auto red_write = [&](int hf) __attribute__((always_inline)) {
#pragma unroll
        for (int o = 0; o < 8; o++) {
            const f32x16& t = acc[o >> 2][hf];
            const int r = 4 * (o & 3);
            *(LAS f32x4*)(rw + hf * 65536 + o * 8192) = f32x4{t[r], t[r + 1], t[r + 2], t[r + 3]};
        }
    };
    auto red_finish = [&](int hf) __attribute__((always_inline)) {
        f32x4 v[8];
#pragma unroll
        for (int s = 0; s < 8; s++) v[s] = *(LAS f32x4*)(rr + hf * 65536 + s * 1024);
        const f32x4 t = ((v[0] + v[1]) + (v[2] + v[3])) + ((v[4] + v[5]) + (v[6] + v[7]));
        const u32 ta = pk_bf16(t[0], t[1]), tb = pk_bf16(t[2], t[3]);
        const int tok = 1024 * hf + 32 * tau2 + col;
        *(v2u*)(p.out + (size_t)tok * D + dd) = v2u{pk_bf16(bflo(ta) + bflo(hr[hf][0]), bfhi(ta) + bfhi(hr[hf][0])),
                                                    pk_bf16(bflo(tb) + bflo(hr[hf][1]), bfhi(tb) + bfhi(hr[hf][1]))};
    };
#pragma unroll
    for (int n = 0; n < 14; n++) {
        acc[0][0] = mfma_bf16(w2[2 * n], a0[n], acc[0][0]);
        acc[1][0] = mfma_bf16(w2[2 * n + 1], a0[n], acc[1][0]);
        if (!OPT && n == (AFB_EARLY ? 1 : 6)) { wait_vm<A0_LATE ? 12 : 7>(); st4(rs, flv, af_set + 1024, tag); }
        if (TAIL && n == AFBP_N) afl = ld4(rs, af_v, af_s + 1024);
        SB();
    }
    PIN(acc[0][0]); PIN(acc[1][0]);
    STAMP(13);
    red_write(0);
    BARRIER();
    afl = ld4(rs, af_v, af_s + 1024);
    SB();
    red_finish(0);
    SB();
    wait_flags(rs, af_v, af_s + 1024, afl, tag);
    STAMP(14);
#pragma unroll
    for (int n = 0; n < 14; n++) { a1[n] = ld16(rs, av + 32 * 112 * 1024, rot14[n]); SB(); }
    SB();
    STAMP(15);
#pragma unroll
    for (int n = 0; n < 14; n++) {
        acc[0][1] = mfma_bf16(w2[2 * n], a1[n], acc[0][1]);
        acc[1][1] = mfma_bf16(w2[2 * n + 1], a1[n], acc[1][1]);
        SB();
    }
    STAMP(16);
    red_write(1);
    BARRIER();
    if (TAIL == 2) red_finish(0);
    red_finish(1);
    STAMP(17);
    STAMP_FLUSH();
    asm volatile(".Lfb_code_end:");
}

}

extern "C" void kernel_launch(void* const* d_in, const int* in_sizes, int n_in,
                              void* d_out, int out_size, void* d_ws, size_t ws_size,
                              hipStream_t stream) {
    Params p;
    p.x = (const u16*)d_in[0]; p.Are = (const u16*)d_in[1]; p.Aim = (const u16*)d_in[2];
    p.Bre = (const u16*)d_in[3]; p.Bim = (const u16*)d_in[4]; p.Cre = (const u16*)d_in[5]; p.Cim = (const u16*)d_in[6];
    p.logdt = (const u16*)d_in[7]; p.Dskip = (const u16*)d_in[8];
    p.wg = (const u16*)d_in[10]; p.wu = (const u16*)d_in[11]; p.wd = (const u16*)d_in[12];
    p.ws = (char*)d_ws; p.out = (u16*)d_out;
    (void)hipFuncSetAttribute((const void*)fused_block, hipFuncAttributeMaxDynamicSharedMemorySize, LDS_BYTES);
    fused_block<<<256, 512, LDS_BYTES, stream>>>(p);
}
```

```cpp
#include <hip/hip_runtime.h>
#include <stdint.h>

namespace {

constexpr int L = 2048, D = 448, N = 256, F = 1792;

typedef unsigned short u16;
typedef unsigned int u32;
typedef unsigned long long u64;
typedef u32 v4u __attribute__((ext_vector_type(4)));
typedef u32 v2u __attribute__((ext_vector_type(2)));
typedef float f32x2 __attribute__((ext_vector_type(2)));
typedef float f32x4 __attribute__((ext_vector_type(4)));
typedef float f32x16 __attribute__((ext_vector_type(16)));
typedef _Float16 f16x2 __attribute__((ext_vector_type(2)));
typedef _Float16 f16x8 __attribute__((ext_vector_type(8)));
typedef __bf16 bf16x2 __attribute__((ext_vector_type(2)));
typedef __bf16 bf16x8 __attribute__((ext_vector_type(8)));
typedef short s16x4 __attribute__((ext_vector_type(4)));
#define GAS __attribute__((address_space(1)))
#define LAS __attribute__((address_space(3)))
#define RLX_AGENT __ATOMIC_RELAXED, __HIP_MEMORY_SCOPE_AGENT
#define SB() __builtin_amdgcn_sched_barrier(0)
#define PIN(x) asm volatile("" : "+v"(x))
#define BARRIER() asm volatile("s_waitcnt lgkmcnt(0)\n\ts_barrier" ::: "memory")
#define BARRIER_RAW() asm volatile("s_barrier" ::: "memory")
constexpr int SC1 = 16;

constexpr int WS_EPOCH = 0;
constexpr int WS_WF2 = 1024;
constexpr int WS_TR = 4096;
constexpr int WS_HF = 8192;
constexpr int WS_AF = WS_HF + 64 * 256;
constexpr int WS_SG = 57344;
constexpr int WS_WF1 = 65536;
constexpr int WS_CS = 65536 + 8192;
constexpr int WS_ST1 = WS_CS + 32768;
constexpr int WS_XX = WS_ST1 + 16384;
constexpr int WS_W1T = WS_XX + 224 * 16384;
constexpr int WS_W2T = WS_W1T + 112 * 28 * 1024;
constexpr int WS_HT = WS_W2T + 14 * 112 * 1024;
constexpr int WS_ACT = WS_HT + D * L * 2;
constexpr int WS_DBG = WS_ACT + 64 * 112 * 1024;
constexpr int NSTAMP = 32;
constexpr int WS_END = WS_DBG + 256 * NSTAMP * 8;
constexpr int SPIN_CAP = 1 << 16;
static_assert(WS_AF + 16 * 2 * 8 * 128 <= WS_SG, "flags overlap");

#define STAMP(k) do {} while (0)
#define STAMP_FLUSH() do {} while (0)
#define STAMP_DECL() do {} while (0)

__device__ __forceinline__ float bflo(u32 v) { return __builtin_bit_cast(float, v << 16); }
__device__ __forceinline__ float bfhi(u32 v) { return __builtin_bit_cast(float, v & 0xffff0000u); }
__device__ __forceinline__ u32 pk_bf16(float a, float b) { f32x2 f = {a, b}; return __builtin_bit_cast(u32, __builtin_convertvector(f, bf16x2)); }
__device__ __forceinline__ u32 pk_bf16s(float a, float b) { asm("" : "+v"(a)); asm("" : "+v"(b)); return pk_bf16(a, b); }
__device__ __forceinline__ u32 pk_f16(float a, float b) { f32x2 f = {a, b}; return __builtin_bit_cast(u32, __builtin_convertvector(f, f16x2)); }
__device__ __forceinline__ float rbf(float a) { return bflo(pk_bf16(a, 0.f)); }
__device__ __forceinline__ float dot2bf(u32 a, u32 b, float c) {
    return __builtin_amdgcn_fdot2_f32_bf16(__builtin_bit_cast(bf16x2, a), __builtin_bit_cast(bf16x2, b), c, false);
}
__device__ __forceinline__ f32x16 mfma_bf16(v4u a, v4u b, f32x16 c) {
    return __builtin_amdgcn_mfma_f32_32x32x16_bf16(__builtin_bit_cast(bf16x8, a), __builtin_bit_cast(bf16x8, b), c, 0, 0, 0);
}
__device__ __forceinline__ f32x16 mfma_f16(v4u a, v4u b, f32x16 c) {
    return __builtin_amdgcn_mfma_f32_32x32x16_f16(__builtin_bit_cast(f16x8, a), __builtin_bit_cast(f16x8, b), c, 0, 0, 0);
}
__device__ __forceinline__ v2u lds_tr(LAS char* p) {
    return __builtin_bit_cast(v2u, __builtin_amdgcn_ds_read_tr16_b64_v4i16((LAS s16x4*)p));
}
__device__ __forceinline__ v4u lds_tr2(LAS char* p0, LAS char* p1) {
    const v2u lo = lds_tr(p0), hi = lds_tr(p1);
    return v4u{lo[0], lo[1], hi[0], hi[1]};
}
template <int CTRL> __device__ __forceinline__ float dpp(float v) {
    return __builtin_bit_cast(float, __builtin_amdgcn_update_dpp(0, __builtin_bit_cast(int, v), CTRL, 0xf, 0xf, true));
}
__device__ __forceinline__ float rdl(float v, int l) { return __builtin_bit_cast(float, __builtin_amdgcn_readlane(__builtin_bit_cast(int, v), l)); }
__device__ __forceinline__ float wave_sum(float v) {
    v += dpp<0xB1>(v); v += dpp<0x4E>(v); v += dpp<0x141>(v); v += dpp<0x140>(v);
    return (rdl(v, 0) + rdl(v, 16)) + (rdl(v, 32) + rdl(v, 48));
}
__device__ __forceinline__ float wave_max(float v) {
    v = fmaxf(v, dpp<0xB1>(v)); v = fmaxf(v, dpp<0x4E>(v)); v = fmaxf(v, dpp<0x141>(v)); v = fmaxf(v, dpp<0x140>(v));
    return fmaxf(fmaxf(rdl(v, 0), rdl(v, 16)), fmaxf(rdl(v, 32), rdl(v, 48)));
}
__device__ __forceinline__ float half_sum(float v) {
    const auto r = __builtin_amdgcn_permlane32_swap(__builtin_bit_cast(u32, v), __builtin_bit_cast(u32, v), false, false);
    return __builtin_bit_cast(float, (u32)r[0]) + __builtin_bit_cast(float, (u32)r[1]);
}
template <int I> struct IC { static constexpr int v = I; };
template <int B, int E, class Fn> __device__ __forceinline__ void static_for(Fn&& f) {
    if constexpr (B < E) { f(IC<B>{}); static_for<B + 1, E>(f); }
}

typedef f32x2 cf;
__device__ __forceinline__ cf rot(cf b) { return cf{-b.y, b.x}; }
__device__ __forceinline__ cf cmul(cf a, cf b) { return __builtin_elementwise_fma(a.yy, rot(b), a.xx * b); }
__device__ __forceinline__ cf cmulc(cf a, cf b) { return __builtin_elementwise_fma(a.yy, b.yx, a.xx * rot(b).yx); }
__device__ __forceinline__ u32 pkh(cf a) { return __builtin_bit_cast(u32, __builtin_convertvector(a, f16x2)); }

typedef __amdgpu_buffer_rsrc_t rsrc_t;
__device__ __forceinline__ v4u ld16(rsrc_t r, int voff, int soff) { return __builtin_amdgcn_raw_buffer_load_b128(r, voff, soff, SC1); }
__device__ __forceinline__ u32 ld4(rsrc_t r, int voff, int soff) { return __builtin_amdgcn_raw_buffer_load_b32(r, voff, soff, SC1); }
__device__ __forceinline__ void st16(rsrc_t r, int voff, v4u v) { __builtin_amdgcn_raw_buffer_store_b128(v, r, voff, 0, SC1); }
__device__ __forceinline__ void st8(rsrc_t r, int voff, int soff, v2u v) { __builtin_amdgcn_raw_buffer_store_b64(v, r, voff, soff, SC1); }
__device__ __forceinline__ void st4(rsrc_t r, int voff, int soff, u32 v) { __builtin_amdgcn_raw_buffer_store_b32(v, r, voff, soff, SC1); }
template <int N> __device__ __forceinline__ void wait_vm() { asm volatile("s_waitcnt vmcnt(%0)" :: "i"(N) : "memory"); }
constexpr int OOB = 0x7f000000;

__device__ __forceinline__ void wait_flags(rsrc_t rs, int voff, int soff, u32 v, u32 tag) {
    if (!__all(v == tag)) {
        int spins = SPIN_CAP;
#pragma unroll 1
        do {
            __builtin_amdgcn_s_sleep(1);
            v = ld4(rs, voff, soff);
        } while (!__all(v == tag) && --spins > 0);
    }
    __builtin_amdgcn_fence(__ATOMIC_ACQUIRE, "wavefront");
}
__device__ __forceinline__ void glds16(v4u srd, int voff, int soff, u32 lds_dst) {
    asm volatile("s_mov_b32 m0, %3\n\ts_nop 0\n\tbuffer_load_dwordx4 %0, %1, %2 offen sc1 lds"
                 :: "v"(voff), "s"(srd), "s"(soff), "s"(lds_dst) : "memory");
}
__device__ __forceinline__ LAS char* opaque(LAS char* q) { u32 a = (u32)(size_t)q; asm volatile("" : "+v"(a)); return (LAS char*)(size_t)a; }

struct Params {
    const u16 *x, *Are, *Aim, *Bre, *Bim, *Cre, *Cim, *logdt, *Dskip, *wg, *wu, *wd;
    char* ws;
    u16* out;
};

constexpr int XY_STRIDE = 272, XY_ROWS = 59, XY_WAVE = XY_STRIDE * XY_ROWS;
constexpr int P_WAVE = 33 * 144;
constexpr int LD_PB = 0, LD_PA = 38144;
constexpr int LD_HA = 38144, LD_HB = LD_HA + 57344;
constexpr int LD_XU = LD_HB;
constexpr int LD_KP = 128512;
constexpr int LD_U = LD_KP + 8 * 1536;
constexpr int LD_SB = LD_HB + 57344;
constexpr int LD_ST = LD_SB + 64;
constexpr int LD_CS = LD_ST + 4096;
constexpr int LDS_BYTES = LD_CS + 1024;
static_assert(LD_U + 2 * 4160 <= LD_SB && LDS_BYTES <= 163840, "lds map");

__device__ __forceinline__ v4u tr_piece(const v4u (&in)[8], int c) {
    v4u o;
#pragma unroll
    for (int kp = 0; kp < 4; kp++)
        o[kp] = __builtin_amdgcn_perm(in[2 * kp + 1][c >> 1], in[2 * kp][c >> 1], (c & 1) ? 0x07060302u : 0x05040100u);
    return o;
}
__device__ __forceinline__ void w1_task_load(const Params& p, int task, int lane, v2u (&in)[8]) {
    const int t = (task < 0 ? 0 : task) >> 1, c4 = task & 1;
    const int phi = t / 28, r = t - phi * 28, mat = r >= 14, kp = r - 14 * mat;
    const rsrc_t rw = __builtin_amdgcn_make_buffer_rsrc((void*)(mat ? p.wu : p.wg), 0, task < 0 ? 0 : D * F * 2, 0x00020000);
    const int voff = (8 * (lane >> 4) * F + (lane & 15) * 8) * 2;
#pragma unroll
    for (int k = 0; k < 8; k++) in[k] = __builtin_amdgcn_raw_buffer_load_b64(rw, voff + (k & 1) * F * 2, ((32 * kp + (k & 6)) * F + phi * 128 + 4 * c4) * 2, 0);
}
__device__ __forceinline__ void w1_task_store(rsrc_t rs, int task, int lane, const v2u (&in)[8]) {
    const int t = task >> 1, c4 = task & 1;
    const int phi = t / 28, r = t - phi * 28, mat = r >= 14, kp = r - 14 * mat;
    const int voff = WS_W1T + ((phi * 8 + 4 * c4) * 28 + 2 * kp) * 1024 + mat * 256 + (lane >> 4) * 512 + (lane & 15) * 16;
#pragma unroll
    for (int c = 0; c < 4; c++) {
        v4u o;
#pragma unroll
        for (int kp2 = 0; kp2 < 4; kp2++) o[kp2] = __builtin_amdgcn_perm(in[2 * kp2 + 1][c >> 1], in[2 * kp2][c >> 1], (c & 1) ? 0x07060302u : 0x05040100u);
        st16(rs, voff + c * 28 * 1024, o);
    }
}
__device__ __forceinline__ int w1_flag(int task) { const int t = task >> 1, phi = t / 28; return WS_WF1 + (phi * 2 + (task & 1)) * 128 + (t - phi * 28) * 4; }
__device__ __forceinline__ void w2_task(const Params& p, rsrc_t rs, int task, int lane, LAS char* scr) {
    const int del = task / 28, kg = task - del * 28;
    const int k4 = (lane >> 3) & 3, ks = kg * 4 + k4, h = lane >> 5, dr = lane & 7;
    const u16* src = p.wd + (size_t)((ks >> 3) * 128 + 32 * h + (ks & 7)) * D + del * 64 + dr * 8;
    v4u in[8];
#pragma unroll
    for (int j = 0; j < 8; j++) in[j] = __builtin_nontemporal_load((const v4u*)(src + (size_t)(8 * ((j & 3) + 8 * (j >> 2))) * D));
    LAS char* w = scr + ((dr >> 2) * 4 + k4) * 1024 + (32 * h + 8 * (dr & 3)) * 16;
#pragma unroll
    for (int c = 0; c < 8; c++) *(LAS v4u*)(w + c * 16) = tr_piece(in, c);
    const int voff = WS_W2T + (del * 2 * 112 + kg * 4) * 1024 + lane * 16;
#pragma unroll
    for (int f = 0; f < 8; f++) st16(rs, voff + ((f >> 2) * 112 + (f & 3)) * 1024, *(LAS v4u*)(scr + f * 1024 + lane * 16));
}

#define W1_CHK 2
#define KPRE 32
#define W1_NUM 2
#define W1_DEN 5
#define W1_BURST 4
#define PF1 0
#define PF2 1
#define GR_EARLY 3
#define CS_T 520
#define GR_T 320
#define AFB_EARLY 1
#define W1_ORDER 1
#define PRIO_C 1
constexpr int w1_cnt(int n) { return n <= W1_CHK ? 0 : W1_BURST + ((n - W1_CHK - 1) * W1_NUM) / W1_DEN > 28 ? 28 : W1_BURST + ((n - W1_CHK - 1) * W1_NUM) / W1_DEN; }
#define PRIO_A 0
#define CS_FIRST 1
#define HBP_N 10
#define W2_N0 34
#define PRIO_G 1
#define BAR_AF 1
#define OPT 1
#define AFBP_N 13
#define TAIL 2
#define ROT_H 0
#define HF_OPT 0
#define HFL_EARLY 0
#define WFL_N -1
#define AFB_N 4
#define SG_N 71
#define A0_LATE 0
#define MFMA_FIRST 1
#define HB_N 24
#define ROT_W1 0
#define ROT_W2 0
#define AF_N 79
#define HF_N 37
#define AFP_N 90
#define A0_IN 7
#define AFC_N 100
constexpr int ST1_N = 67;
constexpr int g1_op(int n) {
    return (n > HB_N && n <= HB_N + 7) ? 32 + (n - HB_N - 1)
         : n == 55 ? 40 : n == ST1_N ? 41 : n == 111 ? 42 : n == AF_N ? 50 : n == AFP_N ? 51 : n == 33 ? 52 : n == SG_N ? 53
         : (n >= 1 && n < 21 && (n & 1) == 1) ? (n - 1) / 2
         : (n >= 36 && n < 52 && (n & 3) == 0) ? 10 + (n - 36) / 4
         : (n == 54 || (n >= 68 && n < 80 && (n & 1) == 0)) ? (n == 54 ? 14 : 15 + (n - 68) / 2)
         : (n >= 85 && n < 99 && (n & 1) == 1) ? 21 + (n - 85) / 2
         : (n > AFC_N && n <= AFC_N + A0_IN) ? 60 + (n - AFC_N - 1)
         : -1;
}
constexpr bool g1_all() { int m = 0; for (int i = 0; i < 112; i++) if (g1_op(i) >= 0 && g1_op(i) < 28) m |= 1 << g1_op(i); return m == (1 << 28) - 1; }
static_assert(g1_all(), "every W2 fragment is requested");
constexpr int g1_cum(int n) { int c = 0; for (int i = 0; i < n; i++) c += g1_op(i) >= 0; return c; }
static_assert(HB_N >= 23 && HB_N < 28 && AF_N > ST1_N && AFP_N > AF_N && AFC_N > AFP_N && AFC_N >= 99 && AFC_N + A0_IN < 111, "schedule");
#define W1_MID 6
#define CODE_PF 23552

__global__ __launch_bounds__(512, 2) void fused_block(Params p) {
    extern __shared__ __attribute__((aligned(16))) char smem_[];
    LAS char* sm = (LAS char*)smem_;
    const int tid = threadIdx.x, lane = tid & 63;
    const int wave = __builtin_amdgcn_readfirstlane(tid >> 6);
    const int b = blockIdx.x;
    STAMP_DECL();
    const bool main_blk = b < 224;
    const int xcd = b & 7, i28 = b >> 3;
    u64 pc0; u32 clen;
    asm volatile("s_getpc_b64 %0\n.Lfb_code_begin:\n\ts_mov_b32 %1, .Lfb_code_end-.Lfb_code_begin" : "=s"(pc0), "=s"(clen));
    const u32 cpf = __builtin_amdgcn_raw_buffer_load_b32(__builtin_amdgcn_make_buffer_rsrc((void*)pc0, 0, main_blk ? 0 : clen, 0x00020000),
                                                         lane < 16 ? lane * 64 : OOB, (wave * 4 + (i28 & 3)) * 1024, 0);
    const int l16 = lane * 16;
    const int rbase = 16 * (b >> 1) + 2 * (b & 1);
    int x2v = lane < 56 ? (lane & 3) * (4 * D * 2) + (lane >> 2) * 8 : OOB, xrv = lane < 56 ? l16 : OOB, t2 = tid * 2;
    int x2s = (rbase * D + 56 * wave) * 2, xrs = (rbase + 4 * (wave >> 1) + (wave & 1)) * D * 2;
    int pair = xcd * 28 + i28;
    asm volatile("" : "+v"(x2v), "+v"(xrv), "+v"(t2), "+s"(x2s), "+s"(xrs), "+s"(pair));
    SB();
    const rsrc_t rs = __builtin_amdgcn_make_buffer_rsrc(p.ws, 0, WS_END, 0x00020000);
    asm volatile("" :: "s"(p.x), "s"(p.wd), "s"(p.ws));
    if (PRIO_A && wave >= 4) __builtin_amdgcn_s_setprio(1);
    STAMP(0);
    const u32 tstart = GR_EARLY == 3 ? (u32)__builtin_amdgcn_s_memrealtime() : 0;
    const u32 tagv = ld4(rs, WS_EPOCH, 0);

    const rsrc_t rx = __builtin_amdgcn_make_buffer_rsrc((void*)p.x, 0, L * D * 2, 0x00020000);
    const v2u xa = __builtin_amdgcn_raw_buffer_load_b64(rx, x2v, x2s, 2);
    const v2u xb = __builtin_amdgcn_raw_buffer_load_b64(rx, x2v + D * 2, x2s, 2);
    const v4u xr = __builtin_amdgcn_raw_buffer_load_b128(rx, xrv, xrs, 2);
    SB();
    const int ch = wave >> 2, wq = wave & 3;
    const int d0 = 2 * pair;
    const int psz = main_blk ? D * N * 2 : 0;
    auto ldp = [&](const u16* q) __attribute__((always_inline)) -> u32 {
        return __builtin_amdgcn_raw_buffer_load_b16(__builtin_amdgcn_make_buffer_rsrc((void*)q, 0, psz, 0x00020000), t2, d0 * N * 2, 2);
    };
    const u32 r_ldt = __builtin_amdgcn_raw_buffer_load_b32(__builtin_amdgcn_make_buffer_rsrc((void*)p.logdt, 0, main_blk ? D * 2 : 0, 0x00020000), 0, d0 * 2, 0);
    const u32 r_are = ldp(p.Are), r_aim = ldp(p.Aim), r_bre = ldp(p.Bre), r_bim = ldp(p.Bim), r_cre = ldp(p.Cre), r_cim = ldp(p.Cim);
    const u32 r_dsk = __builtin_amdgcn_raw_buffer_load_b32(__builtin_amdgcn_make_buffer_rsrc((void*)p.Dskip, 0, main_blk ? D * 2 : 0, 0x00020000), 0, d0 * 2, 0);
    const int hw = (b - 224) + 32 * wave;
    const int w1task = !main_blk ? -1 : wave < 2 ? pair * 2 + wave : (wave < 4 && pair < 168) ? 448 + pair * 2 + wave - 2 : -1;
    SB();
    const int h = lane >> 5, col = lane & 31;
    const int G1 = (lane >> 4) & 1, q4 = (lane & 15) >> 2, pp = lane & 3;
    int xxv = lane < 56 ? WS_XX + ((28 * wave + 2 * (lane >> 2)) * 2 + (b & 1)) * 8192 + (4 * (b >> 1) + (lane & 3)) * 16 : OOB;
    int flv = lane == 0 ? 0 : OOB;
    asm volatile("" : "+v"(xxv), "+v"(flv));
    LAS char* xy = sm + wave * XY_WAVE;
    LAS char* xyw = opaque(xy + lane * 4);
    LAS char* pa = opaque(xy + (32 + (int)((0x1713100d0a080400ull >> (8 * (col >> 2))) & 255) + (col & 3)) * XY_STRIDE + h * 16);
    LAS char* pb = opaque(xy + col * XY_STRIDE + h * 16);
    LAS char* exw = opaque(xy + l16);
    LAS char* exr = opaque(sm + (ch * 4) * XY_WAVE + wq * 1024 + l16);
    LAS char* kpw = opaque(sm + LD_KP + ch * 4 * 1536 + 512 + (wq < 2 ? 64 * wq + 2 * col + h : wq == 2 ? 64 * (2 + h) + 2 * col : 64 * (4 + 2 * h) + 2 * col) * 2);
    LAS char* uw = opaque(sm + LD_U + 64 + tid * 8);
    LAS char* ua = opaque(sm + LD_U + ch * 4160 + 2 * (32 + 32 + 16 * G1 - 32 * h - 16 - 4 * q4 + 4 * pp));
    LAS char* ka = opaque(sm + LD_KP + wave * 1536 + 2 * (256 - 248 + 8 * col + 8 * h));
    LAS char* pw = opaque(sm + wave * P_WAVE + (col + 1) * 144 + h * 16);
    *(LAS v2u*)(sm + LD_KP + wave * 1536 + lane * 8) = v2u{0, 0};
    if (tid < 32) *(LAS u32*)(sm + LD_U + (tid >> 4) * 4160 + (tid & 15) * 4) = 0;
    SB();
    const u32 tag = __builtin_amdgcn_readfirstlane(tagv) + 1u;
    st16(rs, xxv, v4u{xa[0], tag, xb[0], tag});
    st16(rs, xxv + 16384, v4u{xa[1], tag, xb[1], tag});
    SB();
    {
        float a = 0.f, q = 0.f;
#pragma unroll
        for (int i = 0; i < 4; i++) { a = dot2bf(xr[i], 0x3f803f80u, a); q = dot2bf(xr[i], xr[i], q); }
        const float mean = wave_sum(a) * (1.f / D), ex2 = wave_sum(q) * (1.f / D);
        const float var = rbf(fmaxf(ex2 - mean * mean, 0.f));
        const float sd = __builtin_amdgcn_sqrtf(rbf(var + 1e-5f));
        st8(rs, flv, WS_ST1 + (((b & 1) * 512 + 4 * (b >> 1) + (wave >> 1)) * 2 + (wave & 1)) * 8, v2u{pk_bf16(mean, sd), tag});
    }
    SB();
    v2u tin[8];
    w1_task_load(p, w1task, lane, tin);
    SB();
    STAMP(1);
    const int tau = main_blk ? (xcd >> 1) * 4 + (i28 & 3) : (b & 15), phi = (xcd & 1) * 7 + (i28 >> 2);
    const int del = i28 >> 2, tau2 = 2 * tau + (xcd & 1);
    const int trl = q4 * 128 + 8 * (pp & 1) + 16 * (2 * G1 + (pp >> 1)), sw = (q4 & 2) << 5;
    const int hf_v = (lane < 56 ? lane : 0) * 4, hf_s = WS_HF + ((tau >> 2) * 8 + wave) * 256;
    const v4u srd = {(u32)(size_t)p.ws, (u32)((size_t)p.ws >> 32) & 0xffffu, (u32)WS_END, 0x00020000u};
    const int hvoff = WS_HT + ((8 * wave + (lane >> 3)) * L + tau * 64) * 2 + (((lane & 7) ^ (((lane >> 3) & 2) << 1)) << 4);
    if (!main_blk) {
        BARRIER_RAW();
        const int w2task = hw;
        if (w2task < 196) {
            w2_task(p, rs, w2task, lane, sm + LD_HB + wave * 8192);
            wait_vm<0>();
            st4(rs, flv, WS_WF2 + (w2task / 28) * 128 + (w2task % 28) * 4, tag);
        }
        asm volatile("" :: "v"(cpf), "v"(tin[7]));
        STAMP(2);
        const int unit = 255 - hw;
        if (unit < 112) {
            while ((u32)__builtin_amdgcn_s_memrealtime() - tstart < CS_T) __builtin_amdgcn_s_sleep(8);
            const int fv = (col < 28 ? col : 0) * 4, fs = WS_WF1 + ((unit >> 3) * 2 + ((unit >> 2) & 1)) * 128;
            wait_flags(rs, fv, fs, ld4(rs, fv, fs), tag);
            float c0 = 0.f, c1 = 0.f;
#pragma unroll 1
            for (int k0 = 0; k0 < 28; k0 += 14) {
                v4u w[14];
#pragma unroll
                for (int ks = 0; ks < 14; ks++) w[ks] = ld16(rs, l16 + (ks & 3) * 1024, WS_W1T + (unit * 28 + k0 + (ks & ~3)) * 1024);
#pragma unroll
                for (int ks = 0; ks < 14; ks++) {
                    c0 = dot2bf(w[ks][0], 0x3f803f80u, c0); c1 = dot2bf(w[ks][1], 0x3f803f80u, c1);
                    c0 = dot2bf(w[ks][2], 0x3f803f80u, c0); c1 = dot2bf(w[ks][3], 0x3f803f80u, c1);
                }
            }
            st8(rs, lane < 32 ? WS_CS + lane * 8 : OOB, unit * 256, v2u{__builtin_bit_cast(u32, half_sum(c0 + c1)), tag});
        }
        STAMP(3);
        {
            const int tl = (b >> 4) & 1;
            if (wave == 0) {
                int spins = SPIN_CAP;
#pragma unroll 1
                while (ld4(rs, 0, WS_TR + (b - 224) * 128) != tag && --spins > 0) __builtin_amdgcn_s_sleep(4);
            }
            BARRIER();
            if (wave < 7) {
                const int gv = WS_HT + ((lane >> 2) * L + 1024 + tau * 64 + tl * 32) * 2 + (lane & 3) * 16;
                LAS char* as = sm + 4096 + wave * 4096 + (8 * h + q4) * 64 + G1 * 32 + pp * 8;
#pragma unroll
                for (int n = 0; n < 4; n++) glds16(srd, gv, (wave * 4 + n) * 65536, (u32)(size_t)sm + 4096 + wave * 4096 + n * 1024);
                wait_vm<0>();
                v4u f[4];
#pragma unroll
                for (int k = 0; k < 4; k++) f[k] = lds_tr2(as + k * 1024, as + k * 1024 + 256);
                float s1 = 0.f, s2 = 0.f, s1b = 0.f, s2b = 0.f;
#pragma unroll
                for (int k = 0; k < 4; k++) {
                    s1 = dot2bf(f[k][0], 0x3f803f80u, s1); s2 = dot2bf(f[k][0], f[k][0], s2);
                    s1b = dot2bf(f[k][1], 0x3f803f80u, s1b); s2b = dot2bf(f[k][1], f[k][1], s2b);
                    s1 = dot2bf(f[k][2], 0x3f803f80u, s1); s2 = dot2bf(f[k][2], f[k][2], s2);
                    s1b = dot2bf(f[k][3], 0x3f803f80u, s1b); s2b = dot2bf(f[k][3], f[k][3], s2b);
                }
                *(LAS f32x2*)(sm + wave * 512 + lane * 8) = f32x2{s1 + s1b, s2 + s2b};
            }
            BARRIER();
            if (wave == 0) {
                f32x2 t = *(LAS f32x2*)(sm + lane * 8);
#pragma unroll
                for (int k = 1; k < 7; k++) t += *(LAS f32x2*)(sm + k * 512 + lane * 8);
                const float mean = half_sum(t.x) * (1.f / D), ex2 = half_sum(t.y) * (1.f / D);
                const float var = rbf(fmaxf(ex2 - mean * mean, 0.f));
                st8(rs, lane < 32 ? WS_SG + col * 16 + tl * 8 : OOB, tau * 512, v2u{pk_bf16(mean, __builtin_amdgcn_sqrtf(rbf(var + 1e-5f))), tag});
            }
        }
        STAMP(4);
        STAMP_FLUSH();
        return;
    }
    asm volatile("" :: "v"(r_are), "v"(r_aim), "v"(r_bre), "v"(r_bim), "v"(r_cre), "v"(r_cim), "v"(r_dsk), "v"(cpf));
    SB();

    float unscale;
    {
        const float dt = rbf(__expf(ch ? bfhi(r_ldt) : bflo(r_ldt)));
        const float ar = dt * bflo(r_are), ai = dt * bflo(r_aim);
        const float br = dt * bflo(r_bre), bi = dt * bflo(r_bim);
        const float cr = bflo(r_cre), ci = bflo(r_cim);
        cf cb = {cr * br - ci * bi, -(cr * bi + ci * br)};
        const float a2 = ar * 1.44269504088896340736f;
        const float rh = ai * 0.15915494309189535f;
        const float rl = __builtin_fmaf(ai, 0.15915494309189535f, -rh) + ai * 6.4206382e-9f;
        auto cexp = [&](float s) __attribute__((always_inline)) -> cf {
            const float e = __builtin_amdgcn_exp2f(a2 * s);
            const float r = __builtin_amdgcn_fractf(rh * s) + rl * s;
            return cf{__builtin_amdgcn_cosf(r), __builtin_amdgcn_sinf(r)} * e;
        };
        const float grow = fmaxf(1.f, __builtin_amdgcn_exp2f(a2 * 2048.f));
        const float sb = wave_sum((fabsf(cb.x) + fabsf(cb.y)) * grow);
        *(LAS float*)(sm + LD_SB + wave * 4) = sb;
        int ex = (int)((__builtin_bit_cast(u32, sb) >> 23) & 255);
        ex = min(max(ex, 30), 230);
        unscale = __builtin_bit_cast(float, (u32)(127 - 15 - 126 + ex) << 23);
        cb *= __builtin_bit_cast(float, (u32)(127 + 15 + 126 - ex) << 23);

        const cf e1 = cexp(1.f), e16 = cexp(16.f), e256 = cexp(256.f);
        const cf e2 = cmul(e1, e1), e32 = cmul(e16, e16), e512 = cmul(e256, e256), x1 = cmulc(cb, e1), x256 = cmulc(cb, e256);
        SB();
        const cf e4 = cmul(e2, e2), e64 = cmul(e32, e32), e1024 = cmul(e512, e512), x2 = cmulc(cb, e2), x3 = cmulc(x1, e2);
        const cf x512 = cmulc(cb, e512), x768 = cmulc(x256, e512), y6 = cmul(e16, e32);
        SB();
        const cf e8 = cmul(e4, e4), e128 = cmul(e64, e64);
        const cf x4 = cmulc(cb, e4), x5 = cmulc(x1, e4), x6 = cmulc(x2, e4), x7 = cmulc(x3, e4), x258 = cmulc(x2, e256);
        const cf x1024 = cmulc(cb, e1024), x1280 = cmulc(x256, e1024), x1536 = cmulc(x512, e1024), x1792 = cmulc(x768, e1024);
        SB();
        auto put = [&](int q, cf v) __attribute__((always_inline)) { *(LAS u32*)(xyw + q * XY_STRIDE) = pkh(v); };
        cf y[8];
        y[0] = cf{1.f, 0.f}; y[1] = e8; y[2] = e16; y[3] = cmul(e8, e16); y[4] = e32; y[5] = cmul(e8, e32); y[6] = y6;
        const cf e192 = cmul(e128, e64), x8 = cmulc(x4, e4), x260 = cmulc(x4, e256), x262 = cmulc(x6, e256), x516 = cmulc(x4, e512);
        put(32, cb); put(33, x1); put(34, x2); put(35, x3); put(36, x4); put(37, x5); put(38, x6); put(39, x7);
        put(40, x256); put(41, x258);
        SB();
        y[7] = cmul(y[3], e32);
        const cf x264 = cmulc(x8, e256), x520 = cmulc(x8, e512), x772 = cmulc(x260, e512), x1032 = cmulc(x8, e1024);
        cf t[8];
#pragma unroll
        for (int q = 1; q < 7; q++) t[q] = cmul(y[q], e64);
        put(42, x260); put(43, x262); put(45, x512); put(46, x516);
#pragma unroll
        for (int q = 0; q < 6; q++) put(q, y[q]);
        SB();
        t[7] = cmul(y[7], e64);
        const cf x776 = cmulc(x264, e512), x1288 = cmulc(x264, e1024), x1544 = cmulc(x520, e1024);
        cf t2[8];
#pragma unroll
        for (int q = 1; q < 8; q++) t2[q] = cmul(y[q], e128);
        put(6, y[6]); put(7, y[7]); put(8, e64);
#pragma unroll
        for (int q = 1; q < 6; q++) put(8 + q, t[q]);
        put(44, x264); put(47, x520); put(48, x768); put(49, x772); put(51, x1024); put(52, x1032);
        SB();

        const cf x1800 = cmulc(x776, e1024);
        cf t3[8];
#pragma unroll
        for (int q = 1; q < 8; q++) t3[q] = cmul(y[q], e192);
        put(14, t[6]); put(15, t[7]); put(16, e128);
#pragma unroll
        for (int q = 1; q < 8; q++) put(16 + q, t2[q]);
        put(50, x776); put(53, x1280); put(54, x1288); put(55, x1536); put(56, x1544); put(57, x1792);
        SB();
        put(24, e192);
#pragma unroll
        for (int q = 1; q < 8; q++) put(24 + q, t3[q]);
        put(58, x1800);
    }
    SB();
    STAMP(2);
    asm volatile("" :: "v"(tin[7]));
    if (w1task >= 0) w1_task_store(rs, w1task, lane, tin);
    SB();
    const int t16 = tid * 16;
    v4u gx[2], gs[2];
    auto gr_load = [&]() __attribute__((always_inline)) {
        gx[0] = ld16(rs, t16, WS_XX + pair * 16384); gx[1] = ld16(rs, t16, WS_XX + pair * 16384 + 8192);
        gs[0] = ld16(rs, t16, WS_ST1); gs[1] = ld16(rs, t16, WS_ST1 + 8192);
    };
    const bool gr_first = (u32)__builtin_amdgcn_s_memrealtime() - tstart >= GR_T;
    if (gr_first) gr_load();
    SB();
    {
        f32x16 kd;
#pragma unroll
        for (int i2 = 0; i2 < 16; i2++) kd[i2] = 0.f;
        v4u fa[8], fb[8];
#pragma unroll
        for (int s = 0; s < 8; s++) { fa[s] = *(LAS v4u*)(pa + s * 32); fb[s] = *(LAS v4u*)(pb + s * 32); }
        SB();
#pragma unroll
        for (int s = 0; s < 8; s++) kd = mfma_f16(fa[s], fb[s], kd);
        SB();

#pragma unroll
        for (int g2 = 0; g2 < 4; g2++)
            *(LAS f32x4*)(exw + g2 * 1024) = f32x4{kd[4 * g2], kd[4 * g2 + 1], kd[4 * g2 + 2], kd[4 * g2 + 3]} * unscale;
    }
    if (w1task >= 0) { wait_vm<0>(); st4(rs, flv, w1_flag(w1task), tag); }
    if (!gr_first) {
        while ((u32)__builtin_amdgcn_s_memrealtime() - tstart < GR_T) __builtin_amdgcn_s_sleep(2);
        gr_load();
    }
    SB();
    BARRIER();
    __builtin_amdgcn_s_setprio(0);
    if (GR_EARLY == 0) gr_load();
    const int l28 = (col < 28 ? col : 0) * 4;
    SB();
    STAMP(3);
    float ik;
    {
        const f32x4 s0 = *(LAS f32x4*)(sm + LD_SB + ch * 16);
        const f32x4 a0 = *(LAS f32x4*)(exr), a1 = *(LAS f32x4*)(exr + XY_WAVE), a2 = *(LAS f32x4*)(exr + 2 * XY_WAVE), a3 = *(LAS f32x4*)(exr + 3 * XY_WAVE);
        const float b0 = (s0[0] + s0[1]) + (s0[2] + s0[3]);
        int e0 = (int)((__builtin_bit_cast(u32, b0) >> 23) & 255);
        e0 = min(max(e0, 30), 230);
        ik = __builtin_bit_cast(float, (u32)(127 - 15 - 126 + e0) << 23);
        const float sk = __builtin_bit_cast(float, (u32)(127 + 15 + 126 - e0) << 23);
        const f32x4 v = ((a0 + a1) + (a2 + a3)) * sk;
        const u32 p01 = pk_f16(v[0], v[1]), p23 = pk_f16(v[2], v[3]);
        LAS char* o = kpw;
        if (wq == 0) {
            *(LAS u16*)(o) = (u16)p01; *(LAS u16*)(o + 1536) = (u16)(p01 >> 16);
            *(LAS u16*)(o + 2 * 1536) = (u16)p23; *(LAS u16*)(o + 3 * 1536) = (u16)(p23 >> 16);
        } else if (wq == 1) {
            *(LAS u16*)(o) = (u16)p01; *(LAS u16*)(o + 1536) = (u16)p01;
            *(LAS u16*)(o + 2 * 1536) = (u16)(p01 >> 16); *(LAS u16*)(o + 3 * 1536) = (u16)p23;
        } else if (wq == 2) {
            *(LAS u32*)(o) = p01; *(LAS u32*)(o + 1536) = p01;
            *(LAS u32*)(o + 2 * 1536) = (p01 & 0xffffu) | (p23 << 16);
            *(LAS u32*)(o + 3 * 1536) = (p01 >> 16) | (p23 << 16);
        } else {
            const bool ev = (col & 1) == 0;
            *(LAS u32*)(o) = ev ? ((p01 & 0xffffu) | (p01 << 16)) : p01;
            *(LAS u32*)(o + 1536) = p01; *(LAS u32*)(o + 2 * 1536) = p01; *(LAS u32*)(o + 3 * 1536) = p01;
            *(LAS u32*)(o + 128) = ev ? ((p23 & 0xffffu) | (p23 << 16)) : p23;
            *(LAS u32*)(o + 128 + 1536) = p23; *(LAS u32*)(o + 128 + 2 * 1536) = p23; *(LAS u32*)(o + 128 + 3 * 1536) = p23;
        }
    }
    SB();
    auto gr_ok = [&]() __attribute__((always_inline)) {
        const u32 a = (gx[0][1] ^ tag) | (gx[0][3] ^ tag) | (gx[1][1] ^ tag) | (gx[1][3] ^ tag);
        const u32 c = (gs[0][1] ^ tag) | (gs[0][3] ^ tag) | (gs[1][1] ^ tag) | (gs[1][3] ^ tag);
        return __all((a | c) == 0);
    };
    if (!gr_ok()) {
        int spins = SPIN_CAP;
#pragma unroll 1
        do {
            __builtin_amdgcn_s_sleep(4);
            gr_load();
        } while (!gr_ok() && --spins > 0);
    }
    STAMP(4);
    v4u wf[28];
    const int w1off = WS_W1T + (phi * 8 + wave) * 28 * 1024;
    int seg7[4];
#pragma unroll
    for (int i = 0; i < 4; i++) seg7[i] = ((i + i28 * ROT_W1) & 3) * 7;
    auto w1_load = [&](int ks) __attribute__((always_inline)) {
        wf[ks] = ld16(rs, l16 + ((ks % 7) & 3) * 1024, w1off + seg7[ks / 7] * 1024 + ((ks % 7) >> 2) * 4096);
    };
    const int wf_s = WS_WF1 + phi * 256;
    u32 wfl;
    if (WFL_N < 0) wfl = ld4(rs, l28 + h * 128, wf_s);
    SB();
    u32 xc[4], udk[4];
    {
        float u0[4], u1[4];
#pragma unroll
        for (int i = 0; i < 4; i++) {
            xc[i] = gx[i >> 1][2 * (i & 1)];
            const u32 st = gs[i >> 1][2 * (i & 1)];
            const float m = bflo(st), r = __builtin_amdgcn_rcpf(bfhi(st));
            const u32 dd = pk_bf16(bflo(xc[i]) - m, bfhi(xc[i]) - m);
            const u32 uu = pk_bf16(bflo(dd) * r, bfhi(dd) * r);
            u0[i] = bflo(uu); u1[i] = bfhi(uu);
        }
        *(LAS v2u*)(uw) = v2u{pk_f16(u0[0], u0[1]), pk_f16(u0[2], u0[3])};
        *(LAS v2u*)(uw + 4160) = v2u{pk_f16(u1[0], u1[1]), pk_f16(u1[2], u1[3])};
#pragma unroll
        for (int i = 0; i < 4; i++) udk[i] = pk_bf16(u0[i] * bflo(r_dsk), u1[i] * bfhi(r_dsk));
    }
    BARRIER();
    STAMP(5);
    f32x4 r0, r1, l3;
    f32x2 r2, l2;
    float l1, r3;
    v4u xu;
    u32 ya, yb;
    const int c1 = tid & 255;
    LAS char* q1 = opaque(sm + ch * 4 * P_WAVE + ((c1 >> 3) + 1) * 144 + (c1 & 7) * 16);
    LAS char* q0 = opaque(sm + ch * 4 * P_WAVE + (((c1 - 1) >> 3) + 1) * 144 + ((c1 - 1) & 7) * 16);
    LAS char* xur = opaque(sm + LD_XU + ch * 8192 + c1 * 16);
    auto ce_read = [&](int hf) __attribute__((always_inline)) {
        const int pb_ = hf ? LD_PB : LD_PA;
        r0 = *(LAS f32x4*)(q1 + pb_);
        r1 = *(LAS f32x4*)(q1 + pb_ + P_WAVE); l1 = *(LAS float*)(q0 + pb_ + P_WAVE + 12);
        r2 = *(LAS f32x2*)(q1 + pb_ + 2 * P_WAVE); l2 = *(LAS f32x2*)(q0 + pb_ + 2 * P_WAVE + 8);
        r3 = *(LAS float*)(q1 + pb_ + 3 * P_WAVE); l3 = *(LAS f32x4*)(q0 + pb_ + 3 * P_WAVE);
        xu = *(LAS v4u*)(xur + hf * 4096);
    };
    auto ce_part = [&](int hf, int part) __attribute__((always_inline)) {
        if (part == 0) ya = pk_bf16s(((r0[0] + l2[0]) + (l1 + l3[1])) * ik, ((r0[1] + l2[1]) + (r1[0] + l3[2])) * ik);
        if (part == 1) yb = pk_bf16s(((r0[2] + r2[0]) + (r1[1] + l3[3])) * ik, ((r0[3] + r2[1]) + (r1[2] + r3)) * ik);
        if (part == 2) ya = pk_bf16s(bflo(ya) + bfhi(xu[0]), bfhi(ya) + bfhi(xu[1]));
        if (part == 3) yb = pk_bf16s(bflo(yb) + bfhi(xu[2]), bfhi(yb) + bfhi(xu[3]));
        if (part == 4) ya = pk_bf16s(bflo(ya) + bflo(xu[0]), bfhi(ya) + bflo(xu[1]));
        if (part == 5) {
            yb = pk_bf16s(bflo(yb) + bflo(xu[2]), bfhi(yb) + bflo(xu[3]));
            st8(rs, WS_HT + c1 * 8, (d0 + ch) * (L * 2) + hf * 2048, v2u{ya, yb});
        }
    };
    const int hf_set = WS_HF + (wq * 8 + (((d0 + ch) >> 3) & 7)) * 256 + (((d0 + ch) >> 6) * 8 + ((d0 + ch) & 7)) * 4;
    u32 pf1;
    {
        f32x16 acc0, acc1;
#pragma unroll
        for (int i = 0; i < 16; i++) { acc0[i] = 0.f; acc1[i] = 0.f; }
        constexpr int PD = 3;
        v4u fu[PD + 1], fk[16], fk1[PD + 1];
        auto issue = [&](int n) __attribute__((always_inline)) {
            const int B0 = (n >= 16 && n < 32) ? 2016 - 64 * (n - 16) : 992 - 64 * (n & 15);
            fu[n % (PD + 1)] = lds_tr2(ua + 2 * (B0 - 32) + 32, ua + 2 * (B0 - 32));
            if (n < 16) fk[n] = *(LAS v4u*)(ka + 32 * n);
            if (n >= 32) fk1[n % (PD + 1)] = *(LAS v4u*)(ka + 32 * (n - 16));
        };
#pragma unroll
        for (int n = 0; n < PD; n++) issue(n);
        SB();
        static_for<0, 48>([&](auto N_) __attribute__((always_inline)) {
            constexpr int n = decltype(N_)::v;
            if (n + PD < 48) issue(n + PD);
            if constexpr (n < 16) acc0 = mfma_f16(fu[n % (PD + 1)], fk[n], acc0);
            else if constexpr (n < 32) acc1 = mfma_f16(fu[n % (PD + 1)], fk[n - 16], acc1);
            else acc1 = mfma_f16(fu[n % (PD + 1)], fk1[n % (PD + 1)], acc1);
            if (n == 1) *(LAS v4u*)(sm + LD_XU + t16) = v4u{__builtin_amdgcn_perm(udk[0], xc[0], 0x05040100u), __builtin_amdgcn_perm(udk[1], xc[1], 0x05040100u),
                                                           __builtin_amdgcn_perm(udk[2], xc[2], 0x05040100u), __builtin_amdgcn_perm(udk[3], xc[3], 0x05040100u)};
            if (n == 2) *(LAS v4u*)(sm + LD_XU + 8192 + t16) = v4u{__builtin_amdgcn_perm(udk[0], xc[0], 0x07060302u), __builtin_amdgcn_perm(udk[1], xc[1], 0x07060302u),
                                                                  __builtin_amdgcn_perm(udk[2], xc[2], 0x07060302u), __builtin_amdgcn_perm(udk[3], xc[3], 0x07060302u)};
            if (n == WFL_N) wfl = ld4(rs, l28 + h * 128, wf_s);
            if (n == W1_CHK) {
                PIN(acc0); SB();
                wait_flags(rs, l28 + h * 128, wf_s, wfl, tag);
                pf1 = PF1 ? ld4(rs, tid < 448 ? tid * 128 : OOB, WS_W1T + phi * 229376 + (i28 & 3) * 57344) : 0;
                STAMP(18);
            }
            if constexpr (n == 16 || n == 17) {
                constexpr int g = 2 * (n - 16);
                *(LAS f32x4*)(pw + LD_PA + g * 32) = f32x4{acc0[4 * g], acc0[4 * g + 1], acc0[4 * g + 2], acc0[4 * g + 3]};
                *(LAS f32x4*)(pw + LD_PA + g * 32 + 32) = f32x4{acc0[4 * g + 4], acc0[4 * g + 5], acc0[4 * g + 6], acc0[4 * g + 7]};
            }
            if (n == 17) {
                PIN(acc1);
                if (lane == 63) {
                    *(LAS f32x4*)(sm + LD_PA + wave * P_WAVE + 112) = f32x4{0.f, 0.f, 0.f, 0.f};
                    *(LAS f32x4*)(sm + LD_PB + wave * P_WAVE + 112) = f32x4{acc0[12], acc0[13], acc0[14], acc0[15]};
                }
            }
            if (n == 18) {
                BARRIER();
                if (PRIO_C) __builtin_amdgcn_s_setprio(1);
                STAMP(19);
                st4(rs, (b == 0 && tid == 0) ? WS_EPOCH : OOB, 0, tag);
                ce_read(0);
            }
            if (n >= 21 && n < 27) ce_part(0, n - 21);
            if (n == HF_N) {
                STAMP(22);
                if (!HF_OPT) wait_vm<w1_cnt(HF_N) - w1_cnt(26)>();
                st4(rs, flv, hf_set, tag);
                if (PRIO_C) __builtin_amdgcn_s_setprio(0);
                STAMP(23);
            }
            SB();
#pragma unroll
            for (int ks = w1_cnt(n); ks < w1_cnt(n + 1); ks++) w1_load(ks);
            SB();
        });
#pragma unroll
        for (int g = 0; g < 4; g++) *(LAS f32x4*)(pw + LD_PB + g * 32) = f32x4{acc1[4 * g], acc1[4 * g + 1], acc1[4 * g + 2], acc1[4 * g + 3]};
    }
    constexpr int W1_A = w1_cnt(48) + W1_MID > 28 ? 28 : w1_cnt(48) + W1_MID;
#pragma unroll
    for (int ks = w1_cnt(48); ks < W1_A; ks++) { w1_load(ks); SB(); }
    const int kap = phi * 8 + wave;
    const u32 w2fl = ld4(rs, l28, WS_WF2 + del * 128);
    v2u csg = __builtin_amdgcn_raw_buffer_load_b64(rs, WS_CS + col * 8, kap * 256, SC1);
    u32 hfl;
    if (HFL_EARLY) hfl = ld4(rs, hf_v, hf_s);
    BARRIER();
    STAMP(6);
    ce_read(1);
    int rot14[14], rot7[7];
    {
        int i = (2 * del + ((i28 & 3) * 7 >> 1)) * ROT_W2;
        i = i >= 14 ? i - 14 : i;
#pragma unroll
        for (int n = 0; n < 14; n++) { rot14[n] = i * 1024; i = i == 13 ? 0 : i + 1; }
        i = del * ROT_H;
#pragma unroll
        for (int n = 0; n < 7; n++) { rot7[n] = i * 8192; i = i == 6 ? 0 : i + 1; }
    }
    auto load_h = [&](int hf, int n) __attribute__((always_inline)) {
        glds16(srd, hvoff + hf * 2048, rot7[n] * 32, (u32)(size_t)sm + (hf ? LD_HB : LD_HA) + wave * 1024 + rot7[n]);
    };
#pragma unroll
    for (int i = 0; i < 5; i++) ce_part(1, i);
    SB();
    if (!HFL_EARLY) hfl = ld4(rs, hf_v, hf_s);
    SB();
    ce_part(1, 5);
    SB();
    STAMP(21);
#pragma unroll
    for (int ks = W1_A; ks < 28; ks++) { w1_load(ks); SB(); }
    wait_flags(rs, l28, WS_WF2 + del * 128, w2fl, tag);
    if (!__all(csg[1] == tag)) {
        int spins = SPIN_CAP;
#pragma unroll 1
        do {
            __builtin_amdgcn_s_sleep(4);
            csg = __builtin_amdgcn_raw_buffer_load_b64(rs, WS_CS + col * 8, kap * 256, SC1);
        } while (!__all(csg[1] == tag) && --spins > 0);
    }
    *(LAS u32*)(sm + LD_CS + wave * 128 + col * 4) = csg[0];
    wait_flags(rs, hf_v, hf_s, hfl, tag);
    STAMP(7);
#pragma unroll
    for (int n = 0; n < 7; n++) load_h(0, n);
    SB();
    wait_vm<7>();
    st4(rs, flv, hf_set + 8192, tag);
    SB();
    asm volatile("" :: "v"(pf1));
    wait_vm<1>();
    STAMP(8);
    BARRIER();
    STAMP(9);
    v4u w2[28];
    const int w2v = WS_W2T + (del * 2 * 112 + wave * 14) * 1024 + l16;
    auto w2_lds = [&](int f) __attribute__((always_inline)) { return f < 14 ? LD_PB + (wave * 4 + f - 10) * 1024 : LD_HA + (wave * 7 + f - 14) * 1024; };
    auto w2_load = [&](int f) __attribute__((always_inline)) {
        if (f >= 10 && f < 21) glds16(srd, w2v + (f & 1) * 112 * 1024, rot14[f >> 1], (u32)(size_t)sm + w2_lds(f));
        else w2[f] = ld16(rs, w2v + (f & 1) * 112 * 1024, rot14[f >> 1]);
    };
    const int af_v = (lane < 14 ? lane : 0) * 4, af_s = WS_AF + (tau * 16 + wave) * 128;
    const int av = WS_ACT + (tau2 * 112 + wave * 14) * 1024 + l16;
    const int dd = 64 * del + 8 * wave + 4 * h;
    LAS char* hra = sm + LD_HA + dd * 128 + trl + (((xcd & 1) << 6) ^ sw);
    v2u hr[2];
    u32 afl;
    v4u a0[14], a1[14];
    const int af_set = WS_AF + (tau * 16 + kap / 14) * 128 + (kap % 14) * 4;
    {
        LAS char* at0 = opaque(sm + LD_HA + 8 * h * 128 + trl + sw);
        LAS char* at1 = opaque(sm + LD_HA + 8 * h * 128 + trl + (64 ^ sw));
        LAS char* csr = opaque(sm + LD_CS + wave * 128 + h * 16);
        LAS char* as = opaque(sm + LD_HA + 8 * h * 128 + trl + (((wave & 1) << 6) ^ sw) + (wave >> 1) * 7 * 2048);
        LAS char* str = opaque(sm + LD_ST + col * 32);
        LAS char* cur;
        constexpr int PD = 4;
        v4u fr[PD + 1], sf, sf2;
        auto issue = [&](int n) __attribute__((always_inline)) {
            const int tile = n / 28, j = n % 28;
            if (j % 7 == 0) cur = opaque(((tile & 1) ? at1 : at0) + (seg7[j / 7] * 2048 + (tile >> 1) * 57344));
            LAS char* a = cur + (j % 7) * 2048;
            fr[n % (PD + 1)] = lds_tr2(a, a + 512);
        };
        f32x16 acc[2];
        float nm, k0, k1, sd2, eg, eu, ez, er, eo0;
        f32x4 cg4, cu4;
        f32x4 sta, stb;
        float s1, s2, s1b, s2b;
        u32 opk[4];
        auto stat_acc = [&](int k, int part, int which = 0) __attribute__((always_inline)) {
            const u32 f0 = which ? sf2[2 * part] : sf[2 * part], f1 = which ? sf2[2 * part + 1] : sf[2 * part + 1];
            if (k == 0 && part == 0) { s1 = 0.f; s2 = 0.f; s1b = 0.f; s2b = 0.f; }
            s1 = dot2bf(f0, 0x3f803f80u, s1); s2 = dot2bf(f0, f0, s2);
            s1b = dot2bf(f1, 0x3f803f80u, s1b); s2b = dot2bf(f1, f1, s2b);
        };
        auto stat_pin = [&]() __attribute__((always_inline)) { asm volatile("" : "+v"(s1), "+v"(s2), "+v"(s1b), "+v"(s2b)); };
        auto stat_write = [&](int hf) __attribute__((always_inline)) {
            const float a = half_sum(s1 + s1b), c = half_sum(s2 + s2b);
            *(LAS f32x2*)(str + (hf * 2 + (wave & 1)) * 1024 + (wave >> 1) * 8) = f32x2{a, c};
        };
        auto epi_read = [&](int tile) __attribute__((always_inline)) { sta = *(LAS f32x4*)(str + tile * 1024); stb = *(LAS f32x4*)(str + tile * 1024 + 16); };
        auto epi_stats = [&]() __attribute__((always_inline)) {
            float m = ((sta[0] + sta[2]) + (stb[0] + stb[2])) * (1.f / D), e2 = ((sta[1] + sta[3]) + (stb[1] + stb[3])) * (1.f / D);
            asm("" : "+v"(m)); asm("" : "+v"(e2));
            const float var = rbf(fmaxf(e2 - m * m, 0.f));
            const float sd = rbf(__builtin_amdgcn_sqrtf(rbf(var + 1e-5f))), r = __builtin_amdgcn_rcpf(sd);
            nm = -rbf(m); sd2 = sd * sd; k0 = r * -2.3022082f; k1 = (r * r) * (r * -0.10294324f);
        };
        v4u sg;
        auto epi_stats2 = [&](u32 st) __attribute__((always_inline)) {
            const float sd = bfhi(st), r = __builtin_amdgcn_rcpf(sd);
            nm = -bflo(st); sd2 = sd * sd; k0 = r * -2.3022082f; k1 = (r * r) * (r * -0.10294324f);
        };
        auto cs_read = [&](int g) __attribute__((always_inline)) { cg4 = *(LAS f32x4*)(csr + g * 32); cu4 = *(LAS f32x4*)(csr + 64 + g * 32); };
        auto slice = [&](const f32x16& a, int sidx) __attribute__((always_inline)) {
            const int e = sidx / 3, st = sidx % 3;
            if (st == 0) {
                if (e > 0) { float o = eu * er; asm("" : "+v"(o)); if ((e - 1) & 1) opk[(e - 1) >> 1] = pk_bf16(eo0, o); else eo0 = o; }
                if (e < 8) {
                    eg = __builtin_fmaf(nm, cg4[e & 3], a[e & 7]); eu = __builtin_fmaf(nm, cu4[e & 3], a[8 + (e & 7)]);
                    asm("" : "+v"(eg), "+v"(eu));
                }
            } else if (st == 1) { ez = __builtin_amdgcn_exp2f(eg * __builtin_fmaf(eg * eg, k1, k0)); asm("" : "+v"(ez)); }
            else { er = __builtin_amdgcn_rcpf(__builtin_fmaf(ez, sd2, sd2)); eu *= eg; asm("" : "+v"(er), "+v"(eu)); }
        };
        auto epi_four = [&](const f32x16& a, int g) __attribute__((always_inline)) {
            const f32x4 nm4 = {nm, nm, nm, nm}, sd4 = {sd2, sd2, sd2, sd2};
            const f32x4 gg = __builtin_elementwise_fma(nm4, cg4, f32x4{a[4 * g], a[4 * g + 1], a[4 * g + 2], a[4 * g + 3]});
            const f32x4 u = __builtin_elementwise_fma(nm4, cu4, f32x4{a[8 + 4 * g], a[9 + 4 * g], a[10 + 4 * g], a[11 + 4 * g]});
            const f32x4 z = gg * __builtin_elementwise_fma(gg * gg, f32x4{k1, k1, k1, k1}, f32x4{k0, k0, k0, k0});
            const f32x4 e = {__builtin_amdgcn_exp2f(z.x), __builtin_amdgcn_exp2f(z.y), __builtin_amdgcn_exp2f(z.z), __builtin_amdgcn_exp2f(z.w)};
            const f32x4 dn = __builtin_elementwise_fma(e, sd4, sd4);
            const f32x4 o = (gg * u) * f32x4{__builtin_amdgcn_rcpf(dn.x), __builtin_amdgcn_rcpf(dn.y), __builtin_amdgcn_rcpf(dn.z), __builtin_amdgcn_rcpf(dn.w)};
            opk[2 * g] = pk_bf16(o.x, o.y); opk[2 * g + 1] = pk_bf16(o.z, o.w);
        };
        auto epi_store = [&](int tile) __attribute__((always_inline)) {
            const int tok32 = (tile >> 1) * 32 + tau * 2 + (tile & 1);
            st16(rs, WS_ACT + l16 + (tok32 * 112 + kap) * 1024, v4u{opk[0], opk[1], opk[2], opk[3]});
        };
#pragma unroll
        for (int n = 0; n < PD; n++) issue(n);
        SB();
        static_for<0, 112>([&](auto N_) __attribute__((always_inline)) {
            constexpr int n = decltype(N_)::v;
            constexpr int tile = n / 28, j = n % 28;
            if (n == 52) {
                STAMP(27);
                wait_vm<g1_cum(52) - g1_cum(HB_N + 8)>();
                BARRIER();
                if (PRIO_G) __builtin_amdgcn_s_setprio(1);
                STAMP(28);
            }
            if (n + PD < 112) issue(n + PD);
            if (j == 0) {
                f32x16 z;
#pragma unroll
                for (int i = 0; i < 16; i++) z[i] = 0.f;
                acc[tile & 1] = mfma_bf16(wf[0], fr[n % (PD + 1)], z);
            } else {
                acc[tile & 1] = mfma_bf16(wf[j], fr[n % (PD + 1)], acc[tile & 1]);
            }
            if (MFMA_FIRST) SB();
            if constexpr (n < 28) {
                constexpr int sk = n;
                if (sk < 21 && sk % 3 == 0) { if (sk > 0) { stat_acc(sk / 3 - 1, 1); SB(); } sf = lds_tr2(as + (sk / 3) * 2048, as + (sk / 3) * 2048 + 512); }
                if (sk < 21 && sk % 3 == 2) stat_acc(sk / 3, 0);
                if (sk == 21) stat_acc(6, 1);
                if (sk == 22) stat_write(0);
            }
            if (n == HBP_N) hfl = ld4(rs, hf_v, hf_s + 8192);
            if (n == HB_N) {
                PIN(acc[0]);
                SB();
                STAMP(24);
                wait_flags(rs, hf_v, hf_s + 8192, hfl, tag);
                STAMP(25);
            }
            if (n == 48) hr[0] = lds_tr(hra);
            if (n == 27) { STAMP(26); BARRIER_RAW(); STAMP(29); }
            if (n == 27 || n == 55) epi_read(tile);
            if (n == 84) {
                PIN(acc[0]); PIN(acc[1]);
                SB();
                STAMP(30);
                if (!__all(sg[1] == tag && sg[3] == tag)) {
                    int spins = SPIN_CAP;
#pragma unroll 1
                    do {
                        __builtin_amdgcn_s_sleep(2);
                        sg = ld16(rs, WS_SG + col * 16, tau * 512);
                    } while (!__all(sg[1] == tag && sg[3] == tag) && --spins > 0);
                }
                STAMP(31);
            }
            if (tile == 1) {
                if (j == 0) cs_read(0);
                if (j == 1) epi_stats();
                if (j >= 2 && j < 27) slice(acc[0], j - 2);
                if (j == 11) { SB(); cs_read(1); }
            }
            if (tile == 2) {
                if (j == 0) cs_read(0);
                if (j == 1) epi_stats();
                if (j >= 2 && j < 10) { slice(acc[1], 3 * (j - 2)); if (j == 5) { SB(); cs_read(1); } slice(acc[1], 3 * (j - 2) + 1); slice(acc[1], 3 * (j - 2) + 2); }
                if (j == 10) slice(acc[1], 24);
            }
            if (tile == 3) {
                if (j == 0) cs_read(0);
                if (j == 1) epi_stats2(sg[0]);
                if (j >= 2 && j < 27) slice(acc[0], j - 2);
                if (j == 11) { SB(); cs_read(1); }
            }
            if (n == AFC_N) {
                PIN(acc[1]); asm volatile("" : "+v"(eg), "+v"(eu), "+v"(ez), "+v"(er), "+v"(eo0), "+v"(opk[0]), "+v"(opk[1]));
                SB();
                wait_flags(rs, af_v, af_s, afl, tag);
            }
            constexpr int op = g1_op(n);
            if constexpr (op >= 60) a0[op - 60] = ld16(rs, av, rot14[op - 60]);
            if constexpr (op >= 0 && op < 28) w2_load(op);
            if constexpr (op >= 32 && op < 40) load_h(1, op - 32);
            if constexpr (op >= 40 && op < 50) epi_store(op - 40);
            if constexpr (op == 50) { wait_vm<g1_cum(AF_N) - g1_cum(ST1_N + 1)>(); st4(rs, flv, af_set, tag); if (PRIO_G) __builtin_amdgcn_s_setprio(0); if (BAR_AF) BARRIER_RAW(); }
            if constexpr (op == 51) afl = ld4(rs, af_v, af_s);
            if constexpr (op == 52) st4(rs, (tid == 0 && del == 0) ? 0 : OOB, WS_TR + ((xcd & 1) * 16 + tau) * 128, tag);
            if constexpr (op == 53) sg = ld16(rs, WS_SG + col * 16, tau * 512);
            SB();
        });
        PIN(acc[1]);
        STAMP(10);
        STAMP(11);
        cs_read(0);
        epi_stats2(sg[2]);
        epi_four(acc[1], 0);
        cs_read(1);
        epi_four(acc[1], 1);
        epi_store(3);
        if (OPT) { SB(); st4(rs, flv, af_set + 1024, tag); }
    }
    SB();
#pragma unroll
    for (int n = A0_IN; n < 14; n++) { a0[n] = ld16(rs, av, rot14[n]); SB(); }
    hr[1] = lds_tr(hra + 57344);
#pragma unroll
    for (int f = 10; f < 21; f++) w2[f] = *(LAS v4u*)(sm + w2_lds(f) + l16);
    STAMP(12);
    BARRIER();
    f32x16 acc[2][2];
#pragma unroll
    for (int i = 0; i < 16; i++) { acc[0][0][i] = 0.f; acc[0][1][i] = 0.f; acc[1][0][i] = 0.f; acc[1][1][i] = 0.f; }
    LAS char* rw = opaque(sm + wave * 1024 + l16);
    LAS char* rr = opaque(sm + wave * 8192 + l16);
    auto red_write = [&](int hf) __attribute__((always_inline)) {
#pragma unroll
        for (int o = 0; o < 8; o++) {
            const f32x16& t = acc[o >> 2][hf];
            const int r = 4 * (o & 3);
            *(LAS f32x4*)(rw + hf * 65536 + o * 8192) = f32x4{t[r], t[r + 1], t[r + 2], t[r + 3]};
        }
    };
    auto red_finish = [&](int hf) __attribute__((always_inline)) {
        f32x4 v[8];
#pragma unroll
        for (int s = 0; s < 8; s++) v[s] = *(LAS f32x4*)(rr + hf * 65536 + s * 1024);
        const f32x4 t = ((v[0] + v[1]) + (v[2] + v[3])) + ((v[4] + v[5]) + (v[6] + v[7]));
        const u32 ta = pk_bf16(t[0], t[1]), tb = pk_bf16(t[2], t[3]);
        const int tok = 1024 * hf + 32 * tau2 + col;
        *(v2u*)(p.out + (size_t)tok * D + dd) = v2u{pk_bf16(bflo(ta) + bflo(hr[hf][0]), bfhi(ta) + bfhi(hr[hf][0])),
                                                    pk_bf16(bflo(tb) + bflo(hr[hf][1]), bfhi(tb) + bfhi(hr[hf][1]))};
    };
#pragma unroll
    for (int n = 0; n < 14; n++) {
        acc[0][0] = mfma_bf16(w2[2 * n], a0[n], acc[0][0]);
        acc[1][0] = mfma_bf16(w2[2 * n + 1], a0[n], acc[1][0]);
        if (!OPT && n == AFB_N) { wait_vm<14 - A0_IN>(); st4(rs, flv, af_set + 1024, tag); }
        if (TAIL && n == AFBP_N) afl = ld4(rs, af_v, af_s + 1024);
        SB();
    }
    PIN(acc[0][0]); PIN(acc[1][0]);
    STAMP(13);
    wait_flags(rs, af_v, af_s + 1024, afl, tag);
    STAMP(14);
#pragma unroll
    for (int n = 0; n < 14; n++) { a1[n] = ld16(rs, av + 32 * 112 * 1024, rot14[n]); SB(); }
    red_write(0);
    SB();
    STAMP(15);
#pragma unroll
    for (int n = 0; n < 14; n++) {
        acc[0][1] = mfma_bf16(w2[2 * n], a1[n], acc[0][1]);
        acc[1][1] = mfma_bf16(w2[2 * n + 1], a1[n], acc[1][1]);
        SB();
    }
    STAMP(16);
    red_write(1);
    BARRIER();
    if (TAIL == 2) red_finish(0);
    red_finish(1);
    STAMP(17);
    STAMP_FLUSH();
    asm volatile(".Lfb_code_end:");
}

}

extern "C" void kernel_launch(void* const* d_in, const int* in_sizes, int n_in,
                              void* d_out, int out_size, void* d_ws, size_t ws_size,
                              hipStream_t stream) {
    Params p;
    p.x = (const u16*)d_in[0]; p.Are = (const u16*)d_in[1]; p.Aim = (const u16*)d_in[2];
    p.Bre = (const u16*)d_in[3]; p.Bim = (const u16*)d_in[4]; p.Cre = (const u16*)d_in[5]; p.Cim = (const u16*)d_in[6];
    p.logdt = (const u16*)d_in[7]; p.Dskip = (const u16*)d_in[8];
    p.wg = (const u16*)d_in[10]; p.wu = (const u16*)d_in[11]; p.wd = (const u16*)d_in[12];
    p.ws = (char*)d_ws; p.out = (u16*)d_out;
    (void)hipFuncSetAttribute((const void*)fused_block, hipFuncAttributeMaxDynamicSharedMemorySize, LDS_BYTES);
    fused_block<<<256, 512, LDS_BYTES, stream>>>(p);
}
```

```cpp
#include <hip/hip_runtime.h>
#include <stdint.h>

namespace {

constexpr int L = 2048, D = 448, N = 256, F = 1792;

typedef unsigned short u16;
typedef unsigned int u32;
typedef unsigned long long u64;
typedef u32 v4u __attribute__((ext_vector_type(4)));
typedef u32 v2u __attribute__((ext_vector_type(2)));
typedef float f32x2 __attribute__((ext_vector_type(2)));
typedef float f32x4 __attribute__((ext_vector_type(4)));
typedef float f32x16 __attribute__((ext_vector_type(16)));
typedef _Float16 f16x2 __attribute__((ext_vector_type(2)));
typedef _Float16 f16x8 __attribute__((ext_vector_type(8)));
typedef __bf16 bf16x2 __attribute__((ext_vector_type(2)));
typedef __bf16 bf16x8 __attribute__((ext_vector_type(8)));
typedef short s16x4 __attribute__((ext_vector_type(4)));
#define GAS __attribute__((address_space(1)))
#define LAS __attribute__((address_space(3)))
#define RLX_AGENT __ATOMIC_RELAXED, __HIP_MEMORY_SCOPE_AGENT
#define SB() __builtin_amdgcn_sched_barrier(0)
#define PIN(x) asm volatile("" : "+v"(x))
#define BARRIER() asm volatile("s_waitcnt lgkmcnt(0)\n\ts_barrier" ::: "memory")
#define BARRIER_RAW() asm volatile("s_barrier" ::: "memory")
constexpr int SC1 = 16;

constexpr int WS_EPOCH = 0;
constexpr int WS_WF2 = 1024;
constexpr int WS_TR = 4096;
constexpr int WS_HF = 8192;
constexpr int WS_AF = WS_HF + 64 * 256;
constexpr int WS_SG = 57344;
constexpr int WS_WF1 = 65536;
constexpr int WS_CS = 65536 + 8192;
constexpr int WS_ST1 = WS_CS + 32768;
constexpr int WS_XX = WS_ST1 + 16384;
constexpr int WS_W1T = WS_XX + 224 * 16384;
constexpr int WS_W2T = WS_W1T + 112 * 28 * 1024;
constexpr int WS_HT = WS_W2T + 14 * 112 * 1024;
constexpr int WS_ACT = WS_HT + D * L * 2;
constexpr int WS_DBG = WS_ACT + 64 * 112 * 1024;
constexpr int NSTAMP = 32;
constexpr int WS_END = WS_DBG + 256 * NSTAMP * 8;
constexpr int SPIN_CAP = 1 << 16;
static_assert(WS_AF + 16 * 2 * 8 * 128 <= WS_SG, "flags overlap");

#define STAMP(k) do {} while (0)
#define STAMP_FLUSH() do {} while (0)
#define STAMP_DECL() do {} while (0)

__device__ __forceinline__ float bflo(u32 v) { return __builtin_bit_cast(float, v << 16); }
__device__ __forceinline__ float bfhi(u32 v) { return __builtin_bit_cast(float, v & 0xffff0000u); }
__device__ __forceinline__ u32 pk_bf16(float a, float b) { f32x2 f = {a, b}; return __builtin_bit_cast(u32, __builtin_convertvector(f, bf16x2)); }
__device__ __forceinline__ u32 pk_bf16s(float a, float b) { asm("" : "+v"(a)); asm("" : "+v"(b)); return pk_bf16(a, b); }
__device__ __forceinline__ u32 pk_f16(float a, float b) { f32x2 f = {a, b}; return __builtin_bit_cast(u32, __builtin_convertvector(f, f16x2)); }
__device__ __forceinline__ float rbf(float a) { return bflo(pk_bf16(a, 0.f)); }
__device__ __forceinline__ float dot2bf(u32 a, u32 b, float c) {
    return __builtin_amdgcn_fdot2_f32_bf16(__builtin_bit_cast(bf16x2, a), __builtin_bit_cast(bf16x2, b), c, false);
}
__device__ __forceinline__ f32x16 mfma_bf16(v4u a, v4u b, f32x16 c) {
    return __builtin_amdgcn_mfma_f32_32x32x16_bf16(__builtin_bit_cast(bf16x8, a), __builtin_bit_cast(bf16x8, b), c, 0, 0, 0);
}
__device__ __forceinline__ f32x16 mfma_f16(v4u a, v4u b, f32x16 c) {
    return __builtin_amdgcn_mfma_f32_32x32x16_f16(__builtin_bit_cast(f16x8, a), __builtin_bit_cast(f16x8, b), c, 0, 0, 0);
}
__device__ __forceinline__ v2u lds_tr(LAS char* p) {
    return __builtin_bit_cast(v2u, __builtin_amdgcn_ds_read_tr16_b64_v4i16((LAS s16x4*)p));
}
__device__ __forceinline__ v4u lds_tr2(LAS char* p0, LAS char* p1) {
    const v2u lo = lds_tr(p0), hi = lds_tr(p1);
    return v4u{lo[0], lo[1], hi[0], hi[1]};
}
template <int CTRL> __device__ __forceinline__ float dpp(float v) {
    return __builtin_bit_cast(float, __builtin_amdgcn_update_dpp(0, __builtin_bit_cast(int, v), CTRL, 0xf, 0xf, true));
}
__device__ __forceinline__ float rdl(float v, int l) { return __builtin_bit_cast(float, __builtin_amdgcn_readlane(__builtin_bit_cast(int, v), l)); }
__device__ __forceinline__ float wave_sum(float v) {
    v += dpp<0xB1>(v); v += dpp<0x4E>(v); v += dpp<0x141>(v); v += dpp<0x140>(v);
    return (rdl(v, 0) + rdl(v, 16)) + (rdl(v, 32) + rdl(v, 48));
}
__device__ __forceinline__ float wave_max(float v) {
    v = fmaxf(v, dpp<0xB1>(v)); v = fmaxf(v, dpp<0x4E>(v)); v = fmaxf(v, dpp<0x141>(v)); v = fmaxf(v, dpp<0x140>(v));
    return fmaxf(fmaxf(rdl(v, 0), rdl(v, 16)), fmaxf(rdl(v, 32), rdl(v, 48)));
}
__device__ __forceinline__ float half_sum(float v) {
    const auto r = __builtin_amdgcn_permlane32_swap(__builtin_bit_cast(u32, v), __builtin_bit_cast(u32, v), false, false);
    return __builtin_bit_cast(float, (u32)r[0]) + __builtin_bit_cast(float, (u32)r[1]);
}
template <int I> struct IC { static constexpr int v = I; };
template <int B, int E, class Fn> __device__ __forceinline__ void static_for(Fn&& f) {
    if constexpr (B < E) { f(IC<B>{}); static_for<B + 1, E>(f); }
}

typedef f32x2 cf;
__device__ __forceinline__ cf rot(cf b) { return cf{-b.y, b.x}; }
__device__ __forceinline__ cf cmul(cf a, cf b) { return __builtin_elementwise_fma(a.yy, rot(b), a.xx * b); }
__device__ __forceinline__ cf cmulc(cf a, cf b) { return __builtin_elementwise_fma(a.yy, b.yx, a.xx * rot(b).yx); }
__device__ __forceinline__ u32 pkh(cf a) { return __builtin_bit_cast(u32, __builtin_convertvector(a, f16x2)); }

typedef __amdgpu_buffer_rsrc_t rsrc_t;
__device__ __forceinline__ v4u ld16(rsrc_t r, int voff, int soff) { return __builtin_amdgcn_raw_buffer_load_b128(r, voff, soff, SC1); }
__device__ __forceinline__ u32 ld4(rsrc_t r, int voff, int soff) { return __builtin_amdgcn_raw_buffer_load_b32(r, voff, soff, SC1); }
__device__ __forceinline__ void st16(rsrc_t r, int voff, v4u v) { __builtin_amdgcn_raw_buffer_store_b128(v, r, voff, 0, SC1); }
__device__ __forceinline__ void st8(rsrc_t r, int voff, int soff, v2u v) { __builtin_amdgcn_raw_buffer_store_b64(v, r, voff, soff, SC1); }
__device__ __forceinline__ void st4(rsrc_t r, int voff, int soff, u32 v) { __builtin_amdgcn_raw_buffer_store_b32(v, r, voff, soff, SC1); }
template <int N> __device__ __forceinline__ void wait_vm() { asm volatile("s_waitcnt vmcnt(%0)" :: "i"(N) : "memory"); }
constexpr int OOB = 0x7f000000;

__device__ __forceinline__ void wait_flags(rsrc_t rs, int voff, int soff, u32 v, u32 tag) {
    if (!__all(v == tag)) {
        int spins = SPIN_CAP;
#pragma unroll 1
        do {
            __builtin_amdgcn_s_sleep(1);
            v = ld4(rs, voff, soff);
        } while (!__all(v == tag) && --spins > 0);
    }
    __builtin_amdgcn_fence(__ATOMIC_ACQUIRE, "wavefront");
}
__device__ __forceinline__ void glds16(v4u srd, int voff, int soff, u32 lds_dst) {
    asm volatile("s_mov_b32 m0, %3\n\ts_nop 0\n\tbuffer_load_dwordx4 %0, %1, %2 offen sc1 lds"
                 :: "v"(voff), "s"(srd), "s"(soff), "s"(lds_dst) : "memory");
}
__device__ __forceinline__ LAS char* opaque(LAS char* q) { u32 a = (u32)(size_t)q; asm volatile("" : "+v"(a)); return (LAS char*)(size_t)a; }

struct Params {
    const u16 *x, *Are, *Aim, *Bre, *Bim, *Cre, *Cim, *logdt, *Dskip, *wg, *wu, *wd;
    char* ws;
    u16* out;
};

constexpr int XY_STRIDE = 272, XY_ROWS = 59, XY_WAVE = XY_STRIDE * XY_ROWS;
constexpr int P_WAVE = 33 * 144;
constexpr int LD_PB = 0, LD_PA = 38144;
constexpr int LD_HA = 38144, LD_HB = LD_HA + 57344;
constexpr int LD_XU = LD_HB;
constexpr int LD_KP = 128512;
constexpr int LD_U = LD_KP + 8 * 1536;
constexpr int LD_SB = LD_HB + 57344;
constexpr int LD_ST = LD_SB + 64;
constexpr int LD_CS = LD_ST + 4096;
constexpr int LDS_BYTES = LD_CS + 1024;
static_assert(LD_U + 2 * 4160 <= LD_SB && LDS_BYTES <= 163840, "lds map");

__device__ __forceinline__ v4u tr_piece(const v4u (&in)[8], int c) {
    v4u o;
#pragma unroll
    for (int kp = 0; kp < 4; kp++)
        o[kp] = __builtin_amdgcn_perm(in[2 * kp + 1][c >> 1], in[2 * kp][c >> 1], (c & 1) ? 0x07060302u : 0x05040100u);
    return o;
}
__device__ __forceinline__ void w1_task_load(const Params& p, int task, int lane, v2u (&in)[8]) {
    const int t = (task < 0 ? 0 : task) >> 1, c4 = task & 1;
    const int phi = t / 28, r = t - phi * 28, mat = r >= 14, kp = r - 14 * mat;
    const rsrc_t rw = __builtin_amdgcn_make_buffer_rsrc((void*)(mat ? p.wu : p.wg), 0, task < 0 ? 0 : D * F * 2, 0x00020000);
    const int voff = (8 * (lane >> 4) * F + (lane & 15) * 8) * 2;
#pragma unroll
    for (int k = 0; k < 8; k++) in[k] = __builtin_amdgcn_raw_buffer_load_b64(rw, voff + (k & 1) * F * 2, ((32 * kp + (k & 6)) * F + phi * 128 + 4 * c4) * 2, 0);
}
__device__ __forceinline__ void w1_task_store(rsrc_t rs, int task, int lane, const v2u (&in)[8]) {
    const int t = task >> 1, c4 = task & 1;
    const int phi = t / 28, r = t - phi * 28, mat = r >= 14, kp = r - 14 * mat;
    const int voff = WS_W1T + ((phi * 8 + 4 * c4) * 28 + 2 * kp) * 1024 + mat * 256 + (lane >> 4) * 512 + (lane & 15) * 16;
#pragma unroll
    for (int c = 0; c < 4; c++) {
        v4u o;
#pragma unroll
        for (int kp2 = 0; kp2 < 4; kp2++) o[kp2] = __builtin_amdgcn_perm(in[2 * kp2 + 1][c >> 1], in[2 * kp2][c >> 1], (c & 1) ? 0x07060302u : 0x05040100u);
        st16(rs, voff + c * 28 * 1024, o);
    }
}
__device__ __forceinline__ int w1_flag(int task) { const int t = task >> 1, phi = t / 28; return WS_WF1 + (phi * 2 + (task & 1)) * 128 + (t - phi * 28) * 4; }
__device__ __forceinline__ void w2_task(const Params& p, rsrc_t rs, int task, int lane, LAS char* scr) {
    const int del = task / 28, kg = task - del * 28;
    const int k4 = (lane >> 3) & 3, ks = kg * 4 + k4, h = lane >> 5, dr = lane & 7;
    const u16* src = p.wd + (size_t)((ks >> 3) * 128 + 32 * h + (ks & 7)) * D + del * 64 + dr * 8;
    v4u in[8];
#pragma unroll
    for (int j = 0; j < 8; j++) in[j] = __builtin_nontemporal_load((const v4u*)(src + (size_t)(8 * ((j & 3) + 8 * (j >> 2))) * D));
    LAS char* w = scr + ((dr >> 2) * 4 + k4) * 1024 + (32 * h + 8 * (dr & 3)) * 16;
#pragma unroll
    for (int c = 0; c < 8; c++) *(LAS v4u*)(w + c * 16) = tr_piece(in, c);
    const int voff = WS_W2T + (del * 2 * 112 + kg * 4) * 1024 + lane * 16;
#pragma unroll
    for (int f = 0; f < 8; f++) st16(rs, voff + ((f >> 2) * 112 + (f & 3)) * 1024, *(LAS v4u*)(scr + f * 1024 + lane * 16));
}

#define W1_CHK 2
#define KPRE 32
#define W1_NUM 2
#define W1_DEN 5
#define W1_BURST 4
#define PF1 0
#define PF2 1
#define GR_EARLY 3
#define CS_T 520
#define GR_T 320
#define AFB_EARLY 1
#define W1_ORDER 1
#define PRIO_C 1
constexpr int w1_cnt(int n) { return n <= W1_CHK ? 0 : W1_BURST + ((n - W1_CHK - 1) * W1_NUM) / W1_DEN > 28 ? 28 : W1_BURST + ((n - W1_CHK - 1) * W1_NUM) / W1_DEN; }
#define PRIO_A 0
#define CS_FIRST 1
#define HBP_N 10
#define W2_N0 34
#define PRIO_G 1
#define BAR_AF 1
#define OPT 0
#define AFBP_N 13
#define TAIL 0
#define ROT_H 0
#define HF_OPT 0
#define HFL_EARLY 0
#define WFL_N -1
#define AFB_N 4
#define SG_N 77
#define A0_LATE 0
#define MFMA_FIRST 1
#define HB_N 24
#define ROT_W1 0
#define ROT_W2 0
#define AF_N 79
#define HF_N 37
#define AFP_N 90
#define A0_IN 7
#define AFC_N 100
constexpr int ST1_N = 67;
constexpr int g1_op(int n) {
    return (n > HB_N && n <= HB_N + 7) ? 32 + (n - HB_N - 1)
         : n == 55 ? 40 : n == ST1_N ? 41 : n == 111 ? 42 : n == AF_N ? 50 : n == AFP_N ? 51 : n == 33 ? 52 : n == SG_N ? 53
         : (n >= 1 && n < 21 && (n & 1) == 1) ? (n - 1) / 2
         : (n >= 36 && n < 52 && (n & 3) == 0) ? 10 + (n - 36) / 4
         : (n == 54 || (n >= 68 && n < 80 && (n & 1) == 0)) ? (n == 54 ? 14 : 15 + (n - 68) / 2)
         : (n >= 85 && n < 99 && (n & 1) == 1) ? 21 + (n - 85) / 2
         : (n > AFC_N && n <= AFC_N + A0_IN) ? 60 + (n - AFC_N - 1)
         : -1;
}
constexpr bool g1_all() { int m = 0; for (int i = 0; i < 112; i++) if (g1_op(i) >= 0 && g1_op(i) < 28) m |= 1 << g1_op(i); return m == (1 << 28) - 1; }
static_assert(g1_all(), "every W2 fragment is requested");
constexpr int g1_cum(int n) { int c = 0; for (int i = 0; i < n; i++) c += g1_op(i) >= 0; return c; }
static_assert(HB_N >= 23 && HB_N < 28 && AF_N > ST1_N && AFP_N > AF_N && AFC_N > AFP_N && AFC_N >= 99 && AFC_N + A0_IN < 111, "schedule");
#define W1_MID 6
#define CODE_PF 23552

__global__ __launch_bounds__(512, 2) void fused_block(Params p) {
    extern __shared__ __attribute__((aligned(16))) char smem_[];
    LAS char* sm = (LAS char*)smem_;
    const int tid = threadIdx.x, lane = tid & 63;
    const int wave = __builtin_amdgcn_readfirstlane(tid >> 6);
    const int b = blockIdx.x;
    STAMP_DECL();
    const bool main_blk = b < 224;
    const int xcd = b & 7, i28 = b >> 3;
    u64 pc0; u32 clen;
    asm volatile("s_getpc_b64 %0\n.Lfb_code_begin:\n\ts_mov_b32 %1, .Lfb_code_end-.Lfb_code_begin" : "=s"(pc0), "=s"(clen));
    const u32 cpf = __builtin_amdgcn_raw_buffer_load_b32(__builtin_amdgcn_make_buffer_rsrc((void*)pc0, 0, main_blk ? 0 : clen, 0x00020000),
                                                         lane < 16 ? lane * 64 : OOB, (wave * 4 + (i28 & 3)) * 1024, 0);
    const int l16 = lane * 16;
    const int rbase = 16 * (b >> 1) + 2 * (b & 1);
    int x2v = lane < 56 ? (lane & 3) * (4 * D * 2) + (lane >> 2) * 8 : OOB, xrv = lane < 56 ? l16 : OOB, t2 = tid * 2;
    int x2s = (rbase * D + 56 * wave) * 2, xrs = (rbase + 4 * (wave >> 1) + (wave & 1)) * D * 2;
    int pair = xcd * 28 + i28;
    asm volatile("" : "+v"(x2v), "+v"(xrv), "+v"(t2), "+s"(x2s), "+s"(xrs), "+s"(pair));
    SB();
    const rsrc_t rs = __builtin_amdgcn_make_buffer_rsrc(p.ws, 0, WS_END, 0x00020000);
    asm volatile("" :: "s"(p.x), "s"(p.wd), "s"(p.ws));
    if (PRIO_A && wave >= 4) __builtin_amdgcn_s_setprio(1);
    STAMP(0);
    const u32 tstart = GR_EARLY == 3 ? (u32)__builtin_amdgcn_s_memrealtime() : 0;
    const u32 tagv = ld4(rs, WS_EPOCH, 0);

    const rsrc_t rx = __builtin_amdgcn_make_buffer_rsrc((void*)p.x, 0, L * D * 2, 0x00020000);
    const v2u xa = __builtin_amdgcn_raw_buffer_load_b64(rx, x2v, x2s, 2);
    const v2u xb = __builtin_amdgcn_raw_buffer_load_b64(rx, x2v + D * 2, x2s, 2);
    const v4u xr = __builtin_amdgcn_raw_buffer_load_b128(rx, xrv, xrs, 2);
    SB();
    const int ch = wave >> 2, wq = wave & 3;
    const int d0 = 2 * pair;
    const int psz = main_blk ? D * N * 2 : 0;
    auto ldp = [&](const u16* q) __attribute__((always_inline)) -> u32 {
        return __builtin_amdgcn_raw_buffer_load_b16(__builtin_amdgcn_make_buffer_rsrc((void*)q, 0, psz, 0x00020000), t2, d0 * N * 2, 2);
    };
    const u32 r_ldt = __builtin_amdgcn_raw_buffer_load_b32(__builtin_amdgcn_make_buffer_rsrc((void*)p.logdt, 0, main_blk ? D * 2 : 0, 0x00020000), 0, d0 * 2, 0);
    const u32 r_are = ldp(p.Are), r_aim = ldp(p.Aim), r_bre = ldp(p.Bre), r_bim = ldp(p.Bim), r_cre = ldp(p.Cre), r_cim = ldp(p.Cim);
    const u32 r_dsk = __builtin_amdgcn_raw_buffer_load_b32(__builtin_amdgcn_make_buffer_rsrc((void*)p.Dskip, 0, main_blk ? D * 2 : 0, 0x00020000), 0, d0 * 2, 0);
    const int hw = (b - 224) + 32 * wave;
    const int w1task = !main_blk ? -1 : wave < 2 ? pair * 2 + wave : (wave < 4 && pair < 168) ? 448 + pair * 2 + wave - 2 : -1;
    SB();
    const int h = lane >> 5, col = lane & 31;
    const int G1 = (lane >> 4) & 1, q4 = (lane & 15) >> 2, pp = lane & 3;
    int xxv = lane < 56 ? WS_XX + ((28 * wave + 2 * (lane >> 2)) * 2 + (b & 1)) * 8192 + (4 * (b >> 1) + (lane & 3)) * 16 : OOB;
    int flv = lane == 0 ? 0 : OOB;
    asm volatile("" : "+v"(xxv), "+v"(flv));
    LAS char* xy = sm + wave * XY_WAVE;
    LAS char* xyw = opaque(xy + lane * 4);
    LAS char* pa = opaque(xy + (32 + (int)((0x1713100d0a080400ull >> (8 * (col >> 2))) & 255) + (col & 3)) * XY_STRIDE + h * 16);
    LAS char* pb = opaque(xy + col * XY_STRIDE + h * 16);
    LAS char* exw = opaque(xy + l16);
    LAS char* exr = opaque(sm + (ch * 4) * XY_WAVE + wq * 1024 + l16);
    LAS char* kpw = opaque(sm + LD_KP + ch * 4 * 1536 + 512 + (wq < 2 ? 64 * wq + 2 * col + h : wq == 2 ? 64 * (2 + h) + 2 * col : 64 * (4 + 2 * h) + 2 * col) * 2);
    LAS char* uw = opaque(sm + LD_U + 64 + tid * 8);
    LAS char* ua = opaque(sm + LD_U + ch * 4160 + 2 * (32 + 32 + 16 * G1 - 32 * h - 16 - 4 * q4 + 4 * pp));
    LAS char* ka = opaque(sm + LD_KP + wave * 1536 + 2 * (256 - 248 + 8 * col + 8 * h));
    LAS char* pw = opaque(sm + wave * P_WAVE + (col + 1) * 144 + h * 16);
    *(LAS v2u*)(sm + LD_KP + wave * 1536 + lane * 8) = v2u{0, 0};
    if (tid < 32) *(LAS u32*)(sm + LD_U + (tid >> 4) * 4160 + (tid & 15) * 4) = 0;
    SB();
    const u32 tag = __builtin_amdgcn_readfirstlane(tagv) + 1u;
    st16(rs, xxv, v4u{xa[0], tag, xb[0], tag});
    st16(rs, xxv + 16384, v4u{xa[1], tag, xb[1], tag});
    SB();
    {
        float a = 0.f, q = 0.f;
#pragma unroll
        for (int i = 0; i < 4; i++) { a = dot2bf(xr[i], 0x3f803f80u, a); q = dot2bf(xr[i], xr[i], q); }
        const float mean = wave_sum(a) * (1.f / D), ex2 = wave_sum(q) * (1.f / D);
        const float var = rbf(fmaxf(ex2 - mean * mean, 0.f));
        const float sd = __builtin_amdgcn_sqrtf(rbf(var + 1e-5f));
        st8(rs, flv, WS_ST1 + (((b & 1) * 512 + 4 * (b >> 1) + (wave >> 1)) * 2 + (wave & 1)) * 8, v2u{pk_bf16(mean, sd), tag});
    }
    SB();
    v2u tin[8];
    w1_task_load(p, w1task, lane, tin);
    SB();
    STAMP(1);
    const int tau = main_blk ? (xcd >> 1) * 4 + (i28 & 3) : (b & 15), phi = (xcd & 1) * 7 + (i28 >> 2);
    const int del = i28 >> 2, tau2 = 2 * tau + (xcd & 1);
    const int trl = q4 * 128 + 8 * (pp & 1) + 16 * (2 * G1 + (pp >> 1)), sw = (q4 & 2) << 5;
    const int hf_v = (lane < 56 ? lane : 0) * 4, hf_s = WS_HF + ((tau >> 2) * 8 + wave) * 256;
    const v4u srd = {(u32)(size_t)p.ws, (u32)((size_t)p.ws >> 32) & 0xffffu, (u32)WS_END, 0x00020000u};
    const int hvoff = WS_HT + ((8 * wave + (lane >> 3)) * L + tau * 64) * 2 + (((lane & 7) ^ (((lane >> 3) & 2) << 1)) << 4);
    if (!main_blk) {
        BARRIER_RAW();
        const int w2task = hw;
        if (w2task < 196) {
            w2_task(p, rs, w2task, lane, sm + LD_HB + wave * 8192);
            wait_vm<0>();
            st4(rs, flv, WS_WF2 + (w2task / 28) * 128 + (w2task % 28) * 4, tag);
        }
        asm volatile("" :: "v"(cpf), "v"(tin[7]));
        STAMP(2);
        const int unit = 255 - hw;
        if (unit < 112) {
            while ((u32)__builtin_amdgcn_s_memrealtime() - tstart < CS_T) __builtin_amdgcn_s_sleep(8);
            const int fv = (col < 28 ? col : 0) * 4, fs = WS_WF1 + ((unit >> 3) * 2 + ((unit >> 2) & 1)) * 128;
            wait_flags(rs, fv, fs, ld4(rs, fv, fs), tag);
            float c0 = 0.f, c1 = 0.f;
#pragma unroll 1
            for (int k0 = 0; k0 < 28; k0 += 14) {
                v4u w[14];
#pragma unroll
                for (int ks = 0; ks < 14; ks++) w[ks] = ld16(rs, l16 + (ks & 3) * 1024, WS_W1T + (unit * 28 + k0 + (ks & ~3)) * 1024);
#pragma unroll
                for (int ks = 0; ks < 14; ks++) {
                    c0 = dot2bf(w[ks][0], 0x3f803f80u, c0); c1 = dot2bf(w[ks][1], 0x3f803f80u, c1);
                    c0 = dot2bf(w[ks][2], 0x3f803f80u, c0); c1 = dot2bf(w[ks][3], 0x3f803f80u, c1);
                }
            }
            st8(rs, lane < 32 ? WS_CS + lane * 8 : OOB, unit * 256, v2u{__builtin_bit_cast(u32, half_sum(c0 + c1)), tag});
        }
        STAMP(3);
        {
            const int tl = (b >> 4) & 1;
            if (wave == 0) {
                int spins = SPIN_CAP;
#pragma unroll 1
                while (ld4(rs, 0, WS_TR + (b - 224) * 128) != tag && --spins > 0) __builtin_amdgcn_s_sleep(4);
            }
            BARRIER();
            if (wave < 7) {
                const int gv = WS_HT + ((lane >> 2) * L + 1024 + tau * 64 + tl * 32) * 2 + (lane & 3) * 16;
                LAS char* as = sm + 4096 + wave * 4096 + (8 * h + q4) * 64 + G1 * 32 + pp * 8;
#pragma unroll
                for (int n = 0; n < 4; n++) glds16(srd, gv, (wave * 4 + n) * 65536, (u32)(size_t)sm + 4096 + wave * 4096 + n * 1024);
                wait_vm<0>();
                v4u f[4];
#pragma unroll
                for (int k = 0; k < 4; k++) f[k] = lds_tr2(as + k * 1024, as + k * 1024 + 256);
                float s1 = 0.f, s2 = 0.f, s1b = 0.f, s2b = 0.f;
#pragma unroll
                for (int k = 0; k < 4; k++) {
                    s1 = dot2bf(f[k][0], 0x3f803f80u, s1); s2 = dot2bf(f[k][0], f[k][0], s2);
                    s1b = dot2bf(f[k][1], 0x3f803f80u, s1b); s2b = dot2bf(f[k][1], f[k][1], s2b);
                    s1 = dot2bf(f[k][2], 0x3f803f80u, s1); s2 = dot2bf(f[k][2], f[k][2], s2);
                    s1b = dot2bf(f[k][3], 0x3f803f80u, s1b); s2b = dot2bf(f[k][3], f[k][3], s2b);
                }
                *(LAS f32x2*)(sm + wave * 512 + lane * 8) = f32x2{s1 + s1b, s2 + s2b};
            }
            BARRIER();
            if (wave == 0) {
                f32x2 t = *(LAS f32x2*)(sm + lane * 8);
#pragma unroll
                for (int k = 1; k < 7; k++) t += *(LAS f32x2*)(sm + k * 512 + lane * 8);
                const float mean = half_sum(t.x) * (1.f / D), ex2 = half_sum(t.y) * (1.f / D);
                const float var = rbf(fmaxf(ex2 - mean * mean, 0.f));
                st8(rs, lane < 32 ? WS_SG + col * 16 + tl * 8 : OOB, tau * 512, v2u{pk_bf16(mean, __builtin_amdgcn_sqrtf(rbf(var + 1e-5f))), tag});
            }
        }
        STAMP(4);
        STAMP_FLUSH();
        return;
    }
    asm volatile("" :: "v"(r_are), "v"(r_aim), "v"(r_bre), "v"(r_bim), "v"(r_cre), "v"(r_cim), "v"(r_dsk), "v"(cpf));
    SB();

    float unscale;
    {
        const float dt = rbf(__expf(ch ? bfhi(r_ldt) : bflo(r_ldt)));
        const float ar = dt * bflo(r_are), ai = dt * bflo(r_aim);
        const float br = dt * bflo(r_bre), bi = dt * bflo(r_bim);
        const float cr = bflo(r_cre), ci = bflo(r_cim);
        cf cb = {cr * br - ci * bi, -(cr * bi + ci * br)};
        const float a2 = ar * 1.44269504088896340736f;
        const float rh = ai * 0.15915494309189535f;
        const float rl = __builtin_fmaf(ai, 0.15915494309189535f, -rh) + ai * 6.4206382e-9f;
        auto cexp = [&](float s) __attribute__((always_inline)) -> cf {
            const float e = __builtin_amdgcn_exp2f(a2 * s);
            const float r = __builtin_amdgcn_fractf(rh * s) + rl * s;
            return cf{__builtin_amdgcn_cosf(r), __builtin_amdgcn_sinf(r)} * e;
        };
        const float grow = fmaxf(1.f, __builtin_amdgcn_exp2f(a2 * 2048.f));
        const float sb = wave_sum((fabsf(cb.x) + fabsf(cb.y)) * grow);
        *(LAS float*)(sm + LD_SB + wave * 4) = sb;
        int ex = (int)((__builtin_bit_cast(u32, sb) >> 23) & 255);
        ex = min(max(ex, 30), 230);
        unscale = __builtin_bit_cast(float, (u32)(127 - 15 - 126 + ex) << 23);
        cb *= __builtin_bit_cast(float, (u32)(127 + 15 + 126 - ex) << 23);

        const cf e1 = cexp(1.f), e16 = cexp(16.f), e256 = cexp(256.f);
        const cf e2 = cmul(e1, e1), e32 = cmul(e16, e16), e512 = cmul(e256, e256), x1 = cmulc(cb, e1), x256 = cmulc(cb, e256);
        SB();
        const cf e4 = cmul(e2, e2), e64 = cmul(e32, e32), e1024 = cmul(e512, e512), x2 = cmulc(cb, e2), x3 = cmulc(x1, e2);
        const cf x512 = cmulc(cb, e512), x768 = cmulc(x256, e512), y6 = cmul(e16, e32);
        SB();
        const cf e8 = cmul(e4, e4), e128 = cmul(e64, e64);
        const cf x4 = cmulc(cb, e4), x5 = cmulc(x1, e4), x6 = cmulc(x2, e4), x7 = cmulc(x3, e4), x258 = cmulc(x2, e256);
        const cf x1024 = cmulc(cb, e1024), x1280 = cmulc(x256, e1024), x1536 = cmulc(x512, e1024), x1792 = cmulc(x768, e1024);
        SB();
        auto put = [&](int q, cf v) __attribute__((always_inline)) { *(LAS u32*)(xyw + q * XY_STRIDE) = pkh(v); };
        cf y[8];
        y[0] = cf{1.f, 0.f}; y[1] = e8; y[2] = e16; y[3] = cmul(e8, e16); y[4] = e32; y[5] = cmul(e8, e32); y[6] = y6;
        const cf e192 = cmul(e128, e64), x8 = cmulc(x4, e4), x260 = cmulc(x4, e256), x262 = cmulc(x6, e256), x516 = cmulc(x4, e512);
        put(32, cb); put(33, x1); put(34, x2); put(35, x3); put(36, x4); put(37, x5); put(38, x6); put(39, x7);
        put(40, x256); put(41, x258);
        SB();
        y[7] = cmul(y[3], e32);
        const cf x264 = cmulc(x8, e256), x520 = cmulc(x8, e512), x772 = cmulc(x260, e512), x1032 = cmulc(x8, e1024);
        cf t[8];
#pragma unroll
        for (int q = 1; q < 7; q++) t[q] = cmul(y[q], e64);
        put(42, x260); put(43, x262); put(45, x512); put(46, x516);
#pragma unroll
        for (int q = 0; q < 6; q++) put(q, y[q]);
        SB();
        t[7] = cmul(y[7], e64);
        const cf x776 = cmulc(x264, e512), x1288 = cmulc(x264, e1024), x1544 = cmulc(x520, e1024);
        cf t2[8];
#pragma unroll
        for (int q = 1; q < 8; q++) t2[q] = cmul(y[q], e128);
        put(6, y[6]); put(7, y[7]); put(8, e64);
#pragma unroll
        for (int q = 1; q < 6; q++) put(8 + q, t[q]);
        put(44, x264); put(47, x520); put(48, x768); put(49, x772); put(51, x1024); put(52, x1032);
        SB();

        const cf x1800 = cmulc(x776, e1024);
        cf t3[8];
#pragma unroll
        for (int q = 1; q < 8; q++) t3[q] = cmul(y[q], e192);
        put(14, t[6]); put(15, t[7]); put(16, e128);
#pragma unroll
        for (int q = 1; q < 8; q++) put(16 + q, t2[q]);
        put(50, x776); put(53, x1280); put(54, x1288); put(55, x1536); put(56, x1544); put(57, x1792);
        SB();
        put(24, e192);
#pragma unroll
        for (int q = 1; q < 8; q++) put(24 + q, t3[q]);
        put(58, x1800);
    }
    SB();
    STAMP(2);
    asm volatile("" :: "v"(tin[7]));
    if (w1task >= 0) w1_task_store(rs, w1task, lane, tin);
    SB();
    const int t16 = tid * 16;
    v4u gx[2], gs[2];
    auto gr_load = [&]() __attribute__((always_inline)) {
        gx[0] = ld16(rs, t16, WS_XX + pair * 16384); gx[1] = ld16(rs, t16, WS_XX + pair * 16384 + 8192);
        gs[0] = ld16(rs, t16, WS_ST1); gs[1] = ld16(rs, t16, WS_ST1 + 8192);
    };
    const bool gr_first = (u32)__builtin_amdgcn_s_memrealtime() - tstart >= GR_T;
    if (gr_first) gr_load();
    SB();
    {
        f32x16 kd;
#pragma unroll
        for (int i2 = 0; i2 < 16; i2++) kd[i2] = 0.f;
        v4u fa[8], fb[8];
#pragma unroll
        for (int s = 0; s < 8; s++) { fa[s] = *(LAS v4u*)(pa + s * 32); fb[s] = *(LAS v4u*)(pb + s * 32); }
        SB();
#pragma unroll
        for (int s = 0; s < 8; s++) kd = mfma_f16(fa[s], fb[s], kd);
        SB();

#pragma unroll
        for (int g2 = 0; g2 < 4; g2++)
            *(LAS f32x4*)(exw + g2 * 1024) = f32x4{kd[4 * g2], kd[4 * g2 + 1], kd[4 * g2 + 2], kd[4 * g2 + 3]} * unscale;
    }
    if (w1task >= 0) { wait_vm<0>(); st4(rs, flv, w1_flag(w1task), tag); }
    if (!gr_first) {
        while ((u32)__builtin_amdgcn_s_memrealtime() - tstart < GR_T) __builtin_amdgcn_s_sleep(2);
        gr_load();
    }
    SB();
    BARRIER();
    __builtin_amdgcn_s_setprio(0);
    if (GR_EARLY == 0) gr_load();
    const int l28 = (col < 28 ? col : 0) * 4;
    SB();
    STAMP(3);
    float ik;
    {
        const f32x4 s0 = *(LAS f32x4*)(sm + LD_SB + ch * 16);
        const f32x4 a0 = *(LAS f32x4*)(exr), a1 = *(LAS f32x4*)(exr + XY_WAVE), a2 = *(LAS f32x4*)(exr + 2 * XY_WAVE), a3 = *(LAS f32x4*)(exr + 3 * XY_WAVE);
        const float b0 = (s0[0] + s0[1]) + (s0[2] + s0[3]);
        int e0 = (int)((__builtin_bit_cast(u32, b0) >> 23) & 255);
        e0 = min(max(e0, 30), 230);
        ik = __builtin_bit_cast(float, (u32)(127 - 15 - 126 + e0) << 23);
        const float sk = __builtin_bit_cast(float, (u32)(127 + 15 + 126 - e0) << 23);
        const f32x4 v = ((a0 + a1) + (a2 + a3)) * sk;
        const u32 p01 = pk_f16(v[0], v[1]), p23 = pk_f16(v[2], v[3]);
        LAS char* o = kpw;
        if (wq == 0) {
            *(LAS u16*)(o) = (u16)p01; *(LAS u16*)(o + 1536) = (u16)(p01 >> 16);
            *(LAS u16*)(o + 2 * 1536) = (u16)p23; *(LAS u16*)(o + 3 * 1536) = (u16)(p23 >> 16);
        } else if (wq == 1) {
            *(LAS u16*)(o) = (u16)p01; *(LAS u16*)(o + 1536) = (u16)p01;
            *(LAS u16*)(o + 2 * 1536) = (u16)(p01 >> 16); *(LAS u16*)(o + 3 * 1536) = (u16)p23;
        } else if (wq == 2) {
            *(LAS u32*)(o) = p01; *(LAS u32*)(o + 1536) = p01;
            *(LAS u32*)(o + 2 * 1536) = (p01 & 0xffffu) | (p23 << 16);
            *(LAS u32*)(o + 3 * 1536) = (p01 >> 16) | (p23 << 16);
        } else {
            const bool ev = (col & 1) == 0;
            *(LAS u32*)(o) = ev ? ((p01 & 0xffffu) | (p01 << 16)) : p01;
            *(LAS u32*)(o + 1536) = p01; *(LAS u32*)(o + 2 * 1536) = p01; *(LAS u32*)(o + 3 * 1536) = p01;
            *(LAS u32*)(o + 128) = ev ? ((p23 & 0xffffu) | (p23 << 16)) : p23;
            *(LAS u32*)(o + 128 + 1536) = p23; *(LAS u32*)(o + 128 + 2 * 1536) = p23; *(LAS u32*)(o + 128 + 3 * 1536) = p23;
        }
    }
    SB();
    auto gr_ok = [&]() __attribute__((always_inline)) {
        const u32 a = (gx[0][1] ^ tag) | (gx[0][3] ^ tag) | (gx[1][1] ^ tag) | (gx[1][3] ^ tag);
        const u32 c = (gs[0][1] ^ tag) | (gs[0][3] ^ tag) | (gs[1][1] ^ tag) | (gs[1][3] ^ tag);
        return __all((a | c) == 0);
    };
    if (!gr_ok()) {
        int spins = SPIN_CAP;
#pragma unroll 1
        do {
            __builtin_amdgcn_s_sleep(4);
            gr_load();
        } while (!gr_ok() && --spins > 0);
    }
    STAMP(4);
    v4u wf[28];
    const int w1off = WS_W1T + (phi * 8 + wave) * 28 * 1024;
    int seg7[4];
#pragma unroll
    for (int i = 0; i < 4; i++) seg7[i] = ((i + i28 * ROT_W1) & 3) * 7;
    auto w1_load = [&](int ks) __attribute__((always_inline)) {
        wf[ks] = ld16(rs, l16 + ((ks % 7) & 3) * 1024, w1off + seg7[ks / 7] * 1024 + ((ks % 7) >> 2) * 4096);
    };
    const int wf_s = WS_WF1 + phi * 256;
    u32 wfl;
    if (WFL_N < 0) wfl = ld4(rs, l28 + h * 128, wf_s);
    SB();
    u32 xc[4], udk[4];
    {
        float u0[4], u1[4];
#pragma unroll
        for (int i = 0; i < 4; i++) {
            xc[i] = gx[i >> 1][2 * (i & 1)];
            const u32 st = gs[i >> 1][2 * (i & 1)];
            const float m = bflo(st), r = __builtin_amdgcn_rcpf(bfhi(st));
            const u32 dd = pk_bf16(bflo(xc[i]) - m, bfhi(xc[i]) - m);
            const u32 uu = pk_bf16(bflo(dd) * r, bfhi(dd) * r);
            u0[i] = bflo(uu); u1[i] = bfhi(uu);
        }
        *(LAS v2u*)(uw) = v2u{pk_f16(u0[0], u0[1]), pk_f16(u0[2], u0[3])};
        *(LAS v2u*)(uw + 4160) = v2u{pk_f16(u1[0], u1[1]), pk_f16(u1[2], u1[3])};
#pragma unroll
        for (int i = 0; i < 4; i++) udk[i] = pk_bf16(u0[i] * bflo(r_dsk), u1[i] * bfhi(r_dsk));
    }
    BARRIER();
    STAMP(5);
    f32x4 r0, r1, l3;
    f32x2 r2, l2;
    float l1, r3;
    v4u xu;
    u32 ya, yb;
    const int c1 = tid & 255;
    LAS char* q1 = opaque(sm + ch * 4 * P_WAVE + ((c1 >> 3) + 1) * 144 + (c1 & 7) * 16);
    LAS char* q0 = opaque(sm + ch * 4 * P_WAVE + (((c1 - 1) >> 3) + 1) * 144 + ((c1 - 1) & 7) * 16);
    LAS char* xur = opaque(sm + LD_XU + ch * 8192 + c1 * 16);
    auto ce_read = [&](int hf) __attribute__((always_inline)) {
        const int pb_ = hf ? LD_PB : LD_PA;
        r0 = *(LAS f32x4*)(q1 + pb_);
        r1 = *(LAS f32x4*)(q1 + pb_ + P_WAVE); l1 = *(LAS float*)(q0 + pb_ + P_WAVE + 12);
        r2 = *(LAS f32x2*)(q1 + pb_ + 2 * P_WAVE); l2 = *(LAS f32x2*)(q0 + pb_ + 2 * P_WAVE + 8);
        r3 = *(LAS float*)(q1 + pb_ + 3 * P_WAVE); l3 = *(LAS f32x4*)(q0 + pb_ + 3 * P_WAVE);
        xu = *(LAS v4u*)(xur + hf * 4096);
    };
    auto ce_part = [&](int hf, int part) __attribute__((always_inline)) {
        if (part == 0) ya = pk_bf16s(((r0[0] + l2[0]) + (l1 + l3[1])) * ik, ((r0[1] + l2[1]) + (r1[0] + l3[2])) * ik);
        if (part == 1) yb = pk_bf16s(((r0[2] + r2[0]) + (r1[1] + l3[3])) * ik, ((r0[3] + r2[1]) + (r1[2] + r3)) * ik);
        if (part == 2) ya = pk_bf16s(bflo(ya) + bfhi(xu[0]), bfhi(ya) + bfhi(xu[1]));
        if (part == 3) yb = pk_bf16s(bflo(yb) + bfhi(xu[2]), bfhi(yb) + bfhi(xu[3]));
        if (part == 4) ya = pk_bf16s(bflo(ya) + bflo(xu[0]), bfhi(ya) + bflo(xu[1]));
        if (part == 5) {
            yb = pk_bf16s(bflo(yb) + bflo(xu[2]), bfhi(yb) + bflo(xu[3]));
            st8(rs, WS_HT + c1 * 8, (d0 + ch) * (L * 2) + hf * 2048, v2u{ya, yb});
        }
    };
    const int hf_set = WS_HF + (wq * 8 + (((d0 + ch) >> 3) & 7)) * 256 + (((d0 + ch) >> 6) * 8 + ((d0 + ch) & 7)) * 4;
    u32 pf1;
    {
        f32x16 acc0, acc1;
#pragma unroll
        for (int i = 0; i < 16; i++) { acc0[i] = 0.f; acc1[i] = 0.f; }
        constexpr int PD = 3;
        v4u fu[PD + 1], fk[16], fk1[PD + 1];
        auto issue = [&](int n) __attribute__((always_inline)) {
            const int B0 = (n >= 16 && n < 32) ? 2016 - 64 * (n - 16) : 992 - 64 * (n & 15);
            fu[n % (PD + 1)] = lds_tr2(ua + 2 * (B0 - 32) + 32, ua + 2 * (B0 - 32));
            if (n < 16) fk[n] = *(LAS v4u*)(ka + 32 * n);
            if (n >= 32) fk1[n % (PD + 1)] = *(LAS v4u*)(ka + 32 * (n - 16));
        };
#pragma unroll
        for (int n = 0; n < PD; n++) issue(n);
        SB();
        static_for<0, 48>([&](auto N_) __attribute__((always_inline)) {
            constexpr int n = decltype(N_)::v;
            if (n + PD < 48) issue(n + PD);
            if constexpr (n < 16) acc0 = mfma_f16(fu[n % (PD + 1)], fk[n], acc0);
            else if constexpr (n < 32) acc1 = mfma_f16(fu[n % (PD + 1)], fk[n - 16], acc1);
            else acc1 = mfma_f16(fu[n % (PD + 1)], fk1[n % (PD + 1)], acc1);
            if (n == 1) *(LAS v4u*)(sm + LD_XU + t16) = v4u{__builtin_amdgcn_perm(udk[0], xc[0], 0x05040100u), __builtin_amdgcn_perm(udk[1], xc[1], 0x05040100u),
                                                           __builtin_amdgcn_perm(udk[2], xc[2], 0x05040100u), __builtin_amdgcn_perm(udk[3], xc[3], 0x05040100u)};
            if (n == 2) *(LAS v4u*)(sm + LD_XU + 8192 + t16) = v4u{__builtin_amdgcn_perm(udk[0], xc[0], 0x07060302u), __builtin_amdgcn_perm(udk[1], xc[1], 0x07060302u),
                                                                  __builtin_amdgcn_perm(udk[2], xc[2], 0x07060302u), __builtin_amdgcn_perm(udk[3], xc[3], 0x07060302u)};
            if (n == WFL_N) wfl = ld4(rs, l28 + h * 128, wf_s);
            if (n == W1_CHK) {
                PIN(acc0); SB();
                wait_flags(rs, l28 + h * 128, wf_s, wfl, tag);
                pf1 = PF1 ? ld4(rs, tid < 448 ? tid * 128 : OOB, WS_W1T + phi * 229376 + (i28 & 3) * 57344) : 0;
                STAMP(18);
            }
            if constexpr (n == 16 || n == 17) {
                constexpr int g = 2 * (n - 16);
                *(LAS f32x4*)(pw + LD_PA + g * 32) = f32x4{acc0[4 * g], acc0[4 * g + 1], acc0[4 * g + 2], acc0[4 * g + 3]};
                *(LAS f32x4*)(pw + LD_PA + g * 32 + 32) = f32x4{acc0[4 * g + 4], acc0[4 * g + 5], acc0[4 * g + 6], acc0[4 * g + 7]};
            }
            if (n == 17) {
                PIN(acc1);
                if (lane == 63) {
                    *(LAS f32x4*)(sm + LD_PA + wave * P_WAVE + 112) = f32x4{0.f, 0.f, 0.f, 0.f};
                    *(LAS f32x4*)(sm + LD_PB + wave * P_WAVE + 112) = f32x4{acc0[12], acc0[13], acc0[14], acc0[15]};
                }
            }
            if (n == 18) {
                BARRIER();
                if (PRIO_C) __builtin_amdgcn_s_setprio(1);
                STAMP(19);
                st4(rs, (b == 0 && tid == 0) ? WS_EPOCH : OOB, 0, tag);
                ce_read(0);
            }
            if (n >= 21 && n < 27) ce_part(0, n - 21);
            if (n == HF_N) {
                STAMP(22);
                if (!HF_OPT) wait_vm<w1_cnt(HF_N) - w1_cnt(26)>();
                st4(rs, flv, hf_set, tag);
                if (PRIO_C) __builtin_amdgcn_s_setprio(0);
                STAMP(23);
            }
            SB();
#pragma unroll
            for (int ks = w1_cnt(n); ks < w1_cnt(n + 1); ks++) w1_load(ks);
            SB();
        });
#pragma unroll
        for (int g = 0; g < 4; g++) *(LAS f32x4*)(pw + LD_PB + g * 32) = f32x4{acc1[4 * g], acc1[4 * g + 1], acc1[4 * g + 2], acc1[4 * g + 3]};
    }
    constexpr int W1_A = w1_cnt(48) + W1_MID > 28 ? 28 : w1_cnt(48) + W1_MID;
#pragma unroll
    for (int ks = w1_cnt(48); ks < W1_A; ks++) { w1_load(ks); SB(); }
    const int kap = phi * 8 + wave;
    const u32 w2fl = ld4(rs, l28, WS_WF2 + del * 128);
    v2u csg = __builtin_amdgcn_raw_buffer_load_b64(rs, WS_CS + col * 8, kap * 256, SC1);
    u32 hfl;
    if (HFL_EARLY) hfl = ld4(rs, hf_v, hf_s);
    BARRIER();
    STAMP(6);
    ce_read(1);
    int rot14[14], rot7[7];
    {
        int i = (2 * del + ((i28 & 3) * 7 >> 1)) * ROT_W2;
        i = i >= 14 ? i - 14 : i;
#pragma unroll
        for (int n = 0; n < 14; n++) { rot14[n] = i * 1024; i = i == 13 ? 0 : i + 1; }
        i = del * ROT_H;
#pragma unroll
        for (int n = 0; n < 7; n++) { rot7[n] = i * 8192; i = i == 6 ? 0 : i + 1; }
    }
    auto load_h = [&](int hf, int n) __attribute__((always_inline)) {
        glds16(srd, hvoff + hf * 2048, rot7[n] * 32, (u32)(size_t)sm + (hf ? LD_HB : LD_HA) + wave * 1024 + rot7[n]);
    };
#pragma unroll
    for (int i = 0; i < 5; i++) ce_part(1, i);
    SB();
    if (!HFL_EARLY) hfl = ld4(rs, hf_v, hf_s);
    SB();
    ce_part(1, 5);
    SB();
    STAMP(21);
#pragma unroll
    for (int ks = W1_A; ks < 28; ks++) { w1_load(ks); SB(); }
    wait_flags(rs, l28, WS_WF2 + del * 128, w2fl, tag);
    if (!__all(csg[1] == tag)) {
        int spins = SPIN_CAP;
#pragma unroll 1
        do {
            __builtin_amdgcn_s_sleep(4);
            csg = __builtin_amdgcn_raw_buffer_load_b64(rs, WS_CS + col * 8, kap * 256, SC1);
        } while (!__all(csg[1] == tag) && --spins > 0);
    }
    *(LAS u32*)(sm + LD_CS + wave * 128 + col * 4) = csg[0];
    wait_flags(rs, hf_v, hf_s, hfl, tag);
    STAMP(7);
#pragma unroll
    for (int n = 0; n < 7; n++) load_h(0, n);
    SB();
    wait_vm<7>();
    st4(rs, flv, hf_set + 8192, tag);
    SB();
    asm volatile("" :: "v"(pf1));
    wait_vm<1>();
    STAMP(8);
    BARRIER();
    STAMP(9);
    v4u w2[28];
    const int w2v = WS_W2T + (del * 2 * 112 + wave * 14) * 1024 + l16;
    auto w2_lds = [&](int f) __attribute__((always_inline)) { return f < 14 ? LD_PB + (wave * 4 + f - 10) * 1024 : LD_HA + (wave * 7 + f - 14) * 1024; };
    auto w2_load = [&](int f) __attribute__((always_inline)) {
        if (f >= 10 && f < 21) glds16(srd, w2v + (f & 1) * 112 * 1024, rot14[f >> 1], (u32)(size_t)sm + w2_lds(f));
        else w2[f] = ld16(rs, w2v + (f & 1) * 112 * 1024, rot14[f >> 1]);
    };
    const int af_v = (lane < 14 ? lane : 0) * 4, af_s = WS_AF + (tau * 16 + wave) * 128;
    const int av = WS_ACT + (tau2 * 112 + wave * 14) * 1024 + l16;
    const int dd = 64 * del + 8 * wave + 4 * h;
    LAS char* hra = sm + LD_HA + dd * 128 + trl + (((xcd & 1) << 6) ^ sw);
    v2u hr[2];
    u32 afl;
    v4u a0[14], a1[14];
    const int af_set = WS_AF + (tau * 16 + kap / 14) * 128 + (kap % 14) * 4;
    {
        LAS char* at0 = opaque(sm + LD_HA + 8 * h * 128 + trl + sw);
        LAS char* at1 = opaque(sm + LD_HA + 8 * h * 128 + trl + (64 ^ sw));
        LAS char* csr = opaque(sm + LD_CS + wave * 128 + h * 16);
        LAS char* as = opaque(sm + LD_HA + 8 * h * 128 + trl + (((wave & 1) << 6) ^ sw) + (wave >> 1) * 7 * 2048);
        LAS char* str = opaque(sm + LD_ST + col * 32);
        LAS char* cur;
        constexpr int PD = 4;
        v4u fr[PD + 1], sf, sf2;
        auto issue = [&](int n) __attribute__((always_inline)) {
            const int tile = n / 28, j = n % 28;
            if (j % 7 == 0) cur = opaque(((tile & 1) ? at1 : at0) + (seg7[j / 7] * 2048 + (tile >> 1) * 57344));
            LAS char* a = cur + (j % 7) * 2048;
            fr[n % (PD + 1)] = lds_tr2(a, a + 512);
        };
        f32x16 acc[2];
        float nm, k0, k1, sd2, eg, eu, ez, er, eo0;
        f32x4 cg4, cu4;
        f32x4 sta, stb;
        float s1, s2, s1b, s2b;
        u32 opk[4];
        auto stat_acc = [&](int k, int part, int which = 0) __attribute__((always_inline)) {
            const u32 f0 = which ? sf2[2 * part] : sf[2 * part], f1 = which ? sf2[2 * part + 1] : sf[2 * part + 1];
            if (k == 0 && part == 0) { s1 = 0.f; s2 = 0.f; s1b = 0.f; s2b = 0.f; }
            s1 = dot2bf(f0, 0x3f803f80u, s1); s2 = dot2bf(f0, f0, s2);
            s1b = dot2bf(f1, 0x3f803f80u, s1b); s2b = dot2bf(f1, f1, s2b);
        };
        auto stat_pin = [&]() __attribute__((always_inline)) { asm volatile("" : "+v"(s1), "+v"(s2), "+v"(s1b), "+v"(s2b)); };
        auto stat_write = [&](int hf) __attribute__((always_inline)) {
            const float a = half_sum(s1 + s1b), c = half_sum(s2 + s2b);
            *(LAS f32x2*)(str + (hf * 2 + (wave & 1)) * 1024 + (wave >> 1) * 8) = f32x2{a, c};
        };
        auto epi_read = [&](int tile) __attribute__((always_inline)) { sta = *(LAS f32x4*)(str + tile * 1024); stb = *(LAS f32x4*)(str + tile * 1024 + 16); };
        auto epi_stats = [&]() __attribute__((always_inline)) {
            float m = ((sta[0] + sta[2]) + (stb[0] + stb[2])) * (1.f / D), e2 = ((sta[1] + sta[3]) + (stb[1] + stb[3])) * (1.f / D);
            asm("" : "+v"(m)); asm("" : "+v"(e2));
            const float var = rbf(fmaxf(e2 - m * m, 0.f));
            const float sd = rbf(__builtin_amdgcn_sqrtf(rbf(var + 1e-5f))), r = __builtin_amdgcn_rcpf(sd);
            nm = -rbf(m); sd2 = sd * sd; k0 = r * -2.3022082f; k1 = (r * r) * (r * -0.10294324f);
        };
        v4u sg;
        auto epi_stats2 = [&](u32 st) __attribute__((always_inline)) {
            const float sd = bfhi(st), r = __builtin_amdgcn_rcpf(sd);
            nm = -bflo(st); sd2 = sd * sd; k0 = r * -2.3022082f; k1 = (r * r) * (r * -0.10294324f);
        };
        auto cs_read = [&](int g) __attribute__((always_inline)) { cg4 = *(LAS f32x4*)(csr + g * 32); cu4 = *(LAS f32x4*)(csr + 64 + g * 32); };
        auto slice = [&](const f32x16& a, int sidx) __attribute__((always_inline)) {
            const int e = sidx / 3, st = sidx % 3;
            if (st == 0) {
                if (e > 0) { float o = eu * er; asm("" : "+v"(o)); if ((e - 1) & 1) opk[(e - 1) >> 1] = pk_bf16(eo0, o); else eo0 = o; }
                if (e < 8) {
                    eg = __builtin_fmaf(nm, cg4[e & 3], a[e & 7]); eu = __builtin_fmaf(nm, cu4[e & 3], a[8 + (e & 7)]);
                    asm("" : "+v"(eg), "+v"(eu));
                }
            } else if (st == 1) { ez = __builtin_amdgcn_exp2f(eg * __builtin_fmaf(eg * eg, k1, k0)); asm("" : "+v"(ez)); }
            else { er = __builtin_amdgcn_rcpf(__builtin_fmaf(ez, sd2, sd2)); eu *= eg; asm("" : "+v"(er), "+v"(eu)); }
        };
        auto epi_four = [&](const f32x16& a, int g) __attribute__((always_inline)) {
            const f32x4 nm4 = {nm, nm, nm, nm}, sd4 = {sd2, sd2, sd2, sd2};
            const f32x4 gg = __builtin_elementwise_fma(nm4, cg4, f32x4{a[4 * g], a[4 * g + 1], a[4 * g + 2], a[4 * g + 3]});
            const f32x4 u = __builtin_elementwise_fma(nm4, cu4, f32x4{a[8 + 4 * g], a[9 + 4 * g], a[10 + 4 * g], a[11 + 4 * g]});
            const f32x4 z = gg * __builtin_elementwise_fma(gg * gg, f32x4{k1, k1, k1, k1}, f32x4{k0, k0, k0, k0});
            const f32x4 e = {__builtin_amdgcn_exp2f(z.x), __builtin_amdgcn_exp2f(z.y), __builtin_amdgcn_exp2f(z.z), __builtin_amdgcn_exp2f(z.w)};
            const f32x4 dn = __builtin_elementwise_fma(e, sd4, sd4);
            const f32x4 o = (gg * u) * f32x4{__builtin_amdgcn_rcpf(dn.x), __builtin_amdgcn_rcpf(dn.y), __builtin_amdgcn_rcpf(dn.z), __builtin_amdgcn_rcpf(dn.w)};
            opk[2 * g] = pk_bf16(o.x, o.y); opk[2 * g + 1] = pk_bf16(o.z, o.w);
        };
        auto epi_store = [&](int tile) __attribute__((always_inline)) {
            const int tok32 = (tile >> 1) * 32 + tau * 2 + (tile & 1);
            st16(rs, WS_ACT + l16 + (tok32 * 112 + kap) * 1024, v4u{opk[0], opk[1], opk[2], opk[3]});
        };
#pragma unroll
        for (int n = 0; n < PD; n++) issue(n);
        SB();
        static_for<0, 112>([&](auto N_) __attribute__((always_inline)) {
            constexpr int n = decltype(N_)::v;
            constexpr int tile = n / 28, j = n % 28;
            if (n == 52) {
                STAMP(27);
                wait_vm<g1_cum(52) - g1_cum(HB_N + 8)>();
                BARRIER();
                if (PRIO_G) __builtin_amdgcn_s_setprio(1);
                STAMP(28);
            }
            if (n + PD < 112) issue(n + PD);
            if (j == 0) {
                f32x16 z;
#pragma unroll
                for (int i = 0; i < 16; i++) z[i] = 0.f;
                acc[tile & 1] = mfma_bf16(wf[0], fr[n % (PD + 1)], z);
            } else {
                acc[tile & 1] = mfma_bf16(wf[j], fr[n % (PD + 1)], acc[tile & 1]);
            }
            if (MFMA_FIRST) SB();
            if constexpr (n < 28) {
                constexpr int sk = n;
                if (sk < 21 && sk % 3 == 0) { if (sk > 0) { stat_acc(sk / 3 - 1, 1); SB(); } sf = lds_tr2(as + (sk / 3) * 2048, as + (sk / 3) * 2048 + 512); }
                if (sk < 21 && sk % 3 == 2) stat_acc(sk / 3, 0);
                if (sk == 21) stat_acc(6, 1);
                if (sk == 22) stat_write(0);
            }
            if (n == HBP_N) hfl = ld4(rs, hf_v, hf_s + 8192);
            if (n == HB_N) {
                PIN(acc[0]);
                SB();
                STAMP(24);
                wait_flags(rs, hf_v, hf_s + 8192, hfl, tag);
                STAMP(25);
            }
            if (n == 48) hr[0] = lds_tr(hra);
            if (n == 27) { STAMP(26); BARRIER_RAW(); STAMP(29); }
            if (n == 27 || n == 55) epi_read(tile);
            if (n == 84) {
                PIN(acc[0]); PIN(acc[1]);
                SB();
                STAMP(30);
                if (!__all(sg[1] == tag && sg[3] == tag)) {
                    int spins = SPIN_CAP;
#pragma unroll 1
                    do {
                        __builtin_amdgcn_s_sleep(2);
                        sg = ld16(rs, WS_SG + col * 16, tau * 512);
                    } while (!__all(sg[1] == tag && sg[3] == tag) && --spins > 0);
                }
                STAMP(31);
            }
            if (tile == 1) {
                if (j == 0) cs_read(0);
                if (j == 1) epi_stats();
                if (j >= 2 && j < 27) slice(acc[0], j - 2);
                if (j == 11) { SB(); cs_read(1); }
            }
            if (tile == 2) {
                if (j == 0) cs_read(0);
                if (j == 1) epi_stats();
                if (j >= 2 && j < 10) { slice(acc[1], 3 * (j - 2)); if (j == 5) { SB(); cs_read(1); } slice(acc[1], 3 * (j - 2) + 1); slice(acc[1], 3 * (j - 2) + 2); }
                if (j == 10) slice(acc[1], 24);
            }
            if (tile == 3) {
                if (j == 0) cs_read(0);
                if (j == 1) epi_stats2(sg[0]);
                if (j >= 2 && j < 27) slice(acc[0], j - 2);
                if (j == 11) { SB(); cs_read(1); }
            }
            if (n == AFC_N) {
                PIN(acc[1]); asm volatile("" : "+v"(eg), "+v"(eu), "+v"(ez), "+v"(er), "+v"(eo0), "+v"(opk[0]), "+v"(opk[1]));
                SB();
                wait_flags(rs, af_v, af_s, afl, tag);
            }
            constexpr int op = g1_op(n);
            if constexpr (op >= 60) a0[op - 60] = ld16(rs, av, rot14[op - 60]);
            if constexpr (op >= 0 && op < 28) w2_load(op);
            if constexpr (op >= 32 && op < 40) load_h(1, op - 32);
            if constexpr (op >= 40 && op < 50) epi_store(op - 40);
            if constexpr (op == 50) { wait_vm<g1_cum(AF_N) - g1_cum(ST1_N + 1)>(); st4(rs, flv, af_set, tag); if (PRIO_G) __builtin_amdgcn_s_setprio(0); if (BAR_AF) BARRIER_RAW(); }
            if constexpr (op == 51) afl = ld4(rs, af_v, af_s);
            if constexpr (op == 52) st4(rs, (tid == 0 && del == 0) ? 0 : OOB, WS_TR + ((xcd & 1) * 16 + tau) * 128, tag);
            if constexpr (op == 53) sg = ld16(rs, WS_SG + col * 16, tau * 512);
            SB();
        });
        PIN(acc[1]);
        STAMP(10);
        STAMP(11);
        cs_read(0);
        epi_stats2(sg[2]);
        epi_four(acc[1], 0);
        cs_read(1);
        epi_four(acc[1], 1);
        epi_store(3);
        if (OPT) { SB(); st4(rs, flv, af_set + 1024, tag); }
    }
    SB();
#pragma unroll
    for (int n = A0_IN; n < 14; n++) { a0[n] = ld16(rs, av, rot14[n]); SB(); }
    hr[1] = lds_tr(hra + 57344);
#pragma unroll
    for (int f = 10; f < 21; f++) w2[f] = *(LAS v4u*)(sm + w2_lds(f) + l16);
    STAMP(12);
    BARRIER();
    f32x16 acc[2][2];
#pragma unroll
    for (int i = 0; i < 16; i++) { acc[0][0][i] = 0.f; acc[0][1][i] = 0.f; acc[1][0][i] = 0.f; acc[1][1][i] = 0.f; }
    LAS char* rw = opaque(sm + wave * 1024 + l16);
    LAS char* rr = opaque(sm + wave * 8192 + l16);
    auto red_write = [&](int hf) __attribute__((always_inline)) {
#pragma unroll
        for (int o = 0; o < 8; o++) {
            const f32x16& t = acc[o >> 2][hf];
            const int r = 4 * (o & 3);
            *(LAS f32x4*)(rw + hf * 65536 + o * 8192) = f32x4{t[r], t[r + 1], t[r + 2], t[r + 3]};
        }
    };
    auto red_finish = [&](int hf) __attribute__((always_inline)) {
        f32x4 v[8];
#pragma unroll
        for (int s = 0; s < 8; s++) v[s] = *(LAS f32x4*)(rr + hf * 65536 + s * 1024);
        const f32x4 t = ((v[0] + v[1]) + (v[2] + v[3])) + ((v[4] + v[5]) + (v[6] + v[7]));
        const u32 ta = pk_bf16(t[0], t[1]), tb = pk_bf16(t[2], t[3]);
        const int tok = 1024 * hf + 32 * tau2 + col;
        *(v2u*)(p.out + (size_t)tok * D + dd) = v2u{pk_bf16(bflo(ta) + bflo(hr[hf][0]), bfhi(ta) + bfhi(hr[hf][0])),
                                                    pk_bf16(bflo(tb) + bflo(hr[hf][1]), bfhi(tb) + bfhi(hr[hf][1]))};
    };
#pragma unroll
    for (int n = 0; n < 14; n++) {
        acc[0][0] = mfma_bf16(w2[2 * n], a0[n], acc[0][0]);
        acc[1][0] = mfma_bf16(w2[2 * n + 1], a0[n], acc[1][0]);
        if (!OPT && n == AFB_N) { wait_vm<14 - A0_IN>(); st4(rs, flv, af_set + 1024, tag); }
        if (TAIL && n == AFBP_N) afl = ld4(rs, af_v, af_s + 1024);
        SB();
    }
    PIN(acc[0][0]); PIN(acc[1][0]);
    STAMP(13);
    red_write(0);
    BARRIER();
    afl = ld4(rs, af_v, af_s + 1024);
    SB();
    red_finish(0);
    SB();
    wait_flags(rs, af_v, af_s + 1024, afl, tag);
    STAMP(14);
#pragma unroll
    for (int n = 0; n < 14; n++) { a1[n] = ld16(rs, av + 32 * 112 * 1024, rot14[n]); SB(); }
    SB();
    STAMP(15);
#pragma unroll
    for (int n = 0; n < 14; n++) {
        acc[0][1] = mfma_bf16(w2[2 * n], a1[n], acc[0][1]);
        acc[1][1] = mfma_bf16(w2[2 * n + 1], a1[n], acc[1][1]);
        SB();
    }
    STAMP(16);
    red_write(1);
    BARRIER();
    if (TAIL == 2) red_finish(0);
    red_finish(1);
    STAMP(17);
    STAMP_FLUSH();
    asm volatile(".Lfb_code_end:");
}

}

extern "C" void kernel_launch(void* const* d_in, const int* in_sizes, int n_in,
                              void* d_out, int out_size, void* d_ws, size_t ws_size,
                              hipStream_t stream) {
    Params p;
    p.x = (const u16*)d_in[0]; p.Are = (const u16*)d_in[1]; p.Aim = (const u16*)d_in[2];
    p.Bre = (const u16*)d_in[3]; p.Bim = (const u16*)d_in[4]; p.Cre = (const u16*)d_in[5]; p.Cim = (const u16*)d_in[6];
    p.logdt = (const u16*)d_in[7]; p.Dskip = (const u16*)d_in[8];
    p.wg = (const u16*)d_in[10]; p.wu = (const u16*)d_in[11]; p.wd = (const u16*)d_in[12];
    p.ws = (char*)d_ws; p.out = (u16*)d_out;
    (void)hipFuncSetAttribute((const void*)fused_block, hipFuncAttributeMaxDynamicSharedMemorySize, LDS_BYTES);
    fused_block<<<256, 512, LDS_BYTES, stream>>>(p);
}
```

```cpp
#include <hip/hip_runtime.h>
#include <stdint.h>

namespace {

constexpr int L = 2048, D = 448, N = 256, F = 1792;

typedef unsigned short u16;
typedef unsigned int u32;
typedef unsigned long long u64;
typedef u32 v4u __attribute__((ext_vector_type(4)));
typedef u32 v2u __attribute__((ext_vector_type(2)));
typedef float f32x2 __attribute__((ext_vector_type(2)));
typedef float f32x4 __attribute__((ext_vector_type(4)));
typedef float f32x16 __attribute__((ext_vector_type(16)));
typedef _Float16 f16x2 __attribute__((ext_vector_type(2)));
typedef _Float16 f16x8 __attribute__((ext_vector_type(8)));
typedef __bf16 bf16x2 __attribute__((ext_vector_type(2)));
typedef __bf16 bf16x8 __attribute__((ext_vector_type(8)));
typedef short s16x4 __attribute__((ext_vector_type(4)));
#define GAS __attribute__((address_space(1)))
#define LAS __attribute__((address_space(3)))
#define RLX_AGENT __ATOMIC_RELAXED, __HIP_MEMORY_SCOPE_AGENT
#define SB() __builtin_amdgcn_sched_barrier(0)
#define PIN(x) asm volatile("" : "+v"(x))
#define BARRIER() asm volatile("s_waitcnt lgkmcnt(0)\n\ts_barrier" ::: "memory")
#define BARRIER_RAW() asm volatile("s_barrier" ::: "memory")
constexpr int SC1 = 16;

constexpr int WS_EPOCH = 0;
constexpr int WS_WF2 = 1024;
constexpr int WS_TR = 4096;
constexpr int WS_HF = 8192;
constexpr int WS_AF = WS_HF + 64 * 256;
constexpr int WS_SG = 57344;
constexpr int WS_WF1 = 65536;
constexpr int WS_CS = 65536 + 8192;
constexpr int WS_ST1 = WS_CS + 32768;
constexpr int WS_XX = WS_ST1 + 16384;
constexpr int WS_W1T = WS_XX + 224 * 16384;
constexpr int WS_W2T = WS_W1T + 112 * 28 * 1024;
constexpr int WS_HT = WS_W2T + 14 * 112 * 1024;
constexpr int WS_ACT = WS_HT + D * L * 2;
constexpr int WS_DBG = WS_ACT + 64 * 112 * 1024;
constexpr int NSTAMP = 32;
constexpr int WS_END = WS_DBG + 256 * NSTAMP * 8;
constexpr int SPIN_CAP = 1 << 16;
static_assert(WS_AF + 16 * 2 * 8 * 128 <= WS_SG, "flags overlap");

#define STAMP(k) do {} while (0)
#define STAMP_FLUSH() do {} while (0)
#define STAMP_DECL() do {} while (0)

__device__ __forceinline__ float bflo(u32 v) { return __builtin_bit_cast(float, v << 16); }
__device__ __forceinline__ float bfhi(u32 v) { return __builtin_bit_cast(float, v & 0xffff0000u); }
__device__ __forceinline__ u32 pk_bf16(float a, float b) { f32x2 f = {a, b}; return __builtin_bit_cast(u32, __builtin_convertvector(f, bf16x2)); }
__device__ __forceinline__ u32 pk_bf16s(float a, float b) { asm("" : "+v"(a)); asm("" : "+v"(b)); return pk_bf16(a, b); }
__device__ __forceinline__ u32 pk_f16(float a, float b) { f32x2 f = {a, b}; return __builtin_bit_cast(u32, __builtin_convertvector(f, f16x2)); }
__device__ __forceinline__ float rbf(float a) { return bflo(pk_bf16(a, 0.f)); }
__device__ __forceinline__ float dot2bf(u32 a, u32 b, float c) {
    return __builtin_amdgcn_fdot2_f32_bf16(__builtin_bit_cast(bf16x2, a), __builtin_bit_cast(bf16x2, b), c, false);
}
__device__ __forceinline__ f32x16 mfma_bf16(v4u a, v4u b, f32x16 c) {
    return __builtin_amdgcn_mfma_f32_32x32x16_bf16(__builtin_bit_cast(bf16x8, a), __builtin_bit_cast(bf16x8, b), c, 0, 0, 0);
}
__device__ __forceinline__ f32x16 mfma_f16(v4u a, v4u b, f32x16 c) {
    return __builtin_amdgcn_mfma_f32_32x32x16_f16(__builtin_bit_cast(f16x8, a), __builtin_bit_cast(f16x8, b), c, 0, 0, 0);
}
__device__ __forceinline__ v2u lds_tr(LAS char* p) {
    return __builtin_bit_cast(v2u, __builtin_amdgcn_ds_read_tr16_b64_v4i16((LAS s16x4*)p));
}
__device__ __forceinline__ v4u lds_tr2(LAS char* p0, LAS char* p1) {
    const v2u lo = lds_tr(p0), hi = lds_tr(p1);
    return v4u{lo[0], lo[1], hi[0], hi[1]};
}
template <int CTRL> __device__ __forceinline__ float dpp(float v) {
    return __builtin_bit_cast(float, __builtin_amdgcn_update_dpp(0, __builtin_bit_cast(int, v), CTRL, 0xf, 0xf, true));
}
__device__ __forceinline__ float rdl(float v, int l) { return __builtin_bit_cast(float, __builtin_amdgcn_readlane(__builtin_bit_cast(int, v), l)); }
__device__ __forceinline__ float wave_sum(float v) {
    v += dpp<0xB1>(v); v += dpp<0x4E>(v); v += dpp<0x141>(v); v += dpp<0x140>(v);
    return (rdl(v, 0) + rdl(v, 16)) + (rdl(v, 32) + rdl(v, 48));
}
__device__ __forceinline__ float wave_max(float v) {
    v = fmaxf(v, dpp<0xB1>(v)); v = fmaxf(v, dpp<0x4E>(v)); v = fmaxf(v, dpp<0x141>(v)); v = fmaxf(v, dpp<0x140>(v));
    return fmaxf(fmaxf(rdl(v, 0), rdl(v, 16)), fmaxf(rdl(v, 32), rdl(v, 48)));
}
__device__ __forceinline__ float half_sum(float v) {
    const auto r = __builtin_amdgcn_permlane32_swap(__builtin_bit_cast(u32, v), __builtin_bit_cast(u32, v), false, false);
    return __builtin_bit_cast(float, (u32)r[0]) + __builtin_bit_cast(float, (u32)r[1]);
}
template <int I> struct IC { static constexpr int v = I; };
template <int B, int E, class Fn> __device__ __forceinline__ void static_for(Fn&& f) {
    if constexpr (B < E) { f(IC<B>{}); static_for<B + 1, E>(f); }
}

typedef f32x2 cf;
__device__ __forceinline__ cf rot(cf b) { return cf{-b.y, b.x}; }
__device__ __forceinline__ cf cmul(cf a, cf b) { return __builtin_elementwise_fma(a.yy, rot(b), a.xx * b); }
__device__ __forceinline__ cf cmulc(cf a, cf b) { return __builtin_elementwise_fma(a.yy, b.yx, a.xx * rot(b).yx); }
__device__ __forceinline__ u32 pkh(cf a) { return __builtin_bit_cast(u32, __builtin_convertvector(a, f16x2)); }

typedef __amdgpu_buffer_rsrc_t rsrc_t;
__device__ __forceinline__ v4u ld16(rsrc_t r, int voff, int soff) { return __builtin_amdgcn_raw_buffer_load_b128(r, voff, soff, SC1); }
__device__ __forceinline__ u32 ld4(rsrc_t r, int voff, int soff) { return __builtin_amdgcn_raw_buffer_load_b32(r, voff, soff, SC1); }
__device__ __forceinline__ void st16(rsrc_t r, int voff, v4u v) { __builtin_amdgcn_raw_buffer_store_b128(v, r, voff, 0, SC1); }
__device__ __forceinline__ void st8(rsrc_t r, int voff, int soff, v2u v) { __builtin_amdgcn_raw_buffer_store_b64(v, r, voff, soff, SC1); }
__device__ __forceinline__ void st4(rsrc_t r, int voff, int soff, u32 v) { __builtin_amdgcn_raw_buffer_store_b32(v, r, voff, soff, SC1); }
template <int N> __device__ __forceinline__ void wait_vm() { asm volatile("s_waitcnt vmcnt(%0)" :: "i"(N) : "memory"); }
constexpr int OOB = 0x7f000000;

__device__ __forceinline__ void wait_flags(rsrc_t rs, int voff, int soff, u32 v, u32 tag) {
    if (!__all(v == tag)) {
        int spins = SPIN_CAP;
#pragma unroll 1
        do {
            __builtin_amdgcn_s_sleep(1);
            v = ld4(rs, voff, soff);
        } while (!__all(v == tag) && --spins > 0);
    }
    __builtin_amdgcn_fence(__ATOMIC_ACQUIRE, "wavefront");
}
__device__ __forceinline__ void glds16(v4u srd, int voff, int soff, u32 lds_dst) {
    asm volatile("s_mov_b32 m0, %3\n\ts_nop 0\n\tbuffer_load_dwordx4 %0, %1, %2 offen sc1 lds"
                 :: "v"(voff), "s"(srd), "s"(soff), "s"(lds_dst) : "memory");
}
__device__ __forceinline__ LAS char* opaque(LAS char* q) { u32 a = (u32)(size_t)q; asm volatile("" : "+v"(a)); return (LAS char*)(size_t)a; }

struct Params {
    const u16 *x, *Are, *Aim, *Bre, *Bim, *Cre, *Cim, *logdt, *Dskip, *wg, *wu, *wd;
    char* ws;
    u16* out;
};

constexpr int XY_STRIDE = 272, XY_ROWS = 59, XY_WAVE = XY_STRIDE * XY_ROWS;
constexpr int P_WAVE = 33 * 144;
constexpr int LD_PB = 0, LD_PA = 38144;
constexpr int LD_HA = 38144, LD_HB = LD_HA + 57344;
constexpr int LD_XU = LD_HB;
constexpr int LD_KP = 128512;
constexpr int LD_U = LD_KP + 8 * 1536;
constexpr int LD_SB = LD_HB + 57344;
constexpr int LD_ST = LD_SB + 64;
constexpr int LD_CS = LD_ST + 4096;
constexpr int LDS_BYTES = LD_CS + 1024;
static_assert(LD_U + 2 * 4160 <= LD_SB && LDS_BYTES <= 163840, "lds map");

__device__ __forceinline__ v4u tr_piece(const v4u (&in)[8], int c) {
    v4u o;
#pragma unroll
    for (int kp = 0; kp < 4; kp++)
        o[kp] = __builtin_amdgcn_perm(in[2 * kp + 1][c >> 1], in[2 * kp][c >> 1], (c & 1) ? 0x07060302u : 0x05040100u);
    return o;
}
__device__ __forceinline__ void w1_task_load(const Params& p, int task, int lane, v2u (&in)[8]) {
    const int t = (task < 0 ? 0 : task) >> 1, c4 = task & 1;
    const int phi = t / 28, r = t - phi * 28, mat = r >= 14, kp = r - 14 * mat;
    const rsrc_t rw = __builtin_amdgcn_make_buffer_rsrc((void*)(mat ? p.wu : p.wg), 0, task < 0 ? 0 : D * F * 2, 0x00020000);
    const int voff = (8 * (lane >> 4) * F + (lane & 15) * 8) * 2;
#pragma unroll
    for (int k = 0; k < 8; k++) in[k] = __builtin_amdgcn_raw_buffer_load_b64(rw, voff + (k & 1) * F * 2, ((32 * kp + (k & 6)) * F + phi * 128 + 4 * c4) * 2, 0);
}
__device__ __forceinline__ void w1_task_store(rsrc_t rs, int task, int lane, const v2u (&in)[8]) {
    const int t = task >> 1, c4 = task & 1;
    const int phi = t / 28, r = t - phi * 28, mat = r >= 14, kp = r - 14 * mat;
    const int voff = WS_W1T + ((phi * 8 + 4 * c4) * 28 + 2 * kp) * 1024 + mat * 256 + (lane >> 4) * 512 + (lane & 15) * 16;
#pragma unroll
    for (int c = 0; c < 4; c++) {
        v4u o;
#pragma unroll
        for (int kp2 = 0; kp2 < 4; kp2++) o[kp2] = __builtin_amdgcn_perm(in[2 * kp2 + 1][c >> 1], in[2 * kp2][c >> 1], (c & 1) ? 0x07060302u : 0x05040100u);
        st16(rs, voff + c * 28 * 1024, o);
    }
}
__device__ __forceinline__ int w1_flag(int task) { const int t = task >> 1, phi = t / 28; return WS_WF1 + (phi * 2 + (task & 1)) * 128 + (t - phi * 28) * 4; }
__device__ __forceinline__ void w2_task(const Params& p, rsrc_t rs, int task, int lane, LAS char* scr) {
    const int del = task / 28, kg = task - del * 28;
    const int k4 = (lane >> 3) & 3, ks = kg * 4 + k4, h = lane >> 5, dr = lane & 7;
    const u16* src = p.wd + (size_t)((ks >> 3) * 128 + 32 * h + (ks & 7)) * D + del * 64 + dr * 8;
    v4u in[8];
#pragma unroll
    for (int j = 0; j < 8; j++) in[j] = __builtin_nontemporal_load((const v4u*)(src + (size_t)(8 * ((j & 3) + 8 * (j >> 2))) * D));
    LAS char* w = scr + ((dr >> 2) * 4 + k4) * 1024 + (32 * h + 8 * (dr & 3)) * 16;
#pragma unroll
    for (int c = 0; c < 8; c++) *(LAS v4u*)(w + c * 16) = tr_piece(in, c);
    const int voff = WS_W2T + (del * 2 * 112 + kg * 4) * 1024 + lane * 16;
#pragma unroll
    for (int f = 0; f < 8; f++) st16(rs, voff + ((f >> 2) * 112 + (f & 3)) * 1024, *(LAS v4u*)(scr + f * 1024 + lane * 16));
}

#define W1_CHK 2
#define KPRE 32
#define W1_NUM 2
#define W1_DEN 5
#define W1_BURST 4
#define PF1 0
#define PF2 1
#define GR_EARLY 3
#define CS_T 520
#define GR_T 320
#define AFB_EARLY 1
#define W1_ORDER 1
#define PRIO_C 1
constexpr int w1_cnt(int n) { return n <= W1_CHK ? 0 : W1_BURST + ((n - W1_CHK - 1) * W1_NUM) / W1_DEN > 28 ? 28 : W1_BURST + ((n - W1_CHK - 1) * W1_NUM) / W1_DEN; }
#define PRIO_A 0
#define CS_FIRST 1
#define HBP_N 14
#define W2_N0 34
#define PRIO_G 1
#define BAR_AF 1
#define OPT 0
#define AFBP_N 13
#define TAIL 0
#define ROT_H 0
#define HF_OPT 0
#define HFL_EARLY 0
#define WFL_N -1
#define AFB_N 4
#define SG_N 77
#define A0_LATE 0
#define MFMA_FIRST 1
#define HB_N 26
#define ROT_W1 0
#define ROT_W2 0
#define AF_N 83
#define HF_N 37
#define AFP_N 94
#define A0_IN 7
#define AFC_N 103
constexpr int ST1_N = 67;
constexpr int g1_op(int n) {
    return n == 27 ? 52 : (n > HB_N && n <= HB_N + 8) ? 32 + (n - HB_N - 1) - (n > 27)
         : n == 55 ? 40 : n == ST1_N ? 41 : n == 111 ? 42 : n == AF_N ? 50 : n == AFP_N ? 51 : n == SG_N ? 53
         : (n >= 1 && n < 21 && (n & 1) == 1) ? (n - 1) / 2
         : (n >= 36 && n < 52 && (n & 3) == 0) ? 10 + (n - 36) / 4
         : (n == 54 || (n >= 68 && n < 80 && (n & 1) == 0)) ? (n == 54 ? 14 : 15 + (n - 68) / 2)
         : (n >= 85 && n < 99 && (n & 1) == 1) ? 21 + (n - 85) / 2
         : (n > AFC_N && n <= AFC_N + A0_IN) ? 60 + (n - AFC_N - 1)
         : -1;
}
constexpr bool g1_all() { int m = 0; for (int i = 0; i < 112; i++) if (g1_op(i) >= 0 && g1_op(i) < 28) m |= 1 << g1_op(i); return m == (1 << 28) - 1; }
static_assert(g1_all(), "every W2 fragment is requested");
constexpr int g1_cum(int n) { int c = 0; for (int i = 0; i < n; i++) c += g1_op(i) >= 0; return c; }
static_assert(HB_N >= 23 && HB_N < 27 && AF_N > ST1_N && AFP_N > AF_N && AFC_N > AFP_N && AFC_N >= 99 && AFC_N + A0_IN < 111, "schedule");
#define W1_MID 6
#define CODE_PF 23552

__global__ __launch_bounds__(512, 2) void fused_block(Params p) {
    extern __shared__ __attribute__((aligned(16))) char smem_[];
    LAS char* sm = (LAS char*)smem_;
    const int tid = threadIdx.x, lane = tid & 63;
    const int wave = __builtin_amdgcn_readfirstlane(tid >> 6);
    const int b = blockIdx.x;
    STAMP_DECL();
    const bool main_blk = b < 224;
    const int xcd = b & 7, i28 = b >> 3;
    u64 pc0; u32 clen;
    asm volatile("s_getpc_b64 %0\n.Lfb_code_begin:\n\ts_mov_b32 %1, .Lfb_code_end-.Lfb_code_begin" : "=s"(pc0), "=s"(clen));
    const u32 cpf = __builtin_amdgcn_raw_buffer_load_b32(__builtin_amdgcn_make_buffer_rsrc((void*)pc0, 0, main_blk ? 0 : clen, 0x00020000),
                                                         lane < 16 ? lane * 64 : OOB, (wave * 4 + (i28 & 3)) * 1024, 0);
    const int l16 = lane * 16;
    const int rbase = 16 * (b >> 1) + 2 * (b & 1);
    int x2v = lane < 56 ? (lane & 3) * (4 * D * 2) + (lane >> 2) * 8 : OOB, xrv = lane < 56 ? l16 : OOB, t2 = tid * 2;
    int x2s = (rbase * D + 56 * wave) * 2, xrs = (rbase + 4 * (wave >> 1) + (wave & 1)) * D * 2;
    int pair = xcd * 28 + i28;
    asm volatile("" : "+v"(x2v), "+v"(xrv), "+v"(t2), "+s"(x2s), "+s"(xrs), "+s"(pair));
    SB();
    const rsrc_t rs = __builtin_amdgcn_make_buffer_rsrc(p.ws, 0, WS_END, 0x00020000);
    asm volatile("" :: "s"(p.x), "s"(p.wd), "s"(p.ws));
    if (PRIO_A && wave >= 4) __builtin_amdgcn_s_setprio(1);
    STAMP(0);
    const u32 tstart = GR_EARLY == 3 ? (u32)__builtin_amdgcn_s_memrealtime() : 0;
    const u32 tagv = ld4(rs, WS_EPOCH, 0);

    const rsrc_t rx = __builtin_amdgcn_make_buffer_rsrc((void*)p.x, 0, L * D * 2, 0x00020000);
    const v2u xa = __builtin_amdgcn_raw_buffer_load_b64(rx, x2v, x2s, 2);
    const v2u xb = __builtin_amdgcn_raw_buffer_load_b64(rx, x2v + D * 2, x2s, 2);
    const v4u xr = __builtin_amdgcn_raw_buffer_load_b128(rx, xrv, xrs, 2);
    SB();
    const int ch = wave >> 2, wq = wave & 3;
    const int d0 = 2 * pair;
    const int psz = main_blk ? D * N * 2 : 0;
    auto ldp = [&](const u16* q) __attribute__((always_inline)) -> u32 {
        return __builtin_amdgcn_raw_buffer_load_b16(__builtin_amdgcn_make_buffer_rsrc((void*)q, 0, psz, 0x00020000), t2, d0 * N * 2, 2);
    };
    const u32 r_ldt = __builtin_amdgcn_raw_buffer_load_b32(__builtin_amdgcn_make_buffer_rsrc((void*)p.logdt, 0, main_blk ? D * 2 : 0, 0x00020000), 0, d0 * 2, 0);
    const u32 r_are = ldp(p.Are), r_aim = ldp(p.Aim), r_bre = ldp(p.Bre), r_bim = ldp(p.Bim), r_cre = ldp(p.Cre), r_cim = ldp(p.Cim);
    const u32 r_dsk = __builtin_amdgcn_raw_buffer_load_b32(__builtin_amdgcn_make_buffer_rsrc((void*)p.Dskip, 0, main_blk ? D * 2 : 0, 0x00020000), 0, d0 * 2, 0);
    const int hw = (b - 224) + 32 * wave;
    const int w1task = !main_blk ? -1 : wave < 2 ? pair * 2 + wave : (wave < 4 && pair < 168) ? 448 + pair * 2 + wave - 2 : -1;
    SB();
    const int h = lane >> 5, col = lane & 31;
    const int G1 = (lane >> 4) & 1, q4 = (lane & 15) >> 2, pp = lane & 3;
    int xxv = lane < 56 ? WS_XX + ((28 * wave + 2 * (lane >> 2)) * 2 + (b & 1)) * 8192 + (4 * (b >> 1) + (lane & 3)) * 16 : OOB;
    int flv = lane == 0 ? 0 : OOB;
    asm volatile("" : "+v"(xxv), "+v"(flv));
    LAS char* xy = sm + wave * XY_WAVE;
    LAS char* xyw = opaque(xy + lane * 4);
    LAS char* pa = opaque(xy + (32 + (int)((0x1713100d0a080400ull >> (8 * (col >> 2))) & 255) + (col & 3)) * XY_STRIDE + h * 16);
    LAS char* pb = opaque(xy + col * XY_STRIDE + h * 16);
    LAS char* exw = opaque(xy + l16);
    LAS char* exr = opaque(sm + (ch * 4) * XY_WAVE + wq * 1024 + l16);
    LAS char* kpw = opaque(sm + LD_KP + ch * 4 * 1536 + 512 + (wq < 2 ? 64 * wq + 2 * col + h : wq == 2 ? 64 * (2 + h) + 2 * col : 64 * (4 + 2 * h) + 2 * col) * 2);
    LAS char* uw = opaque(sm + LD_U + 64 + tid * 8);
    LAS char* ua = opaque(sm + LD_U + ch * 4160 + 2 * (32 + 32 + 16 * G1 - 32 * h - 16 - 4 * q4 + 4 * pp));
    LAS char* ka = opaque(sm + LD_KP + wave * 1536 + 2 * (256 - 248 + 8 * col + 8 * h));
    LAS char* pw = opaque(sm + wave * P_WAVE + (col + 1) * 144 + h * 16);
    *(LAS v2u*)(sm + LD_KP + wave * 1536 + lane * 8) = v2u{0, 0};
    if (tid < 32) *(LAS u32*)(sm + LD_U + (tid >> 4) * 4160 + (tid & 15) * 4) = 0;
    SB();
    const u32 tag = __builtin_amdgcn_readfirstlane(tagv) + 1u;
    st16(rs, xxv, v4u{xa[0], tag, xb[0], tag});
    st16(rs, xxv + 16384, v4u{xa[1], tag, xb[1], tag});
    SB();
    {
        float a = 0.f, q = 0.f;
#pragma unroll
        for (int i = 0; i < 4; i++) { a = dot2bf(xr[i], 0x3f803f80u, a); q = dot2bf(xr[i], xr[i], q); }
        const float mean = wave_sum(a) * (1.f / D), ex2 = wave_sum(q) * (1.f / D);
        const float var = rbf(fmaxf(ex2 - mean * mean, 0.f));
        const float sd = __builtin_amdgcn_sqrtf(rbf(var + 1e-5f));
        st8(rs, flv, WS_ST1 + (((b & 1) * 512 + 4 * (b >> 1) + (wave >> 1)) * 2 + (wave & 1)) * 8, v2u{pk_bf16(mean, sd), tag});
    }
    SB();
    v2u tin[8];
    w1_task_load(p, w1task, lane, tin);
    SB();
    STAMP(1);
    const int tau = main_blk ? (xcd >> 1) * 4 + (i28 & 3) : (b & 15), phi = (xcd & 1) * 7 + (i28 >> 2);
    const int del = i28 >> 2, tau2 = 2 * tau + (xcd & 1);
    const int trl = q4 * 128 + 8 * (pp & 1) + 16 * (2 * G1 + (pp >> 1)), sw = (q4 & 2) << 5;
    const int hf_v = (lane < 56 ? lane : 0) * 4, hf_s = WS_HF + ((tau >> 2) * 8 + wave) * 256;
    const v4u srd = {(u32)(size_t)p.ws, (u32)((size_t)p.ws >> 32) & 0xffffu, (u32)WS_END, 0x00020000u};
    const int hvoff = WS_HT + ((8 * wave + (lane >> 3)) * L + tau * 64) * 2 + (((lane & 7) ^ (((lane >> 3) & 2) << 1)) << 4);
    if (!main_blk) {
        BARRIER_RAW();
        const int w2task = hw;
        if (w2task < 196) {
            w2_task(p, rs, w2task, lane, sm + LD_HB + wave * 8192);
            wait_vm<0>();
            st4(rs, flv, WS_WF2 + (w2task / 28) * 128 + (w2task % 28) * 4, tag);
        }
        asm volatile("" :: "v"(cpf), "v"(tin[7]));
        STAMP(2);
        const int unit = 255 - hw;
        if (unit < 112) {
            while ((u32)__builtin_amdgcn_s_memrealtime() - tstart < CS_T) __builtin_amdgcn_s_sleep(8);
            const int fv = (col < 28 ? col : 0) * 4, fs = WS_WF1 + ((unit >> 3) * 2 + ((unit >> 2) & 1)) * 128;
            wait_flags(rs, fv, fs, ld4(rs, fv, fs), tag);
            float c0 = 0.f, c1 = 0.f;
#pragma unroll 1
            for (int k0 = 0; k0 < 28; k0 += 14) {
                v4u w[14];
#pragma unroll
                for (int ks = 0; ks < 14; ks++) w[ks] = ld16(rs, l16 + (ks & 3) * 1024, WS_W1T + (unit * 28 + k0 + (ks & ~3)) * 1024);
#pragma unroll
                for (int ks = 0; ks < 14; ks++) {
                    c0 = dot2bf(w[ks][0], 0x3f803f80u, c0); c1 = dot2bf(w[ks][1], 0x3f803f80u, c1);
                    c0 = dot2bf(w[ks][2], 0x3f803f80u, c0); c1 = dot2bf(w[ks][3], 0x3f803f80u, c1);
                }
            }
            st8(rs, lane < 32 ? WS_CS + lane * 8 : OOB, unit * 256, v2u{__builtin_bit_cast(u32, half_sum(c0 + c1)), tag});
        }
        STAMP(3);
        {
            const int tl = (b >> 4) & 1;
            if (wave == 0) {
                int spins = SPIN_CAP;
#pragma unroll 1
                while (ld4(rs, 0, WS_TR + (b - 224) * 128) != tag && --spins > 0) __builtin_amdgcn_s_sleep(4);
            }
            BARRIER();
            if (wave < 7) {
                const int gv = WS_HT + ((lane >> 2) * L + 1024 + tau * 64 + tl * 32) * 2 + (lane & 3) * 16;
                LAS char* as = sm + 4096 + wave * 4096 + (8 * h + q4) * 64 + G1 * 32 + pp * 8;
#pragma unroll
                for (int n = 0; n < 4; n++) glds16(srd, gv, (wave * 4 + n) * 65536, (u32)(size_t)sm + 4096 + wave * 4096 + n * 1024);
                wait_vm<0>();
                v4u f[4];
#pragma unroll
                for (int k = 0; k < 4; k++) f[k] = lds_tr2(as + k * 1024, as + k * 1024 + 256);
                float s1 = 0.f, s2 = 0.f, s1b = 0.f, s2b = 0.f;
#pragma unroll
                for (int k = 0; k < 4; k++) {
                    s1 = dot2bf(f[k][0], 0x3f803f80u, s1); s2 = dot2bf(f[k][0], f[k][0], s2);
                    s1b = dot2bf(f[k][1], 0x3f803f80u, s1b); s2b = dot2bf(f[k][1], f[k][1], s2b);
                    s1 = dot2bf(f[k][2], 0x3f803f80u, s1); s2 = dot2bf(f[k][2], f[k][2], s2);
                    s1b = dot2bf(f[k][3], 0x3f803f80u, s1b); s2b = dot2bf(f[k][3], f[k][3], s2b);
                }
                *(LAS f32x2*)(sm + wave * 512 + lane * 8) = f32x2{s1 + s1b, s2 + s2b};
            }
            BARRIER();
            if (wave == 0) {
                f32x2 t = *(LAS f32x2*)(sm + lane * 8);
#pragma unroll
                for (int k = 1; k < 7; k++) t += *(LAS f32x2*)(sm + k * 512 + lane * 8);
                const float mean = half_sum(t.x) * (1.f / D), ex2 = half_sum(t.y) * (1.f / D);
                const float var = rbf(fmaxf(ex2 - mean * mean, 0.f));
                st8(rs, lane < 32 ? WS_SG + col * 16 + tl * 8 : OOB, tau * 512, v2u{pk_bf16(mean, __builtin_amdgcn_sqrtf(rbf(var + 1e-5f))), tag});
            }
        }
        STAMP(4);
        STAMP_FLUSH();
        return;
    }
    asm volatile("" :: "v"(r_are), "v"(r_aim), "v"(r_bre), "v"(r_bim), "v"(r_cre), "v"(r_cim), "v"(r_dsk), "v"(cpf));
    SB();

    float unscale;
    {
        const float dt = rbf(__expf(ch ? bfhi(r_ldt) : bflo(r_ldt)));
        const float ar = dt * bflo(r_are), ai = dt * bflo(r_aim);
        const float br = dt * bflo(r_bre), bi = dt * bflo(r_bim);
        const float cr = bflo(r_cre), ci = bflo(r_cim);
        cf cb = {cr * br - ci * bi, -(cr * bi + ci * br)};
        const float a2 = ar * 1.44269504088896340736f;
        const float rh = ai * 0.15915494309189535f;
        const float rl = __builtin_fmaf(ai, 0.15915494309189535f, -rh) + ai * 6.4206382e-9f;
        auto cexp = [&](float s) __attribute__((always_inline)) -> cf {
            const float e = __builtin_amdgcn_exp2f(a2 * s);
            const float r = __builtin_amdgcn_fractf(rh * s) + rl * s;
            return cf{__builtin_amdgcn_cosf(r), __builtin_amdgcn_sinf(r)} * e;
        };
        const float grow = fmaxf(1.f, __builtin_amdgcn_exp2f(a2 * 2048.f));
        const float sb = wave_sum((fabsf(cb.x) + fabsf(cb.y)) * grow);
        *(LAS float*)(sm + LD_SB + wave * 4) = sb;
        int ex = (int)((__builtin_bit_cast(u32, sb) >> 23) & 255);
        ex = min(max(ex, 30), 230);
        unscale = __builtin_bit_cast(float, (u32)(127 - 15 - 126 + ex) << 23);
        cb *= __builtin_bit_cast(float, (u32)(127 + 15 + 126 - ex) << 23);

        const cf e1 = cexp(1.f), e16 = cexp(16.f), e256 = cexp(256.f);
        const cf e2 = cmul(e1, e1), e32 = cmul(e16, e16), e512 = cmul(e256, e256), x1 = cmulc(cb, e1), x256 = cmulc(cb, e256);
        SB();
        const cf e4 = cmul(e2, e2), e64 = cmul(e32, e32), e1024 = cmul(e512, e512), x2 = cmulc(cb, e2), x3 = cmulc(x1, e2);
        const cf x512 = cmulc(cb, e512), x768 = cmulc(x256, e512), y6 = cmul(e16, e32);
        SB();
        const cf e8 = cmul(e4, e4), e128 = cmul(e64, e64);
        const cf x4 = cmulc(cb, e4), x5 = cmulc(x1, e4), x6 = cmulc(x2, e4), x7 = cmulc(x3, e4), x258 = cmulc(x2, e256);
        const cf x1024 = cmulc(cb, e1024), x1280 = cmulc(x256, e1024), x1536 = cmulc(x512, e1024), x1792 = cmulc(x768, e1024);
        SB();
        auto put = [&](int q, cf v) __attribute__((always_inline)) { *(LAS u32*)(xyw + q * XY_STRIDE) = pkh(v); };
        cf y[8];
        y[0] = cf{1.f, 0.f}; y[1] = e8; y[2] = e16; y[3] = cmul(e8, e16); y[4] = e32; y[5] = cmul(e8, e32); y[6] = y6;
        const cf e192 = cmul(e128, e64), x8 = cmulc(x4, e4), x260 = cmulc(x4, e256), x262 = cmulc(x6, e256), x516 = cmulc(x4, e512);
        put(32, cb); put(33, x1); put(34, x2); put(35, x3); put(36, x4); put(37, x5); put(38, x6); put(39, x7);
        put(40, x256); put(41, x258);
        SB();
        y[7] = cmul(y[3], e32);
        const cf x264 = cmulc(x8, e256), x520 = cmulc(x8, e512), x772 = cmulc(x260, e512), x1032 = cmulc(x8, e1024);
        cf t[8];
#pragma unroll
        for (int q = 1; q < 7; q++) t[q] = cmul(y[q], e64);
        put(42, x260); put(43, x262); put(45, x512); put(46, x516);
#pragma unroll
        for (int q = 0; q < 6; q++) put(q, y[q]);
        SB();
        t[7] = cmul(y[7], e64);
        const cf x776 = cmulc(x264, e512), x1288 = cmulc(x264, e1024), x1544 = cmulc(x520, e1024);
        cf t2[8];
#pragma unroll
        for (int q = 1; q < 8; q++) t2[q] = cmul(y[q], e128);
        put(6, y[6]); put(7, y[7]); put(8, e64);
#pragma unroll
        for (int q = 1; q < 6; q++) put(8 + q, t[q]);
        put(44, x264); put(47, x520); put(48, x768); put(49, x772); put(51, x1024); put(52, x1032);
        SB();

        const cf x1800 = cmulc(x776, e1024);
        cf t3[8];
#pragma unroll
        for (int q = 1; q < 8; q++) t3[q] = cmul(y[q], e192);
        put(14, t[6]); put(15, t[7]); put(16, e128);
#pragma unroll
        for (int q = 1; q < 8; q++) put(16 + q, t2[q]);
        put(50, x776); put(53, x1280); put(54, x1288); put(55, x1536); put(56, x1544); put(57, x1792);
        SB();
        put(24, e192);
#pragma unroll
        for (int q = 1; q < 8; q++) put(24 + q, t3[q]);
        put(58, x1800);
    }
    SB();
    STAMP(2);
    asm volatile("" :: "v"(tin[7]));
    if (w1task >= 0) w1_task_store(rs, w1task, lane, tin);
    SB();
    const int t16 = tid * 16;
    v4u gx[2], gs[2];
    auto gr_load = [&]() __attribute__((always_inline)) {
        gx[0] = ld16(rs, t16, WS_XX + pair * 16384); gx[1] = ld16(rs, t16, WS_XX + pair * 16384 + 8192);
        gs[0] = ld16(rs, t16, WS_ST1); gs[1] = ld16(rs, t16, WS_ST1 + 8192);
    };
    const bool gr_first = (u32)__builtin_amdgcn_s_memrealtime() - tstart >= GR_T;
    if (gr_first) gr_load();
    SB();
    {
        f32x16 kd;
#pragma unroll
        for (int i2 = 0; i2 < 16; i2++) kd[i2] = 0.f;
        v4u fa[8], fb[8];
#pragma unroll
        for (int s = 0; s < 8; s++) { fa[s] = *(LAS v4u*)(pa + s * 32); fb[s] = *(LAS v4u*)(pb + s * 32); }
        SB();
#pragma unroll
        for (int s = 0; s < 8; s++) kd = mfma_f16(fa[s], fb[s], kd);
        SB();

#pragma unroll
        for (int g2 = 0; g2 < 4; g2++)
            *(LAS f32x4*)(exw + g2 * 1024) = f32x4{kd[4 * g2], kd[4 * g2 + 1], kd[4 * g2 + 2], kd[4 * g2 + 3]} * unscale;
    }
    if (w1task >= 0) { wait_vm<0>(); st4(rs, flv, w1_flag(w1task), tag); }
    if (!gr_first) {
        while ((u32)__builtin_amdgcn_s_memrealtime() - tstart < GR_T) __builtin_amdgcn_s_sleep(2);
        gr_load();
    }
    SB();
    BARRIER();
    __builtin_amdgcn_s_setprio(0);
    if (GR_EARLY == 0) gr_load();
    const int l28 = (col < 28 ? col : 0) * 4;
    SB();
    STAMP(3);
    float ik;
    {
        const f32x4 s0 = *(LAS f32x4*)(sm + LD_SB + ch * 16);
        const f32x4 a0 = *(LAS f32x4*)(exr), a1 = *(LAS f32x4*)(exr + XY_WAVE), a2 = *(LAS f32x4*)(exr + 2 * XY_WAVE), a3 = *(LAS f32x4*)(exr + 3 * XY_WAVE);
        const float b0 = (s0[0] + s0[1]) + (s0[2] + s0[3]);
        int e0 = (int)((__builtin_bit_cast(u32, b0) >> 23) & 255);
        e0 = min(max(e0, 30), 230);
        ik = __builtin_bit_cast(float, (u32)(127 - 15 - 126 + e0) << 23);
        const float sk = __builtin_bit_cast(float, (u32)(127 + 15 + 126 - e0) << 23);
        const f32x4 v = ((a0 + a1) + (a2 + a3)) * sk;
        const u32 p01 = pk_f16(v[0], v[1]), p23 = pk_f16(v[2], v[3]);
        LAS char* o = kpw;
        if (wq == 0) {
            *(LAS u16*)(o) = (u16)p01; *(LAS u16*)(o + 1536) = (u16)(p01 >> 16);
            *(LAS u16*)(o + 2 * 1536) = (u16)p23; *(LAS u16*)(o + 3 * 1536) = (u16)(p23 >> 16);
        } else if (wq == 1) {
            *(LAS u16*)(o) = (u16)p01; *(LAS u16*)(o + 1536) = (u16)p01;
            *(LAS u16*)(o + 2 * 1536) = (u16)(p01 >> 16); *(LAS u16*)(o + 3 * 1536) = (u16)p23;
        } else if (wq == 2) {
            *(LAS u32*)(o) = p01; *(LAS u32*)(o + 1536) = p01;
            *(LAS u32*)(o + 2 * 1536) = (p01 & 0xffffu) | (p23 << 16);
            *(LAS u32*)(o + 3 * 1536) = (p01 >> 16) | (p23 << 16);
        } else {
            const bool ev = (col & 1) == 0;
            *(LAS u32*)(o) = ev ? ((p01 & 0xffffu) | (p01 << 16)) : p01;
            *(LAS u32*)(o + 1536) = p01; *(LAS u32*)(o + 2 * 1536) = p01; *(LAS u32*)(o + 3 * 1536) = p01;
            *(LAS u32*)(o + 128) = ev ? ((p23 & 0xffffu) | (p23 << 16)) : p23;
            *(LAS u32*)(o + 128 + 1536) = p23; *(LAS u32*)(o + 128 + 2 * 1536) = p23; *(LAS u32*)(o + 128 + 3 * 1536) = p23;
        }
    }
    SB();
    auto gr_ok = [&]() __attribute__((always_inline)) {
        const u32 a = (gx[0][1] ^ tag) | (gx[0][3] ^ tag) | (gx[1][1] ^ tag) | (gx[1][3] ^ tag);
        const u32 c = (gs[0][1] ^ tag) | (gs[0][3] ^ tag) | (gs[1][1] ^ tag) | (gs[1][3] ^ tag);
        return __all((a | c) == 0);
    };
    if (!gr_ok()) {
        int spins = SPIN_CAP;
#pragma unroll 1
        do {
            __builtin_amdgcn_s_sleep(4);
            gr_load();
        } while (!gr_ok() && --spins > 0);
    }
    STAMP(4);
    v4u wf[28];
    const int w1off = WS_W1T + (phi * 8 + wave) * 28 * 1024;
    int seg7[4];
#pragma unroll
    for (int i = 0; i < 4; i++) seg7[i] = ((i + i28 * ROT_W1) & 3) * 7;
    auto w1_load = [&](int ks) __attribute__((always_inline)) {
        wf[ks] = ld16(rs, l16 + ((ks % 7) & 3) * 1024, w1off + seg7[ks / 7] * 1024 + ((ks % 7) >> 2) * 4096);
    };
    const int wf_s = WS_WF1 + phi * 256;
    u32 wfl;
    if (WFL_N < 0) wfl = ld4(rs, l28 + h * 128, wf_s);
    SB();
    u32 xc[4], udk[4];
    {
        float u0[4], u1[4];
#pragma unroll
        for (int i = 0; i < 4; i++) {
            xc[i] = gx[i >> 1][2 * (i & 1)];
            const u32 st = gs[i >> 1][2 * (i & 1)];
            const float m = bflo(st), r = __builtin_amdgcn_rcpf(bfhi(st));
            const u32 dd = pk_bf16(bflo(xc[i]) - m, bfhi(xc[i]) - m);
            const u32 uu = pk_bf16(bflo(dd) * r, bfhi(dd) * r);
            u0[i] = bflo(uu); u1[i] = bfhi(uu);
        }
        *(LAS v2u*)(uw) = v2u{pk_f16(u0[0], u0[1]), pk_f16(u0[2], u0[3])};
        *(LAS v2u*)(uw + 4160) = v2u{pk_f16(u1[0], u1[1]), pk_f16(u1[2], u1[3])};
#pragma unroll
        for (int i = 0; i < 4; i++) udk[i] = pk_bf16(u0[i] * bflo(r_dsk), u1[i] * bfhi(r_dsk));
    }
    BARRIER();
    STAMP(5);
    f32x4 r0, r1, l3;
    f32x2 r2, l2;
    float l1, r3;
    v4u xu;
    u32 ya, yb;
    const int c1 = tid & 255;
    LAS char* q1 = opaque(sm + ch * 4 * P_WAVE + ((c1 >> 3) + 1) * 144 + (c1 & 7) * 16);
    LAS char* q0 = opaque(sm + ch * 4 * P_WAVE + (((c1 - 1) >> 3) + 1) * 144 + ((c1 - 1) & 7) * 16);
    LAS char* xur = opaque(sm + LD_XU + ch * 8192 + c1 * 16);
    auto ce_read = [&](int hf) __attribute__((always_inline)) {
        const int pb_ = hf ? LD_PB : LD_PA;
        r0 = *(LAS f32x4*)(q1 + pb_);
        r1 = *(LAS f32x4*)(q1 + pb_ + P_WAVE); l1 = *(LAS float*)(q0 + pb_ + P_WAVE + 12);
        r2 = *(LAS f32x2*)(q1 + pb_ + 2 * P_WAVE); l2 = *(LAS f32x2*)(q0 + pb_ + 2 * P_WAVE + 8);
        r3 = *(LAS float*)(q1 + pb_ + 3 * P_WAVE); l3 = *(LAS f32x4*)(q0 + pb_ + 3 * P_WAVE);
        xu = *(LAS v4u*)(xur + hf * 4096);
    };
    auto ce_part = [&](int hf, int part) __attribute__((always_inline)) {
        if (part == 0) ya = pk_bf16s(((r0[0] + l2[0]) + (l1 + l3[1])) * ik, ((r0[1] + l2[1]) + (r1[0] + l3[2])) * ik);
        if (part == 1) yb = pk_bf16s(((r0[2] + r2[0]) + (r1[1] + l3[3])) * ik, ((r0[3] + r2[1]) + (r1[2] + r3)) * ik);
        if (part == 2) ya = pk_bf16s(bflo(ya) + bfhi(xu[0]), bfhi(ya) + bfhi(xu[1]));
        if (part == 3) yb = pk_bf16s(bflo(yb) + bfhi(xu[2]), bfhi(yb) + bfhi(xu[3]));
        if (part == 4) ya = pk_bf16s(bflo(ya) + bflo(xu[0]), bfhi(ya) + bflo(xu[1]));
        if (part == 5) {
            yb = pk_bf16s(bflo(yb) + bflo(xu[2]), bfhi(yb) + bflo(xu[3]));
            st8(rs, WS_HT + c1 * 8, (d0 + ch) * (L * 2) + hf * 2048, v2u{ya, yb});
        }
    };
    const int hf_set = WS_HF + (wq * 8 + (((d0 + ch) >> 3) & 7)) * 256 + (((d0 + ch) >> 6) * 8 + ((d0 + ch) & 7)) * 4;
    u32 pf1;
    {
        f32x16 acc0, acc1;
#pragma unroll
        for (int i = 0; i < 16; i++) { acc0[i] = 0.f; acc1[i] = 0.f; }
        constexpr int PD = 3;
        v4u fu[PD + 1], fk[16], fk1[PD + 1];
        auto issue = [&](int n) __attribute__((always_inline)) {
            const int B0 = (n >= 16 && n < 32) ? 2016 - 64 * (n - 16) : 992 - 64 * (n & 15);
            fu[n % (PD + 1)] = lds_tr2(ua + 2 * (B0 - 32) + 32, ua + 2 * (B0 - 32));
            if (n < 16) fk[n] = *(LAS v4u*)(ka + 32 * n);
            if (n >= 32) fk1[n % (PD + 1)] = *(LAS v4u*)(ka + 32 * (n - 16));
        };
#pragma unroll
        for (int n = 0; n < PD; n++) issue(n);
        SB();
        static_for<0, 48>([&](auto N_) __attribute__((always_inline)) {
            constexpr int n = decltype(N_)::v;
            if (n + PD < 48) issue(n + PD);
            if constexpr (n < 16) acc0 = mfma_f16(fu[n % (PD + 1)], fk[n], acc0);
            else if constexpr (n < 32) acc1 = mfma_f16(fu[n % (PD + 1)], fk[n - 16], acc1);
            else acc1 = mfma_f16(fu[n % (PD + 1)], fk1[n % (PD + 1)], acc1);
            if (n == 1) *(LAS v4u*)(sm + LD_XU + t16) = v4u{__builtin_amdgcn_perm(udk[0], xc[0], 0x05040100u), __builtin_amdgcn_perm(udk[1], xc[1], 0x05040100u),
                                                           __builtin_amdgcn_perm(udk[2], xc[2], 0x05040100u), __builtin_amdgcn_perm(udk[3], xc[3], 0x05040100u)};
            if (n == 2) *(LAS v4u*)(sm + LD_XU + 8192 + t16) = v4u{__builtin_amdgcn_perm(udk[0], xc[0], 0x07060302u), __builtin_amdgcn_perm(udk[1], xc[1], 0x07060302u),
                                                                  __builtin_amdgcn_perm(udk[2], xc[2], 0x07060302u), __builtin_amdgcn_perm(udk[3], xc[3], 0x07060302u)};
            if (n == WFL_N) wfl = ld4(rs, l28 + h * 128, wf_s);
            if (n == W1_CHK) {
                PIN(acc0); SB();
                wait_flags(rs, l28 + h * 128, wf_s, wfl, tag);
                pf1 = PF1 ? ld4(rs, tid < 448 ? tid * 128 : OOB, WS_W1T + phi * 229376 + (i28 & 3) * 57344) : 0;
                STAMP(18);
            }
            if constexpr (n == 16 || n == 17) {
                constexpr int g = 2 * (n - 16);
                *(LAS f32x4*)(pw + LD_PA + g * 32) = f32x4{acc0[4 * g], acc0[4 * g + 1], acc0[4 * g + 2], acc0[4 * g + 3]};
                *(LAS f32x4*)(pw + LD_PA + g * 32 + 32) = f32x4{acc0[4 * g + 4], acc0[4 * g + 5], acc0[4 * g + 6], acc0[4 * g + 7]};
            }
            if (n == 17) {
                PIN(acc1);
                if (lane == 63) {
                    *(LAS f32x4*)(sm + LD_PA + wave * P_WAVE + 112) = f32x4{0.f, 0.f, 0.f, 0.f};
                    *(LAS f32x4*)(sm + LD_PB + wave * P_WAVE + 112) = f32x4{acc0[12], acc0[13], acc0[14], acc0[15]};
                }
            }
            if (n == 18) {
                BARRIER();
                if (PRIO_C) __builtin_amdgcn_s_setprio(1);
                STAMP(19);
                st4(rs, (b == 0 && tid == 0) ? WS_EPOCH : OOB, 0, tag);
                ce_read(0);
            }
            if (n >= 21 && n < 27) ce_part(0, n - 21);
            if (n == HF_N) {
                STAMP(22);
                if (!HF_OPT) wait_vm<w1_cnt(HF_N) - w1_cnt(26)>();
                st4(rs, flv, hf_set, tag);
                if (PRIO_C) __builtin_amdgcn_s_setprio(0);
                STAMP(23);
            }
            SB();
#pragma unroll
            for (int ks = w1_cnt(n); ks < w1_cnt(n + 1); ks++) w1_load(ks);
            SB();
        });
#pragma unroll
        for (int g = 0; g < 4; g++) *(LAS f32x4*)(pw + LD_PB + g * 32) = f32x4{acc1[4 * g], acc1[4 * g + 1], acc1[4 * g + 2], acc1[4 * g + 3]};
    }
    constexpr int W1_A = w1_cnt(48) + W1_MID > 28 ? 28 : w1_cnt(48) + W1_MID;
#pragma unroll
    for (int ks = w1_cnt(48); ks < W1_A; ks++) { w1_load(ks); SB(); }
    const int kap = phi * 8 + wave;
    const u32 w2fl = ld4(rs, l28, WS_WF2 + del * 128);
    v2u csg = __builtin_amdgcn_raw_buffer_load_b64(rs, WS_CS + col * 8, kap * 256, SC1);
    u32 hfl;
    if (HFL_EARLY) hfl = ld4(rs, hf_v, hf_s);
    BARRIER();
    STAMP(6);
    ce_read(1);
    int rot14[14], rot7[7];
    {
        int i = (2 * del + ((i28 & 3) * 7 >> 1)) * ROT_W2;
        i = i >= 14 ? i - 14 : i;
#pragma unroll
        for (int n = 0; n < 14; n++) { rot14[n] = i * 1024; i = i == 13 ? 0 : i + 1; }
        i = del * ROT_H;
#pragma unroll
        for (int n = 0; n < 7; n++) { rot7[n] = i * 8192; i = i == 6 ? 0 : i + 1; }
    }
    auto load_h = [&](int hf, int n) __attribute__((always_inline)) {
        glds16(srd, hvoff + hf * 2048, rot7[n] * 32, (u32)(size_t)sm + (hf ? LD_HB : LD_HA) + wave * 1024 + rot7[n]);
    };
#pragma unroll
    for (int i = 0; i < 5; i++) ce_part(1, i);
    SB();
    if (!HFL_EARLY) hfl = ld4(rs, hf_v, hf_s);
    SB();
    ce_part(1, 5);
    SB();
    STAMP(21);
#pragma unroll
    for (int ks = W1_A; ks < 28; ks++) { w1_load(ks); SB(); }
    wait_flags(rs, l28, WS_WF2 + del * 128, w2fl, tag);
    if (!__all(csg[1] == tag)) {
        int spins = SPIN_CAP;
#pragma unroll 1
        do {
            __builtin_amdgcn_s_sleep(4);
            csg = __builtin_amdgcn_raw_buffer_load_b64(rs, WS_CS + col * 8, kap * 256, SC1);
        } while (!__all(csg[1] == tag) && --spins > 0);
    }
    *(LAS u32*)(sm + LD_CS + wave * 128 + col * 4) = csg[0];
    wait_flags(rs, hf_v, hf_s, hfl, tag);
    STAMP(7);
#pragma unroll
    for (int n = 0; n < 7; n++) load_h(0, n);
    SB();
    wait_vm<7>();
    st4(rs, flv, hf_set + 8192, tag);
    SB();
    asm volatile("" :: "v"(pf1));
    wait_vm<1>();
    STAMP(8);
    BARRIER();
    STAMP(9);
    v4u w2[28];
    const int w2v = WS_W2T + (del * 2 * 112 + wave * 14) * 1024 + l16;
    auto w2_lds = [&](int f) __attribute__((always_inline)) { return f < 14 ? LD_PB + (wave * 4 + f - 10) * 1024 : LD_HA + (wave * 7 + f - 14) * 1024; };
    auto w2_load = [&](int f) __attribute__((always_inline)) {
        if (f >= 10 && f < 21) glds16(srd, w2v + (f & 1) * 112 * 1024, rot14[f >> 1], (u32)(size_t)sm + w2_lds(f));
        else w2[f] = ld16(rs, w2v + (f & 1) * 112 * 1024, rot14[f >> 1]);
    };
    const int af_v = (lane < 14 ? lane : 0) * 4, af_s = WS_AF + (tau * 16 + wave) * 128;
    const int av = WS_ACT + (tau2 * 112 + wave * 14) * 1024 + l16;
    const int dd = 64 * del + 8 * wave + 4 * h;
    LAS char* hra = sm + LD_HA + dd * 128 + trl + (((xcd & 1) << 6) ^ sw);
    v2u hr[2];
    u32 afl;
    v4u a0[14], a1[14];
    const int af_set = WS_AF + (tau * 16 + kap / 14) * 128 + (kap % 14) * 4;
    {
        LAS char* at0 = opaque(sm + LD_HA + 8 * h * 128 + trl + sw);
        LAS char* at1 = opaque(sm + LD_HA + 8 * h * 128 + trl + (64 ^ sw));
        LAS char* csr = opaque(sm + LD_CS + wave * 128 + h * 16);
        LAS char* as = opaque(sm + LD_HA + 8 * h * 128 + trl + (((wave & 1) << 6) ^ sw) + (wave >> 1) * 7 * 2048);
        LAS char* str = opaque(sm + LD_ST + col * 32);
        LAS char* cur;
        constexpr int PD = 4;
        v4u fr[PD + 1], sf, sf2;
        auto issue = [&](int n) __attribute__((always_inline)) {
            const int tile = n / 28, j = n % 28;
            if (j % 7 == 0) cur = opaque(((tile & 1) ? at1 : at0) + (seg7[j / 7] * 2048 + (tile >> 1) * 57344));
            LAS char* a = cur + (j % 7) * 2048;
            fr[n % (PD + 1)] = lds_tr2(a, a + 512);
        };
        f32x16 acc[2];
        float nm, k0, k1, sd2, eg, eu, ez, er, eo0;
        f32x4 cg4, cu4;
        f32x4 sta, stb;
        float s1, s2, s1b, s2b;
        u32 opk[4];
        auto stat_acc = [&](int k, int part, int which = 0) __attribute__((always_inline)) {
            const u32 f0 = which ? sf2[2 * part] : sf[2 * part], f1 = which ? sf2[2 * part + 1] : sf[2 * part + 1];
            if (k == 0 && part == 0) { s1 = 0.f; s2 = 0.f; s1b = 0.f; s2b = 0.f; }
            s1 = dot2bf(f0, 0x3f803f80u, s1); s2 = dot2bf(f0, f0, s2);
            s1b = dot2bf(f1, 0x3f803f80u, s1b); s2b = dot2bf(f1, f1, s2b);
        };
        auto stat_pin = [&]() __attribute__((always_inline)) { asm volatile("" : "+v"(s1), "+v"(s2), "+v"(s1b), "+v"(s2b)); };
        auto stat_write = [&](int hf) __attribute__((always_inline)) {
            const float a = half_sum(s1 + s1b), c = half_sum(s2 + s2b);
            *(LAS f32x2*)(str + (hf * 2 + (wave & 1)) * 1024 + (wave >> 1) * 8) = f32x2{a, c};
        };
        auto epi_read = [&](int tile) __attribute__((always_inline)) { sta = *(LAS f32x4*)(str + tile * 1024); stb = *(LAS f32x4*)(str + tile * 1024 + 16); };
        auto epi_stats = [&]() __attribute__((always_inline)) {
            float m = ((sta[0] + sta[2]) + (stb[0] + stb[2])) * (1.f / D), e2 = ((sta[1] + sta[3]) + (stb[1] + stb[3])) * (1.f / D);
            asm("" : "+v"(m)); asm("" : "+v"(e2));
            const float var = rbf(fmaxf(e2 - m * m, 0.f));
            const float sd = rbf(__builtin_amdgcn_sqrtf(rbf(var + 1e-5f))), r = __builtin_amdgcn_rcpf(sd);
            nm = -rbf(m); sd2 = sd * sd; k0 = r * -2.3022082f; k1 = (r * r) * (r * -0.10294324f);
        };
        v4u sg;
        auto epi_stats2 = [&](u32 st) __attribute__((always_inline)) {
            const float sd = bfhi(st), r = __builtin_amdgcn_rcpf(sd);
            nm = -bflo(st); sd2 = sd * sd; k0 = r * -2.3022082f; k1 = (r * r) * (r * -0.10294324f);
        };
        auto cs_read = [&](int g) __attribute__((always_inline)) { cg4 = *(LAS f32x4*)(csr + g * 32); cu4 = *(LAS f32x4*)(csr + 64 + g * 32); };
        auto slice = [&](const f32x16& a, int sidx) __attribute__((always_inline)) {
            const int e = sidx / 3, st = sidx % 3;
            if (st == 0) {
                if (e > 0) { float o = eu * er; asm("" : "+v"(o)); if ((e - 1) & 1) opk[(e - 1) >> 1] = pk_bf16(eo0, o); else eo0 = o; }
                if (e < 8) {
                    eg = __builtin_fmaf(nm, cg4[e & 3], a[e & 7]); eu = __builtin_fmaf(nm, cu4[e & 3], a[8 + (e & 7)]);
                    asm("" : "+v"(eg), "+v"(eu));
                }
            } else if (st == 1) { ez = __builtin_amdgcn_exp2f(eg * __builtin_fmaf(eg * eg, k1, k0)); asm("" : "+v"(ez)); }
            else { er = __builtin_amdgcn_rcpf(__builtin_fmaf(ez, sd2, sd2)); eu *= eg; asm("" : "+v"(er), "+v"(eu)); }
        };
        auto epi_four = [&](const f32x16& a, int g) __attribute__((always_inline)) {
            const f32x4 nm4 = {nm, nm, nm, nm}, sd4 = {sd2, sd2, sd2, sd2};
            const f32x4 gg = __builtin_elementwise_fma(nm4, cg4, f32x4{a[4 * g], a[4 * g + 1], a[4 * g + 2], a[4 * g + 3]});
            const f32x4 u = __builtin_elementwise_fma(nm4, cu4, f32x4{a[8 + 4 * g], a[9 + 4 * g], a[10 + 4 * g], a[11 + 4 * g]});
            const f32x4 z = gg * __builtin_elementwise_fma(gg * gg, f32x4{k1, k1, k1, k1}, f32x4{k0, k0, k0, k0});
            const f32x4 e = {__builtin_amdgcn_exp2f(z.x), __builtin_amdgcn_exp2f(z.y), __builtin_amdgcn_exp2f(z.z), __builtin_amdgcn_exp2f(z.w)};
            const f32x4 dn = __builtin_elementwise_fma(e, sd4, sd4);
            const f32x4 o = (gg * u) * f32x4{__builtin_amdgcn_rcpf(dn.x), __builtin_amdgcn_rcpf(dn.y), __builtin_amdgcn_rcpf(dn.z), __builtin_amdgcn_rcpf(dn.w)};
            opk[2 * g] = pk_bf16(o.x, o.y); opk[2 * g + 1] = pk_bf16(o.z, o.w);
        };
        auto epi_store = [&](int tile) __attribute__((always_inline)) {
            const int tok32 = (tile >> 1) * 32 + tau * 2 + (tile & 1);
            st16(rs, WS_ACT + l16 + (tok32 * 112 + kap) * 1024, v4u{opk[0], opk[1], opk[2], opk[3]});
        };
#pragma unroll
        for (int n = 0; n < PD; n++) issue(n);
        SB();
        static_for<0, 112>([&](auto N_) __attribute__((always_inline)) {
            constexpr int n = decltype(N_)::v;
            constexpr int tile = n / 28, j = n % 28;
            if (n == 52) {
                STAMP(27);
                wait_vm<g1_cum(52) - g1_cum(HB_N + 9)>();
                BARRIER();
                if (PRIO_G) __builtin_amdgcn_s_setprio(1);
                STAMP(28);
            }
            if (n + PD < 112) issue(n + PD);
            if (j == 0) {
                f32x16 z;
#pragma unroll
                for (int i = 0; i < 16; i++) z[i] = 0.f;
                acc[tile & 1] = mfma_bf16(wf[0], fr[n % (PD + 1)], z);
            } else {
                acc[tile & 1] = mfma_bf16(wf[j], fr[n % (PD + 1)], acc[tile & 1]);
            }
            if (MFMA_FIRST) SB();
            if constexpr (n < 28) {
                constexpr int sk = n;
                if (sk < 21 && sk % 3 == 0) { if (sk > 0) { stat_acc(sk / 3 - 1, 1); SB(); } sf = lds_tr2(as + (sk / 3) * 2048, as + (sk / 3) * 2048 + 512); }
                if (sk < 21 && sk % 3 == 2) stat_acc(sk / 3, 0);
                if (sk == 21) stat_acc(6, 1);
                if (sk == 22) stat_write(0);
            }
            if (n == HBP_N) hfl = ld4(rs, hf_v, hf_s + 8192);
            if (n == HB_N) {
                PIN(acc[0]);
                SB();
                STAMP(24);
                wait_flags(rs, hf_v, hf_s + 8192, hfl, tag);
                STAMP(25);
            }
            if (n == 48) hr[0] = lds_tr(hra);
            if (n == 27) { STAMP(26); BARRIER_RAW(); STAMP(29); }
            if (n == 27 || n == 55) epi_read(tile);
            if (n == 84) {
                PIN(acc[0]); PIN(acc[1]);
                SB();
                STAMP(30);
                if (!__all(sg[1] == tag && sg[3] == tag)) {
                    int spins = SPIN_CAP;
#pragma unroll 1
                    do {
                        __builtin_amdgcn_s_sleep(2);
                        sg = ld16(rs, WS_SG + col * 16, tau * 512);
                    } while (!__all(sg[1] == tag && sg[3] == tag) && --spins > 0);
                }
                STAMP(31);
            }
            if (tile == 1) {
                if (j == 0) cs_read(0);
                if (j == 1) epi_stats();
                if (j >= 2 && j < 27) slice(acc[0], j - 2);
                if (j == 11) { SB(); cs_read(1); }
            }
            if (tile == 2) {
                if (j == 0) cs_read(0);
                if (j == 1) epi_stats();
                if (j >= 2 && j < 10) { slice(acc[1], 3 * (j - 2)); if (j == 5) { SB(); cs_read(1); } slice(acc[1], 3 * (j - 2) + 1); slice(acc[1], 3 * (j - 2) + 2); }
                if (j == 10) slice(acc[1], 24);
            }
            if (tile == 3) {
                if (j == 0) cs_read(0);
                if (j == 1) epi_stats2(sg[0]);
                if (j >= 2 && j < 27) slice(acc[0], j - 2);
                if (j == 11) { SB(); cs_read(1); }
            }
            if (n == AFC_N) {
                PIN(acc[1]); asm volatile("" : "+v"(eg), "+v"(eu), "+v"(ez), "+v"(er), "+v"(eo0), "+v"(opk[0]), "+v"(opk[1]));
                SB();
                wait_flags(rs, af_v, af_s, afl, tag);
            }
            constexpr int op = g1_op(n);
            if constexpr (op >= 60) a0[op - 60] = ld16(rs, av, rot14[op - 60]);
            if constexpr (op >= 0 && op < 28) w2_load(op);
            if constexpr (op >= 32 && op < 40) load_h(1, op - 32);
            if constexpr (op >= 40 && op < 50) epi_store(op - 40);
            if constexpr (op == 50) { wait_vm<g1_cum(AF_N) - g1_cum(ST1_N + 1)>(); st4(rs, flv, af_set, tag); if (PRIO_G) __builtin_amdgcn_s_setprio(0); if (BAR_AF) BARRIER_RAW(); }
            if constexpr (op == 51) afl = ld4(rs, af_v, af_s);
            if constexpr (op == 52) st4(rs, (tid == 0 && del == 0) ? 0 : OOB, WS_TR + ((xcd & 1) * 16 + tau) * 128, tag);
            if constexpr (op == 53) sg = ld16(rs, WS_SG + col * 16, tau * 512);
            SB();
        });
        PIN(acc[1]);
        STAMP(10);
        STAMP(11);
        cs_read(0);
        epi_stats2(sg[2]);
        epi_four(acc[1], 0);
        cs_read(1);
        epi_four(acc[1], 1);
        epi_store(3);
        if (OPT) { SB(); st4(rs, flv, af_set + 1024, tag); }
    }
    SB();
#pragma unroll
    for (int n = A0_IN; n < 14; n++) { a0[n] = ld16(rs, av, rot14[n]); SB(); }
    hr[1] = lds_tr(hra + 57344);
#pragma unroll
    for (int f = 10; f < 21; f++) w2[f] = *(LAS v4u*)(sm + w2_lds(f) + l16);
    STAMP(12);
    BARRIER();
    f32x16 acc[2][2];
#pragma unroll
    for (int i = 0; i < 16; i++) { acc[0][0][i] = 0.f; acc[0][1][i] = 0.f; acc[1][0][i] = 0.f; acc[1][1][i] = 0.f; }
    LAS char* rw = opaque(sm + wave * 1024 + l16);
    LAS char* rr = opaque(sm + wave * 8192 + l16);
    auto red_write = [&](int hf) __attribute__((always_inline)) {
#pragma unroll
        for (int o = 0; o < 8; o++) {
            const f32x16& t = acc[o >> 2][hf];
            const int r = 4 * (o & 3);
            *(LAS f32x4*)(rw + hf * 65536 + o * 8192) = f32x4{t[r], t[r + 1], t[r + 2], t[r + 3]};
        }
    };
    auto red_finish = [&](int hf) __attribute__((always_inline)) {
        f32x4 v[8];
#pragma unroll
        for (int s = 0; s < 8; s++) v[s] = *(LAS f32x4*)(rr + hf * 65536 + s * 1024);
        const f32x4 t = ((v[0] + v[1]) + (v[2] + v[3])) + ((v[4] + v[5]) + (v[6] + v[7]));
        const u32 ta = pk_bf16(t[0], t[1]), tb = pk_bf16(t[2], t[3]);
        const int tok = 1024 * hf + 32 * tau2 + col;
        *(v2u*)(p.out + (size_t)tok * D + dd) = v2u{pk_bf16(bflo(ta) + bflo(hr[hf][0]), bfhi(ta) + bfhi(hr[hf][0])),
                                                    pk_bf16(bflo(tb) + bflo(hr[hf][1]), bfhi(tb) + bfhi(hr[hf][1]))};
    };
#pragma unroll
    for (int n = 0; n < 14; n++) {
        acc[0][0] = mfma_bf16(w2[2 * n], a0[n], acc[0][0]);
        acc[1][0] = mfma_bf16(w2[2 * n + 1], a0[n], acc[1][0]);
        if (!OPT && n == AFB_N) { wait_vm<14 - A0_IN>(); st4(rs, flv, af_set + 1024, tag); }
        if (TAIL && n == AFBP_N) afl = ld4(rs, af_v, af_s + 1024);
        SB();
    }
    PIN(acc[0][0]); PIN(acc[1][0]);
    STAMP(13);
    red_write(0);
    BARRIER();
    afl = ld4(rs, af_v, af_s + 1024);
    SB();
    red_finish(0);
    SB();
    wait_flags(rs, af_v, af_s + 1024, afl, tag);
    STAMP(14);
#pragma unroll
    for (int n = 0; n < 14; n++) { a1[n] = ld16(rs, av + 32 * 112 * 1024, rot14[n]); SB(); }
    SB();
    STAMP(15);
#pragma unroll
    for (int n = 0; n < 14; n++) {
        acc[0][1] = mfma_bf16(w2[2 * n], a1[n], acc[0][1]);
        acc[1][1] = mfma_bf16(w2[2 * n + 1], a1[n], acc[1][1]);
        SB();
    }
    STAMP(16);
    red_write(1);
    BARRIER();
    if (TAIL == 2) red_finish(0);
    red_finish(1);
    STAMP(17);
    STAMP_FLUSH();
    asm volatile(".Lfb_code_end:");
}

}

extern "C" void kernel_launch(void* const* d_in, const int* in_sizes, int n_in,
                              void* d_out, int out_size, void* d_ws, size_t ws_size,
                              hipStream_t stream) {
    Params p;
    p.x = (const u16*)d_in[0]; p.Are = (const u16*)d_in[1]; p.Aim = (const u16*)d_in[2];
    p.Bre = (const u16*)d_in[3]; p.Bim = (const u16*)d_in[4]; p.Cre = (const u16*)d_in[5]; p.Cim = (const u16*)d_in[6];
    p.logdt = (const u16*)d_in[7]; p.Dskip = (const u16*)d_in[8];
    p.wg = (const u16*)d_in[10]; p.wu = (const u16*)d_in[11]; p.wd = (const u16*)d_in[12];
    p.ws = (char*)d_ws; p.out = (u16*)d_out;
    (void)hipFuncSetAttribute((const void*)fused_block, hipFuncAttributeMaxDynamicSharedMemorySize, LDS_BYTES);
    fused_block<<<256, 512, LDS_BYTES, stream>>>(p);
}
```

```cpp
#include <hip/hip_runtime.h>
#include <stdint.h>

namespace {
#define TV 2
#define B1P 10
#define ST2N 100

constexpr int L = 2048, D = 448, N = 256, F = 1792;

typedef unsigned short u16;
typedef unsigned int u32;
typedef unsigned long long u64;
typedef u32 v4u __attribute__((ext_vector_type(4)));
typedef u32 v2u __attribute__((ext_vector_type(2)));
typedef float f32x2 __attribute__((ext_vector_type(2)));
typedef float f32x4 __attribute__((ext_vector_type(4)));
typedef float f32x16 __attribute__((ext_vector_type(16)));
typedef _Float16 f16x2 __attribute__((ext_vector_type(2)));
typedef _Float16 f16x8 __attribute__((ext_vector_type(8)));
typedef __bf16 bf16x2 __attribute__((ext_vector_type(2)));
typedef __bf16 bf16x8 __attribute__((ext_vector_type(8)));
typedef short s16x4 __attribute__((ext_vector_type(4)));
#define LAS __attribute__((address_space(3)))
#define SB() __builtin_amdgcn_sched_barrier(0)
#define PIN(x) asm volatile("" : "+v"(x))
#define BARRIER() asm volatile("s_waitcnt lgkmcnt(0)\n\ts_barrier" ::: "memory")
#define BARRIER_RAW() asm volatile("s_barrier" ::: "memory")
constexpr int SC1 = 16;

constexpr int WS_EPOCH = 0;
constexpr int WS_WF2 = 1024;
constexpr int WS_TR = 4096;
constexpr int WS_HF = 8192;
constexpr int WS_AF = WS_HF + 64 * 256;
constexpr int WS_SG = WS_AF + 65536;
constexpr int WS_WF1 = WS_SG + 8192;
constexpr int WS_CS = WS_WF1 + 8192;
constexpr int WS_ST1 = WS_CS + 32768;
constexpr int WS_XX = WS_ST1 + 16384;
constexpr int WS_W1T = WS_XX + 224 * 16384;
constexpr int WS_W2T = WS_W1T + 112 * 28 * 1024;
constexpr int WS_HT = WS_W2T + 14 * 112 * 1024;
constexpr int WS_ACT = WS_HT + D * L * 2;
constexpr int WS_END = WS_ACT + 64 * 112 * 1024;
constexpr int SPIN_CAP = 1 << 16;

__device__ __forceinline__ float bflo(u32 v) { return __builtin_bit_cast(float, v << 16); }
__device__ __forceinline__ float bfhi(u32 v) { return __builtin_bit_cast(float, v & 0xffff0000u); }
__device__ __forceinline__ u32 pk_bf16(float a, float b) { f32x2 f = {a, b}; return __builtin_bit_cast(u32, __builtin_convertvector(f, bf16x2)); }
__device__ __forceinline__ u32 pk_bf16s(float a, float b) { asm("" : "+v"(a)); asm("" : "+v"(b)); return pk_bf16(a, b); }
__device__ __forceinline__ u32 pk_f16(float a, float b) { f32x2 f = {a, b}; return __builtin_bit_cast(u32, __builtin_convertvector(f, f16x2)); }
__device__ __forceinline__ float rbf(float a) { return bflo(pk_bf16(a, 0.f)); }
__device__ __forceinline__ float dot2bf(u32 a, u32 b, float c) {
    return __builtin_amdgcn_fdot2_f32_bf16(__builtin_bit_cast(bf16x2, a), __builtin_bit_cast(bf16x2, b), c, false);
}
__device__ __forceinline__ f32x16 mfma_bf16(v4u a, v4u b, f32x16 c) {
    return __builtin_amdgcn_mfma_f32_32x32x16_bf16(__builtin_bit_cast(bf16x8, a), __builtin_bit_cast(bf16x8, b), c, 0, 0, 0);
}
__device__ __forceinline__ f32x16 mfma_f16(v4u a, v4u b, f32x16 c) {
    return __builtin_amdgcn_mfma_f32_32x32x16_f16(__builtin_bit_cast(f16x8, a), __builtin_bit_cast(f16x8, b), c, 0, 0, 0);
}
__device__ __forceinline__ v2u lds_tr(LAS char* p) {
    return __builtin_bit_cast(v2u, __builtin_amdgcn_ds_read_tr16_b64_v4i16((LAS s16x4*)p));
}
__device__ __forceinline__ v4u lds_tr2(LAS char* p0, LAS char* p1) {
    const v2u lo = lds_tr(p0), hi = lds_tr(p1);
    return v4u{lo[0], lo[1], hi[0], hi[1]};
}
template <int CTRL> __device__ __forceinline__ float dpp(float v) {
    return __builtin_bit_cast(float, __builtin_amdgcn_update_dpp(0, __builtin_bit_cast(int, v), CTRL, 0xf, 0xf, true));
}
__device__ __forceinline__ float rdl(float v, int l) { return __builtin_bit_cast(float, __builtin_amdgcn_readlane(__builtin_bit_cast(int, v), l)); }
__device__ __forceinline__ float wave_sum(float v) {
    v += dpp<0xB1>(v); v += dpp<0x4E>(v); v += dpp<0x141>(v); v += dpp<0x140>(v);
    return (rdl(v, 0) + rdl(v, 16)) + (rdl(v, 32) + rdl(v, 48));
}
__device__ __forceinline__ float half_sum(float v) {
    const auto r = __builtin_amdgcn_permlane32_swap(__builtin_bit_cast(u32, v), __builtin_bit_cast(u32, v), false, false);
    return __builtin_bit_cast(float, (u32)r[0]) + __builtin_bit_cast(float, (u32)r[1]);
}
template <int I> struct IC { static constexpr int v = I; };
template <int B, int E, class Fn> __device__ __forceinline__ void static_for(Fn&& f) {
    if constexpr (B < E) { f(IC<B>{}); static_for<B + 1, E>(f); }
}

typedef f32x2 cf;
__device__ __forceinline__ cf rot(cf b) { return cf{-b.y, b.x}; }
__device__ __forceinline__ cf cmul(cf a, cf b) { return __builtin_elementwise_fma(a.yy, rot(b), a.xx * b); }
__device__ __forceinline__ cf cmulc(cf a, cf b) { return __builtin_elementwise_fma(a.yy, b.yx, a.xx * rot(b).yx); }
__device__ __forceinline__ u32 pkh(cf a) { return __builtin_bit_cast(u32, __builtin_convertvector(a, f16x2)); }

typedef __amdgpu_buffer_rsrc_t rsrc_t;
__device__ __forceinline__ v4u ld16(rsrc_t r, int voff, int soff) { return __builtin_amdgcn_raw_buffer_load_b128(r, voff, soff, SC1); }
__device__ __forceinline__ u32 ld4(rsrc_t r, int voff, int soff) { return __builtin_amdgcn_raw_buffer_load_b32(r, voff, soff, SC1); }
__device__ __forceinline__ void st16(rsrc_t r, int voff, v4u v) { __builtin_amdgcn_raw_buffer_store_b128(v, r, voff, 0, SC1); }
__device__ __forceinline__ void st8(rsrc_t r, int voff, int soff, v2u v) { __builtin_amdgcn_raw_buffer_store_b64(v, r, voff, soff, SC1); }
__device__ __forceinline__ void st4(rsrc_t r, int voff, int soff, u32 v) { __builtin_amdgcn_raw_buffer_store_b32(v, r, voff, soff, SC1); }
template <int N> __device__ __forceinline__ void wait_vm() { asm volatile("s_waitcnt vmcnt(%0)" :: "i"(N) : "memory"); }
constexpr int OOB = 0x7f000000;

__device__ __forceinline__ void wait_flags(rsrc_t rs, int voff, int soff, u32 v, u32 tag) {
    if (!__all(v == tag)) {
        int spins = SPIN_CAP;
#pragma unroll 1
        do {
            __builtin_amdgcn_s_sleep(1);
            v = ld4(rs, voff, soff);
        } while (!__all(v == tag) && --spins > 0);
    }
    __builtin_amdgcn_fence(__ATOMIC_ACQUIRE, "wavefront");
}
__device__ __forceinline__ void glds16(v4u srd, int voff, int soff, u32 lds_dst) {
    asm volatile("s_mov_b32 m0, %3\n\ts_nop 0\n\tbuffer_load_dwordx4 %0, %1, %2 offen sc1 lds"
                 :: "v"(voff), "s"(srd), "s"(soff), "s"(lds_dst) : "memory");
}
__device__ __forceinline__ LAS char* opaque(LAS char* q) { u32 a = (u32)(size_t)q; asm volatile("" : "+v"(a)); return (LAS char*)(size_t)a; }

struct Params {
    const u16 *x, *Are, *Aim, *Bre, *Bim, *Cre, *Cim, *logdt, *Dskip, *wg, *wu, *wd;
    char* ws;
    u16* out;
};

constexpr int XY_STRIDE = 272, XY_ROWS = 59, XY_WAVE = XY_STRIDE * XY_ROWS;
constexpr int P_WAVE = 33 * 144;
constexpr int LD_PB = 0, LD_PA = 38144;
constexpr int LD_HA = 38144, LD_HB = LD_HA + 57344;
constexpr int LD_XU = LD_HB;
constexpr int LD_KP = 128512;
constexpr int LD_U = LD_KP + 8 * 1536;
constexpr int LD_SB = LD_HB + 57344;
constexpr int LD_ST = LD_SB + 64;
constexpr int LD_CS = LD_ST + 4096;
constexpr int LDS_BYTES = LD_CS + 1024;
static_assert(LD_U + 2 * 4160 <= LD_SB && LDS_BYTES <= 163840, "lds map");

__device__ __forceinline__ v4u tr_piece(const v4u (&in)[8], int c) {
    v4u o;
#pragma unroll
    for (int kp = 0; kp < 4; kp++)
        o[kp] = __builtin_amdgcn_perm(in[2 * kp + 1][c >> 1], in[2 * kp][c >> 1], (c & 1) ? 0x07060302u : 0x05040100u);
    return o;
}
__device__ __forceinline__ void w1_task_load(const Params& p, int task, int lane, v2u (&in)[8]) {
    const int t = (task < 0 ? 0 : task) >> 1, c4 = task & 1;
    const int phi = t / 28, r = t - phi * 28, mat = r >= 14, kp = r - 14 * mat;
    const rsrc_t rw = __builtin_amdgcn_make_buffer_rsrc((void*)(mat ? p.wu : p.wg), 0, task < 0 ? 0 : D * F * 2, 0x00020000);
    const int voff = (8 * (lane >> 4) * F + (lane & 15) * 8) * 2;
#pragma unroll
    for (int k = 0; k < 8; k++) in[k] = __builtin_amdgcn_raw_buffer_load_b64(rw, voff + (k & 1) * F * 2, ((32 * kp + (k & 6)) * F + phi * 128 + 4 * c4) * 2, 0);
}
__device__ __forceinline__ void w1_task_store(rsrc_t rs, int task, int lane, const v2u (&in)[8]) {
    const int t = task >> 1, c4 = task & 1;
    const int phi = t / 28, r = t - phi * 28, mat = r >= 14, kp = r - 14 * mat;
    const int voff = WS_W1T + ((phi * 8 + 4 * c4) * 28 + 2 * kp) * 1024 + mat * 256 + (lane >> 4) * 512 + (lane & 15) * 16;
#pragma unroll
    for (int c = 0; c < 4; c++) {
        v4u o;
#pragma unroll
        for (int kp2 = 0; kp2 < 4; kp2++) o[kp2] = __builtin_amdgcn_perm(in[2 * kp2 + 1][c >> 1], in[2 * kp2][c >> 1], (c & 1) ? 0x07060302u : 0x05040100u);
        st16(rs, voff + c * 28 * 1024, o);
    }
}
__device__ __forceinline__ int w1_flag(int task) { const int t = task >> 1, phi = t / 28; return WS_WF1 + (phi * 2 + (task & 1)) * 128 + (t - phi * 28) * 4; }
__device__ __forceinline__ void w2_task(const Params& p, rsrc_t rs, int task, int lane, LAS char* scr) {
    const int del = task / 28, kg = task - del * 28;
    const int k4 = (lane >> 3) & 3, ks = kg * 4 + k4, h = lane >> 5, dr = lane & 7;
    const u16* src = p.wd + (size_t)((ks >> 3) * 128 + 32 * h + (ks & 7)) * D + del * 64 + dr * 8;
    v4u in[8];
#pragma unroll
    for (int j = 0; j < 8; j++) in[j] = __builtin_nontemporal_load((const v4u*)(src + (size_t)(8 * ((j & 3) + 8 * (j >> 2))) * D));
    LAS char* w = scr + ((dr >> 2) * 4 + k4) * 1024 + (32 * h + 8 * (dr & 3)) * 16;
#pragma unroll
    for (int c = 0; c < 8; c++) *(LAS v4u*)(w + c * 16) = tr_piece(in, c);
    const int voff = WS_W2T + (del * 2 * 112 + kg * 4) * 1024 + lane * 16;
#pragma unroll
    for (int f = 0; f < 8; f++) st16(rs, voff + ((f >> 2) * 112 + (f & 3)) * 1024, *(LAS v4u*)(scr + f * 1024 + lane * 16));
}

constexpr u32 GR_T = 320, CS_T = 520;
constexpr int W1_CHK = 2;
constexpr int w1_cnt(int n) { return n <= W1_CHK ? 0 : 4 + ((n - W1_CHK - 1) * 2) / 5; }
constexpr int HF_N = 37;
constexpr int HBP_N = 14, HB_N = 26;
constexpr int ST1_N = 67, AF_N = 83;
constexpr int SG_N = 77;
constexpr int AFP_N = 94, AFC_N = 103, A0_IN = 7;
constexpr int AFB_N = 4;
constexpr int g1_op(int n) {
    return n == 27 ? 52 : (n > HB_N && n <= HB_N + 8) ? 32 + (n - HB_N - 1) - (n > 27)
         : n == 55 ? 40 : n == ST1_N ? 41 : n == ST2N ? 42 : n == AF_N ? 50 : n == AFP_N ? 51 : n == SG_N ? 53
         : (n >= 1 && n < 21 && (n & 1) == 1) ? (n - 1) / 2
         : (n >= 36 && n < 52 && (n & 3) == 0) ? 10 + (n - 36) / 4
         : (n == 54 || (n >= 68 && n < 80 && (n & 1) == 0)) ? (n == 54 ? 14 : 15 + (n - 68) / 2)
         : (n >= 85 && n < 99 && (n & 1) == 1) ? 21 + (n - 85) / 2
         : (n > AFC_N && n <= AFC_N + A0_IN) ? 60 + (n - AFC_N - 1)
         : -1;
}
constexpr bool g1_all() { int m = 0; for (int i = 0; i < 112; i++) if (g1_op(i) >= 0 && g1_op(i) < 28) m |= 1 << g1_op(i); return m == (1 << 28) - 1; }
constexpr int g1_cum(int n) { int c = 0; for (int i = 0; i < n; i++) c += g1_op(i) >= 0; return c; }
static_assert(g1_all() && HB_N >= 23 && HB_N < 27 && AF_N > ST1_N && AFP_N > AF_N && AFC_N > AFP_N && AFC_N >= 99 && AFC_N + A0_IN < 111 && w1_cnt(48) <= 28, "schedule");

__global__ __launch_bounds__(512, 2) void fused_block(Params p) {
    extern __shared__ __attribute__((aligned(16))) char smem_[];
    LAS char* sm = (LAS char*)smem_;
    const int tid = threadIdx.x, lane = tid & 63;
    const int wave = __builtin_amdgcn_readfirstlane(tid >> 6);
    const int b = blockIdx.x;
    const bool main_blk = b < 224;
    const int xcd = b & 7, i28 = b >> 3;
    u64 pc0; u32 clen;
    asm volatile("s_getpc_b64 %0\n.Lfb_code_begin:\n\ts_mov_b32 %1, .Lfb_code_end-.Lfb_code_begin" : "=s"(pc0), "=s"(clen));
    const u32 cpf = __builtin_amdgcn_raw_buffer_load_b32(__builtin_amdgcn_make_buffer_rsrc((void*)pc0, 0, main_blk ? 0 : clen, 0x00020000),
                                                         lane < 16 ? lane * 64 : OOB, (wave * 4 + (i28 & 3)) * 1024, 0);
    const int l16 = lane * 16;
    const int rbase = 16 * (b >> 1) + 2 * (b & 1);
    int x2v = lane < 56 ? (lane & 3) * (4 * D * 2) + (lane >> 2) * 8 : OOB, xrv = lane < 56 ? l16 : OOB, t2 = tid * 2;
    int x2s = (rbase * D + 56 * wave) * 2, xrs = (rbase + 4 * (wave >> 1) + (wave & 1)) * D * 2;
    int pair = xcd * 28 + i28;
    asm volatile("" : "+v"(x2v), "+v"(xrv), "+v"(t2), "+s"(x2s), "+s"(xrs), "+s"(pair));
    SB();
    const rsrc_t rs = __builtin_amdgcn_make_buffer_rsrc(p.ws, 0, WS_END, 0x00020000);
    asm volatile("" :: "s"(p.x), "s"(p.wd), "s"(p.ws));
    const u32 tstart = (u32)__builtin_amdgcn_s_memrealtime();
    const u32 tagv = ld4(rs, WS_EPOCH, 0);

    const rsrc_t rx = __builtin_amdgcn_make_buffer_rsrc((void*)p.x, 0, L * D * 2, 0x00020000);
    const v2u xa = __builtin_amdgcn_raw_buffer_load_b64(rx, x2v, x2s, 2);
    const v2u xb = __builtin_amdgcn_raw_buffer_load_b64(rx, x2v + D * 2, x2s, 2);
    const v4u xr = __builtin_amdgcn_raw_buffer_load_b128(rx, xrv, xrs, 2);
    SB();
    const int ch = wave >> 2, wq = wave & 3;
    const int d0 = 2 * pair;
    const int psz = main_blk ? D * N * 2 : 0;
    auto ldp = [&](const u16* q) __attribute__((always_inline)) -> u32 {
        return __builtin_amdgcn_raw_buffer_load_b16(__builtin_amdgcn_make_buffer_rsrc((void*)q, 0, psz, 0x00020000), t2, d0 * N * 2, 2);
    };
    const u32 r_ldt = __builtin_amdgcn_raw_buffer_load_b32(__builtin_amdgcn_make_buffer_rsrc((void*)p.logdt, 0, main_blk ? D * 2 : 0, 0x00020000), 0, d0 * 2, 0);
    const u32 r_are = ldp(p.Are), r_aim = ldp(p.Aim), r_bre = ldp(p.Bre), r_bim = ldp(p.Bim), r_cre = ldp(p.Cre), r_cim = ldp(p.Cim);
    const u32 r_dsk = __builtin_amdgcn_raw_buffer_load_b32(__builtin_amdgcn_make_buffer_rsrc((void*)p.Dskip, 0, main_blk ? D * 2 : 0, 0x00020000), 0, d0 * 2, 0);
    const int hw = (b - 224) + 32 * wave;
    const int w1task = !main_blk ? -1 : wave < 2 ? pair * 2 + wave : (wave < 4 && pair < 168) ? 448 + pair * 2 + wave - 2 : -1;
    SB();
    const int h = lane >> 5, col = lane & 31;
    const int G1 = (lane >> 4) & 1, q4 = (lane & 15) >> 2, pp = lane & 3;
    int xxv = lane < 56 ? WS_XX + ((28 * wave + 2 * (lane >> 2)) * 2 + (b & 1)) * 8192 + (4 * (b >> 1) + (lane & 3)) * 16 : OOB;
    int flv = lane == 0 ? 0 : OOB;
    asm volatile("" : "+v"(xxv), "+v"(flv));
    LAS char* xy = sm + wave * XY_WAVE;
    LAS char* xyw = opaque(xy + lane * 4);
    LAS char* pa = opaque(xy + (32 + (int)((0x1713100d0a080400ull >> (8 * (col >> 2))) & 255) + (col & 3)) * XY_STRIDE + h * 16);
    LAS char* pb = opaque(xy + col * XY_STRIDE + h * 16);
    LAS char* exw = opaque(xy + l16);
    LAS char* exr = opaque(sm + (ch * 4) * XY_WAVE + wq * 1024 + l16);
    LAS char* kpw = opaque(sm + LD_KP + ch * 4 * 1536 + 512 + (wq < 2 ? 64 * wq + 2 * col + h : wq == 2 ? 64 * (2 + h) + 2 * col : 64 * (4 + 2 * h) + 2 * col) * 2);
    LAS char* uw = opaque(sm + LD_U + 64 + tid * 8);
    LAS char* ua = opaque(sm + LD_U + ch * 4160 + 2 * (32 + 32 + 16 * G1 - 32 * h - 16 - 4 * q4 + 4 * pp));
    LAS char* ka = opaque(sm + LD_KP + wave * 1536 + 2 * (256 - 248 + 8 * col + 8 * h));
    LAS char* pw = opaque(sm + wave * P_WAVE + (col + 1) * 144 + h * 16);
    *(LAS v2u*)(sm + LD_KP + wave * 1536 + lane * 8) = v2u{0, 0};
    if (tid < 32) *(LAS u32*)(sm + LD_U + (tid >> 4) * 4160 + (tid & 15) * 4) = 0;
    SB();
    const u32 tag = __builtin_amdgcn_readfirstlane(tagv) + 1u;
    st16(rs, xxv, v4u{xa[0], tag, xb[0], tag});
    st16(rs, xxv + 16384, v4u{xa[1], tag, xb[1], tag});
    SB();
    {
        float a = 0.f, q = 0.f;
#pragma unroll
        for (int i = 0; i < 4; i++) { a = dot2bf(xr[i], 0x3f803f80u, a); q = dot2bf(xr[i], xr[i], q); }
        const float mean = wave_sum(a) * (1.f / D), ex2 = wave_sum(q) * (1.f / D);
        const float var = rbf(fmaxf(ex2 - mean * mean, 0.f));
        const float sd = __builtin_amdgcn_sqrtf(rbf(var + 1e-5f));
        st8(rs, flv, WS_ST1 + (((b & 1) * 512 + 4 * (b >> 1) + (wave >> 1)) * 2 + (wave & 1)) * 8, v2u{pk_bf16(mean, sd), tag});
    }
    SB();
    v2u tin[8];
    w1_task_load(p, w1task, lane, tin);
    SB();
    const int tau = main_blk ? (xcd >> 1) * 4 + (i28 & 3) : (b & 15), phi = (xcd & 1) * 7 + (i28 >> 2);
    const int del = i28 >> 2, tau2 = 2 * tau + (xcd & 1);
    const int trl = q4 * 128 + 8 * (pp & 1) + 16 * (2 * G1 + (pp >> 1)), sw = (q4 & 2) << 5;
    const int hf_v = (lane < 56 ? lane : 0) * 4, hf_s = WS_HF + ((tau >> 2) * 8 + wave) * 256;
    const v4u srd = {(u32)(size_t)p.ws, (u32)((size_t)p.ws >> 32) & 0xffffu, (u32)WS_END, 0x00020000u};
    const int hvoff = WS_HT + ((8 * wave + (lane >> 3)) * L + tau * 64) * 2 + (((lane & 7) ^ (((lane >> 3) & 2) << 1)) << 4);
    if (!main_blk) {
        BARRIER_RAW();
        const int w2task = hw;
        if (w2task < 196) {
            w2_task(p, rs, w2task, lane, sm + LD_HB + wave * 8192);
            wait_vm<0>();
            st4(rs, flv, WS_WF2 + (w2task / 28) * 128 + (w2task % 28) * 4, tag);
        }
        asm volatile("" :: "v"(cpf), "v"(tin[7]));
        const int unit = 255 - hw;
        if (unit < 112) {
            while ((u32)__builtin_amdgcn_s_memrealtime() - tstart < CS_T) __builtin_amdgcn_s_sleep(8);
            const int fv = (col < 28 ? col : 0) * 4, fs = WS_WF1 + ((unit >> 3) * 2 + ((unit >> 2) & 1)) * 128;
            wait_flags(rs, fv, fs, ld4(rs, fv, fs), tag);
            float c0 = 0.f, c1 = 0.f;
#pragma unroll 1
            for (int k0 = 0; k0 < 28; k0 += 14) {
                v4u w[14];
#pragma unroll
                for (int ks = 0; ks < 14; ks++) w[ks] = ld16(rs, l16 + (ks & 3) * 1024, WS_W1T + (unit * 28 + k0 + (ks & ~3)) * 1024);
#pragma unroll
                for (int ks = 0; ks < 14; ks++) {
                    c0 = dot2bf(w[ks][0], 0x3f803f80u, c0); c1 = dot2bf(w[ks][1], 0x3f803f80u, c1);
                    c0 = dot2bf(w[ks][2], 0x3f803f80u, c0); c1 = dot2bf(w[ks][3], 0x3f803f80u, c1);
                }
            }
            st8(rs, lane < 32 ? WS_CS + lane * 8 : OOB, unit * 256, v2u{__builtin_bit_cast(u32, half_sum(c0 + c1)), tag});
        }
        {
            const int tl = (b >> 4) & 1;
            if (wave == 0) {
                int spins = SPIN_CAP;
#pragma unroll 1
                while (ld4(rs, 0, WS_TR + (b - 224) * 128) != tag && --spins > 0) __builtin_amdgcn_s_sleep(4);
            }
            BARRIER();
            if (wave < 7) {
                const int gv = WS_HT + ((lane >> 2) * L + 1024 + tau * 64 + tl * 32) * 2 + (lane & 3) * 16;
                LAS char* as = sm + 4096 + wave * 4096 + (8 * h + q4) * 64 + G1 * 32 + pp * 8;
#pragma unroll
                for (int n = 0; n < 4; n++) glds16(srd, gv, (wave * 4 + n) * 65536, (u32)(size_t)sm + 4096 + wave * 4096 + n * 1024);
                wait_vm<0>();
                v4u f[4];
#pragma unroll
                for (int k = 0; k < 4; k++) f[k] = lds_tr2(as + k * 1024, as + k * 1024 + 256);
                float s1 = 0.f, s2 = 0.f, s1b = 0.f, s2b = 0.f;
#pragma unroll
                for (int k = 0; k < 4; k++) {
                    s1 = dot2bf(f[k][0], 0x3f803f80u, s1); s2 = dot2bf(f[k][0], f[k][0], s2);
                    s1b = dot2bf(f[k][1], 0x3f803f80u, s1b); s2b = dot2bf(f[k][1], f[k][1], s2b);
                    s1 = dot2bf(f[k][2], 0x3f803f80u, s1); s2 = dot2bf(f[k][2], f[k][2], s2);
                    s1b = dot2bf(f[k][3], 0x3f803f80u, s1b); s2b = dot2bf(f[k][3], f[k][3], s2b);
                }
                *(LAS f32x2*)(sm + wave * 512 + lane * 8) = f32x2{s1 + s1b, s2 + s2b};
            }
            BARRIER();
            if (wave == 0) {
                f32x2 t = *(LAS f32x2*)(sm + lane * 8);
#pragma unroll
                for (int k = 1; k < 7; k++) t += *(LAS f32x2*)(sm + k * 512 + lane * 8);
                const float mean = half_sum(t.x) * (1.f / D), ex2 = half_sum(t.y) * (1.f / D);
                const float var = rbf(fmaxf(ex2 - mean * mean, 0.f));
                st8(rs, lane < 32 ? WS_SG + col * 16 + tl * 8 : OOB, tau * 512, v2u{pk_bf16(mean, __builtin_amdgcn_sqrtf(rbf(var + 1e-5f))), tag});
            }
        }
        return;
    }
    asm volatile("" :: "v"(r_are), "v"(r_aim), "v"(r_bre), "v"(r_bim), "v"(r_cre), "v"(r_cim), "v"(r_dsk), "v"(cpf));
    SB();

    float unscale;
    {
        const float dt = rbf(__expf(ch ? bfhi(r_ldt) : bflo(r_ldt)));
        const float ar = dt * bflo(r_are), ai = dt * bflo(r_aim);
        const float br = dt * bflo(r_bre), bi = dt * bflo(r_bim);
        const float cr = bflo(r_cre), ci = bflo(r_cim);
        cf cb = {cr * br - ci * bi, -(cr * bi + ci * br)};
        const float a2 = ar * 1.44269504088896340736f;
        const float rh = ai * 0.15915494309189535f;
        const float rl = __builtin_fmaf(ai, 0.15915494309189535f, -rh) + ai * 6.4206382e-9f;
        auto cexp = [&](float s) __attribute__((always_inline)) -> cf {
            const float e = __builtin_amdgcn_exp2f(a2 * s);
            const float r = __builtin_amdgcn_fractf(rh * s) + rl * s;
            return cf{__builtin_amdgcn_cosf(r), __builtin_amdgcn_sinf(r)} * e;
        };
        const float grow = fmaxf(1.f, __builtin_amdgcn_exp2f(a2 * 2048.f));
        const float sb = wave_sum((fabsf(cb.x) + fabsf(cb.y)) * grow);
        *(LAS float*)(sm + LD_SB + wave * 4) = sb;
        int ex = (int)((__builtin_bit_cast(u32, sb) >> 23) & 255);
        ex = min(max(ex, 30), 230);
        unscale = __builtin_bit_cast(float, (u32)(127 - 15 - 126 + ex) << 23);
        cb *= __builtin_bit_cast(float, (u32)(127 + 15 + 126 - ex) << 23);

        const cf e1 = cexp(1.f), e16 = cexp(16.f), e256 = cexp(256.f);
        const cf e2 = cmul(e1, e1), e32 = cmul(e16, e16), e512 = cmul(e256, e256), x1 = cmulc(cb, e1), x256 = cmulc(cb, e256);
        SB();
        const cf e4 = cmul(e2, e2), e64 = cmul(e32, e32), e1024 = cmul(e512, e512), x2 = cmulc(cb, e2), x3 = cmulc(x1, e2);
        const cf x512 = cmulc(cb, e512), x768 = cmulc(x256, e512), y6 = cmul(e16, e32);
        SB();
        const cf e8 = cmul(e4, e4), e128 = cmul(e64, e64);
        const cf x4 = cmulc(cb, e4), x5 = cmulc(x1, e4), x6 = cmulc(x2, e4), x7 = cmulc(x3, e4), x258 = cmulc(x2, e256);
        const cf x1024 = cmulc(cb, e1024), x1280 = cmulc(x256, e1024), x1536 = cmulc(x512, e1024), x1792 = cmulc(x768, e1024);
        SB();
        auto put = [&](int q, cf v) __attribute__((always_inline)) { *(LAS u32*)(xyw + q * XY_STRIDE) = pkh(v); };
        cf y[8];
        y[0] = cf{1.f, 0.f}; y[1] = e8; y[2] = e16; y[3] = cmul(e8, e16); y[4] = e32; y[5] = cmul(e8, e32); y[6] = y6;
        const cf e192 = cmul(e128, e64), x8 = cmulc(x4, e4), x260 = cmulc(x4, e256), x262 = cmulc(x6, e256), x516 = cmulc(x4, e512);
        put(32, cb); put(33, x1); put(34, x2); put(35, x3); put(36, x4); put(37, x5); put(38, x6); put(39, x7);
        put(40, x256); put(41, x258);
        SB();
        y[7] = cmul(y[3], e32);
        const cf x264 = cmulc(x8, e256), x520 = cmulc(x8, e512), x772 = cmulc(x260, e512), x1032 = cmulc(x8, e1024);
        cf t[8];
#pragma unroll
        for (int q = 1; q < 7; q++) t[q] = cmul(y[q], e64);
        put(42, x260); put(43, x262); put(45, x512); put(46, x516);
#pragma unroll
        for (int q = 0; q < 6; q++) put(q, y[q]);
        SB();
        t[7] = cmul(y[7], e64);
        const cf x776 = cmulc(x264, e512), x1288 = cmulc(x264, e1024), x1544 = cmulc(x520, e1024);
        cf t2[8];
#pragma unroll
        for (int q = 1; q < 8; q++) t2[q] = cmul(y[q], e128);
        put(6, y[6]); put(7, y[7]); put(8, e64);
#pragma unroll
        for (int q = 1; q < 6; q++) put(8 + q, t[q]);
        put(44, x264); put(47, x520); put(48, x768); put(49, x772); put(51, x1024); put(52, x1032);
        SB();

        const cf x1800 = cmulc(x776, e1024);
        cf t3[8];
#pragma unroll
        for (int q = 1; q < 8; q++) t3[q] = cmul(y[q], e192);
        put(14, t[6]); put(15, t[7]); put(16, e128);
#pragma unroll
        for (int q = 1; q < 8; q++) put(16 + q, t2[q]);
        put(50, x776); put(53, x1280); put(54, x1288); put(55, x1536); put(56, x1544); put(57, x1792);
        SB();
        put(24, e192);
#pragma unroll
        for (int q = 1; q < 8; q++) put(24 + q, t3[q]);
        put(58, x1800);
    }
    SB();
    asm volatile("" :: "v"(tin[7]));
    if (w1task >= 0) w1_task_store(rs, w1task, lane, tin);
    SB();
    const int t16 = tid * 16;
    v4u gx[2], gs[2];
    auto gr_load = [&]() __attribute__((always_inline)) {
        gx[0] = ld16(rs, t16, WS_XX + pair * 16384); gx[1] = ld16(rs, t16, WS_XX + pair * 16384 + 8192);
        gs[0] = ld16(rs, t16, WS_ST1); gs[1] = ld16(rs, t16, WS_ST1 + 8192);
    };
    const bool gr_first = (u32)__builtin_amdgcn_s_memrealtime() - tstart >= GR_T;
    if (gr_first) gr_load();
    SB();
    {
        f32x16 kd;
#pragma unroll
        for (int i2 = 0; i2 < 16; i2++) kd[i2] = 0.f;
        v4u fa[8], fb[8];
#pragma unroll
        for (int s = 0; s < 8; s++) { fa[s] = *(LAS v4u*)(pa + s * 32); fb[s] = *(LAS v4u*)(pb + s * 32); }
        SB();
#pragma unroll
        for (int s = 0; s < 8; s++) kd = mfma_f16(fa[s], fb[s], kd);
        SB();

#pragma unroll
        for (int g2 = 0; g2 < 4; g2++)
            *(LAS f32x4*)(exw + g2 * 1024) = f32x4{kd[4 * g2], kd[4 * g2 + 1], kd[4 * g2 + 2], kd[4 * g2 + 3]} * unscale;
    }
    if (w1task >= 0) { wait_vm<0>(); st4(rs, flv, w1_flag(w1task), tag); }
    if (!gr_first) {
        while ((u32)__builtin_amdgcn_s_memrealtime() - tstart < GR_T) __builtin_amdgcn_s_sleep(2);
        gr_load();
    }
    SB();
    BARRIER();
    const int l28 = (col < 28 ? col : 0) * 4;
    SB();
    float ik;
    {
        const f32x4 s0 = *(LAS f32x4*)(sm + LD_SB + ch * 16);
        const f32x4 a0 = *(LAS f32x4*)(exr), a1 = *(LAS f32x4*)(exr + XY_WAVE), a2 = *(LAS f32x4*)(exr + 2 * XY_WAVE), a3 = *(LAS f32x4*)(exr + 3 * XY_WAVE);
        const float b0 = (s0[0] + s0[1]) + (s0[2] + s0[3]);
        int e0 = (int)((__builtin_bit_cast(u32, b0) >> 23) & 255);
        e0 = min(max(e0, 30), 230);
        ik = __builtin_bit_cast(float, (u32)(127 - 15 - 126 + e0) << 23);
        const float sk = __builtin_bit_cast(float, (u32)(127 + 15 + 126 - e0) << 23);
        const f32x4 v = ((a0 + a1) + (a2 + a3)) * sk;
        const u32 p01 = pk_f16(v[0], v[1]), p23 = pk_f16(v[2], v[3]);
        LAS char* o = kpw;
        if (wq == 0) {
            *(LAS u16*)(o) = (u16)p01; *(LAS u16*)(o + 1536) = (u16)(p01 >> 16);
            *(LAS u16*)(o + 2 * 1536) = (u16)p23; *(LAS u16*)(o + 3 * 1536) = (u16)(p23 >> 16);
        } else if (wq == 1) {
            *(LAS u16*)(o) = (u16)p01; *(LAS u16*)(o + 1536) = (u16)p01;
            *(LAS u16*)(o + 2 * 1536) = (u16)(p01 >> 16); *(LAS u16*)(o + 3 * 1536) = (u16)p23;
        } else if (wq == 2) {
            *(LAS u32*)(o) = p01; *(LAS u32*)(o + 1536) = p01;
            *(LAS u32*)(o + 2 * 1536) = (p01 & 0xffffu) | (p23 << 16);
            *(LAS u32*)(o + 3 * 1536) = (p01 >> 16) | (p23 << 16);
        } else {
            const bool ev = (col & 1) == 0;
            *(LAS u32*)(o) = ev ? ((p01 & 0xffffu) | (p01 << 16)) : p01;
            *(LAS u32*)(o + 1536) = p01; *(LAS u32*)(o + 2 * 1536) = p01; *(LAS u32*)(o + 3 * 1536) = p01;
            *(LAS u32*)(o + 128) = ev ? ((p23 & 0xffffu) | (p23 << 16)) : p23;
            *(LAS u32*)(o + 128 + 1536) = p23; *(LAS u32*)(o + 128 + 2 * 1536) = p23; *(LAS u32*)(o + 128 + 3 * 1536) = p23;
        }
    }
    SB();
    auto gr_ok = [&]() __attribute__((always_inline)) {
        const u32 a = (gx[0][1] ^ tag) | (gx[0][3] ^ tag) | (gx[1][1] ^ tag) | (gx[1][3] ^ tag);
        const u32 c = (gs[0][1] ^ tag) | (gs[0][3] ^ tag) | (gs[1][1] ^ tag) | (gs[1][3] ^ tag);
        return __all((a | c) == 0);
    };
    if (!gr_ok()) {
        int spins = SPIN_CAP;
#pragma unroll 1
        do {
            __builtin_amdgcn_s_sleep(4);
            gr_load();
        } while (!gr_ok() && --spins > 0);
    }
    v4u wf[28];
    const int w1off = WS_W1T + (phi * 8 + wave) * 28 * 1024;
    auto w1_load = [&](int ks) __attribute__((always_inline)) {
        wf[ks] = ld16(rs, l16 + ((ks % 7) & 3) * 1024, w1off + (ks / 7) * 7168 + ((ks % 7) >> 2) * 4096);
    };
    const int wf_s = WS_WF1 + phi * 256 + ch * 128;
    const u32 wfl = ld4(rs, l28, wf_s);
    SB();
    u32 xc[4], udk[4];
    {
        float u0[4], u1[4];
#pragma unroll
        for (int i = 0; i < 4; i++) {
            xc[i] = gx[i >> 1][2 * (i & 1)];
            const u32 st = gs[i >> 1][2 * (i & 1)];
            const float m = bflo(st), r = __builtin_amdgcn_rcpf(bfhi(st));
            const u32 dd = pk_bf16(bflo(xc[i]) - m, bfhi(xc[i]) - m);
            const u32 uu = pk_bf16(bflo(dd) * r, bfhi(dd) * r);
            u0[i] = bflo(uu); u1[i] = bfhi(uu);
        }
        *(LAS v2u*)(uw) = v2u{pk_f16(u0[0], u0[1]), pk_f16(u0[2], u0[3])};
        *(LAS v2u*)(uw + 4160) = v2u{pk_f16(u1[0], u1[1]), pk_f16(u1[2], u1[3])};
#pragma unroll
        for (int i = 0; i < 4; i++) udk[i] = pk_bf16(u0[i] * bflo(r_dsk), u1[i] * bfhi(r_dsk));
    }
    BARRIER();
    f32x4 r0, r1, l3;
    f32x2 r2, l2;
    float l1, r3;
    v4u xu;
    u32 ya, yb;
    const int c1 = tid & 255;
    LAS char* q1 = opaque(sm + ch * 4 * P_WAVE + ((c1 >> 3) + 1) * 144 + (c1 & 7) * 16);
    LAS char* q0 = opaque(sm + ch * 4 * P_WAVE + (((c1 - 1) >> 3) + 1) * 144 + ((c1 - 1) & 7) * 16);
    LAS char* xur = opaque(sm + LD_XU + ch * 8192 + c1 * 16);
    auto ce_read = [&](int hf) __attribute__((always_inline)) {
        const int pb_ = hf ? LD_PB : LD_PA;
        r0 = *(LAS f32x4*)(q1 + pb_);
        r1 = *(LAS f32x4*)(q1 + pb_ + P_WAVE); l1 = *(LAS float*)(q0 + pb_ + P_WAVE + 12);
        r2 = *(LAS f32x2*)(q1 + pb_ + 2 * P_WAVE); l2 = *(LAS f32x2*)(q0 + pb_ + 2 * P_WAVE + 8);
        r3 = *(LAS float*)(q1 + pb_ + 3 * P_WAVE); l3 = *(LAS f32x4*)(q0 + pb_ + 3 * P_WAVE);
        xu = *(LAS v4u*)(xur + hf * 4096);
    };
    auto ce_part = [&](int hf, int part) __attribute__((always_inline)) {
        if (part == 0) ya = pk_bf16s(((r0[0] + l2[0]) + (l1 + l3[1])) * ik, ((r0[1] + l2[1]) + (r1[0] + l3[2])) * ik);
        if (part == 1) yb = pk_bf16s(((r0[2] + r2[0]) + (r1[1] + l3[3])) * ik, ((r0[3] + r2[1]) + (r1[2] + r3)) * ik);
        if (part == 2) ya = pk_bf16s(bflo(ya) + bfhi(xu[0]), bfhi(ya) + bfhi(xu[1]));
        if (part == 3) yb = pk_bf16s(bflo(yb) + bfhi(xu[2]), bfhi(yb) + bfhi(xu[3]));
        if (part == 4) ya = pk_bf16s(bflo(ya) + bflo(xu[0]), bfhi(ya) + bflo(xu[1]));
        if (part == 5) {
            yb = pk_bf16s(bflo(yb) + bflo(xu[2]), bfhi(yb) + bflo(xu[3]));
            st8(rs, WS_HT + c1 * 8, (d0 + ch) * (L * 2) + hf * 2048, v2u{ya, yb});
        }
    };
    const int hf_set = WS_HF + (wq * 8 + (((d0 + ch) >> 3) & 7)) * 256 + (((d0 + ch) >> 6) * 8 + ((d0 + ch) & 7)) * 4;
    {
        f32x16 acc0, acc1;
#pragma unroll
        for (int i = 0; i < 16; i++) { acc0[i] = 0.f; acc1[i] = 0.f; }
        constexpr int PD = 3;
        v4u fu[PD + 1], fk[16], fk1[PD + 1];
        auto issue = [&](int n) __attribute__((always_inline)) {
            const int B0 = (n >= 16 && n < 32) ? 2016 - 64 * (n - 16) : 992 - 64 * (n & 15);
            fu[n % (PD + 1)] = lds_tr2(ua + 2 * (B0 - 32) + 32, ua + 2 * (B0 - 32));
            if (n < 16) fk[n] = *(LAS v4u*)(ka + 32 * n);
            if (n >= 32) fk1[n % (PD + 1)] = *(LAS v4u*)(ka + 32 * (n - 16));
        };
#pragma unroll
        for (int n = 0; n < PD; n++) issue(n);
        SB();
        static_for<0, 48>([&](auto N_) __attribute__((always_inline)) {
            constexpr int n = decltype(N_)::v;
            if (n + PD < 48) issue(n + PD);
            if constexpr (n < 16) acc0 = mfma_f16(fu[n % (PD + 1)], fk[n], acc0);
            else if constexpr (n < 32) acc1 = mfma_f16(fu[n % (PD + 1)], fk[n - 16], acc1);
            else acc1 = mfma_f16(fu[n % (PD + 1)], fk1[n % (PD + 1)], acc1);
            if (n == 1) *(LAS v4u*)(sm + LD_XU + t16) = v4u{__builtin_amdgcn_perm(udk[0], xc[0], 0x05040100u), __builtin_amdgcn_perm(udk[1], xc[1], 0x05040100u),
                                                           __builtin_amdgcn_perm(udk[2], xc[2], 0x05040100u), __builtin_amdgcn_perm(udk[3], xc[3], 0x05040100u)};
            if (n == 2) *(LAS v4u*)(sm + LD_XU + 8192 + t16) = v4u{__builtin_amdgcn_perm(udk[0], xc[0], 0x07060302u), __builtin_amdgcn_perm(udk[1], xc[1], 0x07060302u),
                                                                  __builtin_amdgcn_perm(udk[2], xc[2], 0x07060302u), __builtin_amdgcn_perm(udk[3], xc[3], 0x07060302u)};
            if (n == W1_CHK) {
                PIN(acc0); SB();
                wait_flags(rs, l28, wf_s, wfl, tag);
            }
            if constexpr (n == 16 || n == 17) {
                constexpr int g = 2 * (n - 16);
                *(LAS f32x4*)(pw + LD_PA + g * 32) = f32x4{acc0[4 * g], acc0[4 * g + 1], acc0[4 * g + 2], acc0[4 * g + 3]};
                *(LAS f32x4*)(pw + LD_PA + g * 32 + 32) = f32x4{acc0[4 * g + 4], acc0[4 * g + 5], acc0[4 * g + 6], acc0[4 * g + 7]};
            }
            if (n == 17) {
                PIN(acc1);
                if (lane == 63) {
                    *(LAS f32x4*)(sm + LD_PA + wave * P_WAVE + 112) = f32x4{0.f, 0.f, 0.f, 0.f};
                    *(LAS f32x4*)(sm + LD_PB + wave * P_WAVE + 112) = f32x4{acc0[12], acc0[13], acc0[14], acc0[15]};
                }
            }
            if (n == 18) {
                BARRIER();
                __builtin_amdgcn_s_setprio(1);
                st4(rs, (b == 0 && tid == 0) ? WS_EPOCH : OOB, 0, tag);
                ce_read(0);
            }
            if (n >= 21 && n < 27) ce_part(0, n - 21);
            if (n == HF_N) {
                wait_vm<w1_cnt(HF_N) - w1_cnt(26)>();
                st4(rs, flv, hf_set, tag);
                __builtin_amdgcn_s_setprio(0);
            }
            SB();
#pragma unroll
            for (int ks = w1_cnt(n); ks < w1_cnt(n + 1); ks++) w1_load(ks);
            SB();
        });
#pragma unroll
        for (int g = 0; g < 4; g++) *(LAS f32x4*)(pw + LD_PB + g * 32) = f32x4{acc1[4 * g], acc1[4 * g + 1], acc1[4 * g + 2], acc1[4 * g + 3]};
    }
#pragma unroll
    for (int ks = w1_cnt(48); ks < 28; ks++) { w1_load(ks); SB(); }
    const int kap = phi * 8 + wave;
    const u32 w2fl = ld4(rs, l28, WS_WF2 + del * 128);
    v2u csg = __builtin_amdgcn_raw_buffer_load_b64(rs, WS_CS + col * 8, kap * 256, SC1);
    BARRIER();
    ce_read(1);
    auto load_h = [&](int hf, int n) __attribute__((always_inline)) {
        glds16(srd, hvoff + hf * 2048, n * 262144, (u32)(size_t)sm + (hf ? LD_HB : LD_HA) + wave * 1024 + n * 8192);
    };
#pragma unroll
    for (int i = 0; i < 5; i++) ce_part(1, i);
    SB();
    u32 hfl = ld4(rs, hf_v, hf_s);
    SB();
    ce_part(1, 5);
    SB();
    wait_flags(rs, l28, WS_WF2 + del * 128, w2fl, tag);
    if (!__all(csg[1] == tag)) {
        int spins = SPIN_CAP;
#pragma unroll 1
        do {
            __builtin_amdgcn_s_sleep(4);
            csg = __builtin_amdgcn_raw_buffer_load_b64(rs, WS_CS + col * 8, kap * 256, SC1);
        } while (!__all(csg[1] == tag) && --spins > 0);
    }
    *(LAS u32*)(sm + LD_CS + wave * 128 + col * 4) = csg[0];
    wait_flags(rs, hf_v, hf_s, hfl, tag);
#pragma unroll
    for (int n = 0; n < 7; n++) load_h(0, n);
    SB();
    wait_vm<7>();
    st4(rs, flv, hf_set + 8192, tag);
    SB();
    wait_vm<1>();
    BARRIER();
    v4u w2[28];
    const int par = xcd & 1;
    auto kof = [&](int n) __attribute__((always_inline)) { return n < 7 ? n * 2048 + par * 1024 : (n - 7) * 2048 + 1024 - par * 1024; };
    const int w2v = WS_W2T + (del * 2 * 112 + wave * 14) * 1024 + l16;
    auto w2_lds = [&](int f) __attribute__((always_inline)) { return f < 14 ? LD_PB + (wave * 4 + f - 10) * 1024 : LD_HA + (wave * 7 + f - 14) * 1024; };
    auto w2_load = [&](int f) __attribute__((always_inline)) {
        if (f >= 10 && f < 21) glds16(srd, w2v + (f & 1) * 112 * 1024, kof(f >> 1), (u32)(size_t)sm + w2_lds(f));
        else w2[f] = ld16(rs, w2v + (f & 1) * 112 * 1024, kof(f >> 1));
    };
    const int af_v = (lane < 14 ? lane : 0) * 4, af_s = WS_AF + (tau * 32 + wave) * 128;
    const int af_e = ((lane < 7 ? lane : 0) * 2 + par) * 4, af_l = af_e ^ 4;
    const int av = WS_ACT + (tau2 * 112 + wave * 14) * 1024 + l16;
    const int dd = 64 * del + 8 * wave + 4 * h;
    LAS char* hra = sm + LD_HA + dd * 128 + trl + (((xcd & 1) << 6) ^ sw);
    v2u hr[2];
    u32 afl;
    v4u a0[14], a1[14];
    const int af_set = WS_AF + (tau * 32 + kap / 14) * 128 + (kap % 14) * 4;
    const int wp = wave & 1;
    {
        LAS char* at0 = opaque(sm + LD_HA + 8 * h * 128 + trl + sw);
        LAS char* at1 = opaque(sm + LD_HA + 8 * h * 128 + trl + (64 ^ sw));
        LAS char* csr = opaque(sm + LD_CS + wave * 128 + h * 16);
        LAS char* as = opaque(sm + LD_HA + 8 * h * 128 + trl + (((wave & 1) << 6) ^ sw) + (wave >> 1) * 7 * 2048);
        LAS char* str = opaque(sm + LD_ST + col * 32);
        LAS char* cur;
        constexpr int PD = 4;
        v4u fr[PD + 1], sf;
        auto issue = [&](int n) __attribute__((always_inline)) {
            const int tile = n / 28, j = n % 28;
            if (n == 56 && wp) { LAS char* t_ = at0; at0 = at1; at1 = t_; }
            if (j % 7 == 0) cur = opaque(((tile & 1) ? at1 : at0) + ((j / 7) * 14336 + (tile >> 1) * 57344));
            LAS char* a = cur + (j % 7) * 2048;
            fr[n % (PD + 1)] = lds_tr2(a, a + 512);
        };
        f32x16 acc[2];
        float nm, k0, k1, sd2, eg, eu, ez, er, eo0;
        f32x4 cg4, cu4;
        f32x4 sta, stb;
        float s1, s2, s1b, s2b;
        u32 opk[4];
        auto stat_acc = [&](int k, int part) __attribute__((always_inline)) {
            const u32 f0 = sf[2 * part], f1 = sf[2 * part + 1];
            if (k == 0 && part == 0) { s1 = 0.f; s2 = 0.f; s1b = 0.f; s2b = 0.f; }
            s1 = dot2bf(f0, 0x3f803f80u, s1); s2 = dot2bf(f0, f0, s2);
            s1b = dot2bf(f1, 0x3f803f80u, s1b); s2b = dot2bf(f1, f1, s2b);
        };
        auto stat_write = [&](int hf) __attribute__((always_inline)) {
            const float a = half_sum(s1 + s1b), c = half_sum(s2 + s2b);
            *(LAS f32x2*)(str + (hf * 2 + (wave & 1)) * 1024 + (wave >> 1) * 8) = f32x2{a, c};
        };
        auto epi_read = [&](int tile) __attribute__((always_inline)) { sta = *(LAS f32x4*)(str + tile * 1024); stb = *(LAS f32x4*)(str + tile * 1024 + 16); };
        auto epi_stats = [&]() __attribute__((always_inline)) {
            float m = ((sta[0] + sta[2]) + (stb[0] + stb[2])) * (1.f / D), e2 = ((sta[1] + sta[3]) + (stb[1] + stb[3])) * (1.f / D);
            asm("" : "+v"(m)); asm("" : "+v"(e2));
            const float var = rbf(fmaxf(e2 - m * m, 0.f));
            const float sd = rbf(__builtin_amdgcn_sqrtf(rbf(var + 1e-5f))), r = __builtin_amdgcn_rcpf(sd);
            nm = -rbf(m); sd2 = sd * sd; k0 = r * -2.3022082f; k1 = (r * r) * (r * -0.10294324f);
        };
        v4u sg;
        auto epi_stats2 = [&](u32 st) __attribute__((always_inline)) {
            const float sd = bfhi(st), r = __builtin_amdgcn_rcpf(sd);
            nm = -bflo(st); sd2 = sd * sd; k0 = r * -2.3022082f; k1 = (r * r) * (r * -0.10294324f);
        };
        auto cs_read = [&](int g) __attribute__((always_inline)) { cg4 = *(LAS f32x4*)(csr + g * 32); cu4 = *(LAS f32x4*)(csr + 64 + g * 32); };
        auto slice = [&](const f32x16& a, int sidx) __attribute__((always_inline)) {
            const int e = sidx / 3, st = sidx % 3;
            if (st == 0) {
                if (e > 0) { float o = eu * er; asm("" : "+v"(o)); if ((e - 1) & 1) opk[(e - 1) >> 1] = pk_bf16(eo0, o); else eo0 = o; }
                if (e < 8) {
                    eg = __builtin_fmaf(nm, cg4[e & 3], a[e & 7]); eu = __builtin_fmaf(nm, cu4[e & 3], a[8 + (e & 7)]);
                    asm("" : "+v"(eg), "+v"(eu));
                }
            } else if (st == 1) { ez = __builtin_amdgcn_exp2f(eg * __builtin_fmaf(eg * eg, k1, k0)); asm("" : "+v"(ez)); }
            else { er = __builtin_amdgcn_rcpf(__builtin_fmaf(ez, sd2, sd2)); eu *= eg; asm("" : "+v"(er), "+v"(eu)); }
        };
        auto epi_four = [&](const f32x16& a, int g) __attribute__((always_inline)) {
            const f32x4 nm4 = {nm, nm, nm, nm}, sd4 = {sd2, sd2, sd2, sd2};
            const f32x4 gg = __builtin_elementwise_fma(nm4, cg4, f32x4{a[4 * g], a[4 * g + 1], a[4 * g + 2], a[4 * g + 3]});
            const f32x4 u = __builtin_elementwise_fma(nm4, cu4, f32x4{a[8 + 4 * g], a[9 + 4 * g], a[10 + 4 * g], a[11 + 4 * g]});
            const f32x4 z = gg * __builtin_elementwise_fma(gg * gg, f32x4{k1, k1, k1, k1}, f32x4{k0, k0, k0, k0});
            const f32x4 e = {__builtin_amdgcn_exp2f(z.x), __builtin_amdgcn_exp2f(z.y), __builtin_amdgcn_exp2f(z.z), __builtin_amdgcn_exp2f(z.w)};
            const f32x4 dn = __builtin_elementwise_fma(e, sd4, sd4);
            const f32x4 o = (gg * u) * f32x4{__builtin_amdgcn_rcpf(dn.x), __builtin_amdgcn_rcpf(dn.y), __builtin_amdgcn_rcpf(dn.z), __builtin_amdgcn_rcpf(dn.w)};
            opk[2 * g] = pk_bf16(o.x, o.y); opk[2 * g + 1] = pk_bf16(o.z, o.w);
        };
        auto epi_store = [&](int tile) __attribute__((always_inline)) {
            const int tok32 = (tile >> 1) * 32 + tau * 2 + (tile >= 2 ? (tile & 1) ^ wp : (tile & 1));
            st16(rs, WS_ACT + l16 + (tok32 * 112 + kap) * 1024, v4u{opk[0], opk[1], opk[2], opk[3]});
        };
#pragma unroll
        for (int n = 0; n < PD; n++) issue(n);
        SB();
        static_for<0, 112>([&](auto N_) __attribute__((always_inline)) {
            constexpr int n = decltype(N_)::v;
            constexpr int tile = n / 28, j = n % 28;
            if (n == 52) {
                wait_vm<g1_cum(52) - g1_cum(HB_N + 9)>();
                BARRIER();
                __builtin_amdgcn_s_setprio(1);
            }
            if (n + PD < 112) issue(n + PD);
            if (j == 0) {
                f32x16 z;
#pragma unroll
                for (int i = 0; i < 16; i++) z[i] = 0.f;
                acc[tile & 1] = mfma_bf16(wf[0], fr[n % (PD + 1)], z);
            } else {
                acc[tile & 1] = mfma_bf16(wf[j], fr[n % (PD + 1)], acc[tile & 1]);
            }
            SB();
            if constexpr (n < 28) {
                constexpr int sk = n;
                if (sk < 21 && sk % 3 == 0) { if (sk > 0) { stat_acc(sk / 3 - 1, 1); SB(); } sf = lds_tr2(as + (sk / 3) * 2048, as + (sk / 3) * 2048 + 512); }
                if (sk < 21 && sk % 3 == 2) stat_acc(sk / 3, 0);
                if (sk == 21) stat_acc(6, 1);
                if (sk == 22) stat_write(0);
            }
            if (n == HBP_N) hfl = ld4(rs, hf_v, hf_s + 8192);
            if (n == HB_N) {
                PIN(acc[0]);
                SB();
                wait_flags(rs, hf_v, hf_s + 8192, hfl, tag);
            }
            if (n == 48) hr[0] = lds_tr(hra);
            if (n == 27) BARRIER_RAW();
            if (n == 27 || n == 55) epi_read(tile);
            if (n == 84) {
                PIN(acc[0]); PIN(acc[1]);
                SB();
                if (!__all(sg[1] == tag && sg[3] == tag)) {
                    int spins = SPIN_CAP;
#pragma unroll 1
                    do {
                        __builtin_amdgcn_s_sleep(2);
                        sg = ld16(rs, WS_SG + col * 16, tau * 512);
                    } while (!__all(sg[1] == tag && sg[3] == tag) && --spins > 0);
                }
            }
            if (tile == 1) {
                if (j == 0) cs_read(0);
                if (j == 1) epi_stats();
                if (j >= 2 && j < 27) slice(acc[0], j - 2);
                if (j == 11) { SB(); cs_read(1); }
            }
            if (tile == 2) {
                if (j == 0) cs_read(0);
                if (j == 1) epi_stats();
                if (j >= 2 && j < 10) { slice(acc[1], 3 * (j - 2)); if (j == 5) { SB(); cs_read(1); } slice(acc[1], 3 * (j - 2) + 1); slice(acc[1], 3 * (j - 2) + 2); }
                if (j == 10) slice(acc[1], 24);
            }
            if (tile == 3) {
                if (j == 0) cs_read(0);
                if (j == 1) epi_stats2(wp ? sg[2] : sg[0]);
                if (j >= 2 && j < 14) { slice(acc[0], 2 * (j - 2)); if (j == 6) { slice(acc[0], 9); SB(); cs_read(1); } else slice(acc[0], 2 * (j - 2) + 1); }
                if (j == 14) slice(acc[0], 24);
            }
            if (n == AFC_N) {
                PIN(acc[1]);
                SB();
                wait_flags(rs, af_v, af_s, afl, tag);
            }
            constexpr int op = g1_op(n);
            if constexpr (op >= 60) a0[op - 60] = ld16(rs, av, kof(op - 60));
            if constexpr (op >= 0 && op < 28) w2_load(op);
            if constexpr (op >= 32 && op < 40) load_h(1, op - 32);
            if constexpr (op >= 40 && op < 50) epi_store(op - 40);
            if constexpr (op == 50) { wait_vm<g1_cum(AF_N) - g1_cum(ST1_N + 1)>(); st4(rs, flv, af_set, tag); __builtin_amdgcn_s_setprio(0); BARRIER_RAW(); }
            if constexpr (op == 51) afl = ld4(rs, af_v, af_s);
            if constexpr (op == 52) st4(rs, (tid == 0 && del == 0) ? 0 : OOB, WS_TR + ((xcd & 1) * 16 + tau) * 128, tag);
            if constexpr (op == 53) sg = ld16(rs, WS_SG + col * 16, tau * 512);
            SB();
        });
        PIN(acc[1]);
        cs_read(0);
        epi_stats2(wp ? sg[0] : sg[2]);
        epi_four(acc[1], 0);
        cs_read(1);
        epi_four(acc[1], 1);
        SB();
        wait_vm<g1_cum(112) - g1_cum(ST2N + 1)>();
        st4(rs, flv, af_set + 1024, tag);
        SB();
        epi_store(3);
    }
    SB();
#pragma unroll
    for (int n = A0_IN; n < 14; n++) { a0[n] = ld16(rs, av, kof(n)); SB(); }
    hr[1] = lds_tr(hra + 57344);
#pragma unroll
    for (int f = 10; f < 21; f++) w2[f] = *(LAS v4u*)(sm + w2_lds(f) + l16);
    BARRIER();
    f32x16 acc[2][2];
#pragma unroll
    for (int i = 0; i < 16; i++) { acc[0][0][i] = 0.f; acc[0][1][i] = 0.f; acc[1][0][i] = 0.f; acc[1][1][i] = 0.f; }
    LAS char* rw = opaque(sm + wave * 1024 + l16);
    LAS char* rr = opaque(sm + wave * 8192 + l16);
    auto red_write = [&](int hf) __attribute__((always_inline)) {
#pragma unroll
        for (int o = 0; o < 8; o++) {
            const f32x16& t = acc[o >> 2][hf];
            const int r = 4 * (o & 3);
            *(LAS f32x4*)(rw + hf * 65536 + o * 8192) = f32x4{t[r], t[r + 1], t[r + 2], t[r + 3]};
        }
    };
    auto red_finish = [&](int hf) __attribute__((always_inline)) {
        f32x4 v[8];
#pragma unroll
        for (int s = 0; s < 8; s++) v[s] = *(LAS f32x4*)(rr + hf * 65536 + s * 1024);
        const f32x4 t = ((v[0] + v[1]) + (v[2] + v[3])) + ((v[4] + v[5]) + (v[6] + v[7]));
        const u32 ta = pk_bf16(t[0], t[1]), tb = pk_bf16(t[2], t[3]);
        const int tok = 1024 * hf + 32 * tau2 + col;
        *(v2u*)(p.out + (size_t)tok * D + dd) = v2u{pk_bf16(bflo(ta) + bflo(hr[hf][0]), bfhi(ta) + bfhi(hr[hf][0])),
                                                    pk_bf16(bflo(tb) + bflo(hr[hf][1]), bfhi(tb) + bfhi(hr[hf][1]))};
    };
#pragma unroll
    for (int n = 0; n < 14; n++) {
        acc[0][0] = mfma_bf16(w2[2 * n], a0[n], acc[0][0]);
        acc[1][0] = mfma_bf16(w2[2 * n + 1], a0[n], acc[1][0]);
        if (n == AFB_N) { wait_vm<14 - A0_IN>(); st4(rs, flv, af_set + 2048, tag); }
        if (n == B1P) afl = ld4(rs, af_e, af_s + 1024);
        SB();
    }
    PIN(acc[0][0]); PIN(acc[1][0]);
    auto mfma_b = [&](int n0, int n1) __attribute__((always_inline)) {
#pragma unroll
        for (int n = n0; n < n1; n++) {
            acc[0][1] = mfma_bf16(w2[2 * n], a1[n], acc[0][1]);
            acc[1][1] = mfma_bf16(w2[2 * n + 1], a1[n], acc[1][1]);
            SB();
        }
    };
    auto load_a1 = [&](int n0, int n1) __attribute__((always_inline)) {
#pragma unroll
        for (int n = n0; n < n1; n++) { a1[n] = ld16(rs, av + 32 * 112 * 1024, kof(n)); SB(); }
    };
    if (!(TV & 1)) { red_write(0); SB(); }
    wait_flags(rs, af_e, af_s + 1024, afl, tag);
    load_a1(0, 7);
    if (TV & 1) { red_write(0); SB(); }
    if (TV & 4) {
        afl = ld4(rs, af_l, af_s + 2048);
        SB();
        mfma_b(0, 7);
        PIN(acc[0][1]); PIN(acc[1][1]);
        wait_flags(rs, af_l, af_s + 2048, afl, tag);
        load_a1(7, 14);
        BARRIER();
        red_finish(0);
        SB();
        mfma_b(7, 14);
    } else {
        BARRIER();
        afl = ld4(rs, af_l, af_s + 2048);
        SB();
        if (TV & 2) {
            mfma_b(0, 7);
            PIN(acc[0][1]); PIN(acc[1][1]);
            wait_flags(rs, af_l, af_s + 2048, afl, tag);
            load_a1(7, 14);
            red_finish(0);
            SB();
            mfma_b(7, 14);
        } else {
            red_finish(0);
            SB();
            wait_flags(rs, af_l, af_s + 2048, afl, tag);
            load_a1(7, 14);
            mfma_b(0, 14);
        }
    }
    red_write(1);
    BARRIER();
    red_finish(1);
    asm volatile(".Lfb_code_end:");
}

}

extern "C" void kernel_launch(void* const* d_in, const int* in_sizes, int n_in,
                              void* d_out, int out_size, void* d_ws, size_t ws_size,
                              hipStream_t stream) {
    Params p;
    p.x = (const u16*)d_in[0]; p.Are = (const u16*)d_in[1]; p.Aim = (const u16*)d_in[2];
    p.Bre = (const u16*)d_in[3]; p.Bim = (const u16*)d_in[4]; p.Cre = (const u16*)d_in[5]; p.Cim = (const u16*)d_in[6];
    p.logdt = (const u16*)d_in[7]; p.Dskip = (const u16*)d_in[8];
    p.wg = (const u16*)d_in[10]; p.wu = (const u16*)d_in[11]; p.wd = (const u16*)d_in[12];
    p.ws = (char*)d_ws; p.out = (u16*)d_out;
    (void)hipFuncSetAttribute((const void*)fused_block, hipFuncAttributeMaxDynamicSharedMemorySize, LDS_BYTES);
    fused_block<<<256, 512, LDS_BYTES, stream>>>(p);
}
```

```cpp
#include <hip/hip_runtime.h>
#include <stdint.h>

namespace {

constexpr int L = 2048, D = 448, N = 256, F = 1792;

typedef unsigned short u16;
typedef unsigned int u32;
typedef unsigned long long u64;
typedef u32 v4u __attribute__((ext_vector_type(4)));
typedef u32 v2u __attribute__((ext_vector_type(2)));
typedef float f32x2 __attribute__((ext_vector_type(2)));
typedef float f32x4 __attribute__((ext_vector_type(4)));
typedef float f32x16 __attribute__((ext_vector_type(16)));
typedef _Float16 f16x2 __attribute__((ext_vector_type(2)));
typedef _Float16 f16x8 __attribute__((ext_vector_type(8)));
typedef __bf16 bf16x2 __attribute__((ext_vector_type(2)));
typedef __bf16 bf16x8 __attribute__((ext_vector_type(8)));
typedef short s16x4 __attribute__((ext_vector_type(4)));
#define LAS __attribute__((address_space(3)))
#define SB() __builtin_amdgcn_sched_barrier(0)
#define PIN(x) asm volatile("" : "+v"(x))
#define BARRIER() asm volatile("s_waitcnt lgkmcnt(0)\n\ts_barrier" ::: "memory")
#define BARRIER_RAW() asm volatile("s_barrier" ::: "memory")
constexpr int SC1 = 16;


constexpr int WS_EPOCH = 0;
constexpr int WS_WF2 = 1024;
constexpr int WS_TR = 4096;
constexpr int WS_HF = 8192;
constexpr int WS_AF = WS_HF + 64 * 256;
constexpr int WS_SG = 57344;
constexpr int WS_WF1 = 65536;
constexpr int WS_CS = 65536 + 8192;
constexpr int WS_ST1 = WS_CS + 32768;
constexpr int WS_XX = WS_ST1 + 16384;
constexpr int WS_W1T = WS_XX + 224 * 16384;
constexpr int WS_W2T = WS_W1T + 112 * 28 * 1024;
constexpr int WS_HT = WS_W2T + 14 * 112 * 1024;
constexpr int WS_ACT = WS_HT + D * L * 2;
constexpr int WS_END = WS_ACT + 64 * 112 * 1024;
constexpr int SPIN_CAP = 1 << 16;
static_assert(WS_AF + 16 * 2 * 8 * 128 <= WS_SG, "flags overlap");

__device__ __forceinline__ float bflo(u32 v) { return __builtin_bit_cast(float, v << 16); }
__device__ __forceinline__ float bfhi(u32 v) { return __builtin_bit_cast(float, v & 0xffff0000u); }
__device__ __forceinline__ u32 pk_bf16(float a, float b) { f32x2 f = {a, b}; return __builtin_bit_cast(u32, __builtin_convertvector(f, bf16x2)); }
__device__ __forceinline__ u32 pk_bf16s(float a, float b) { asm("" : "+v"(a)); asm("" : "+v"(b)); return pk_bf16(a, b); }
__device__ __forceinline__ u32 pk_f16(float a, float b) { f32x2 f = {a, b}; return __builtin_bit_cast(u32, __builtin_convertvector(f, f16x2)); }
__device__ __forceinline__ float rbf(float a) { return bflo(pk_bf16(a, 0.f)); }
__device__ __forceinline__ float dot2bf(u32 a, u32 b, float c) {
    return __builtin_amdgcn_fdot2_f32_bf16(__builtin_bit_cast(bf16x2, a), __builtin_bit_cast(bf16x2, b), c, false);
}
__device__ __forceinline__ f32x16 mfma_bf16(v4u a, v4u b, f32x16 c) {
    return __builtin_amdgcn_mfma_f32_32x32x16_bf16(__builtin_bit_cast(bf16x8, a), __builtin_bit_cast(bf16x8, b), c, 0, 0, 0);
}
__device__ __forceinline__ f32x16 mfma_f16(v4u a, v4u b, f32x16 c) {
    return __builtin_amdgcn_mfma_f32_32x32x16_f16(__builtin_bit_cast(f16x8, a), __builtin_bit_cast(f16x8, b), c, 0, 0, 0);
}
__device__ __forceinline__ v2u lds_tr(LAS char* p) {
    return __builtin_bit_cast(v2u, __builtin_amdgcn_ds_read_tr16_b64_v4i16((LAS s16x4*)p));
}
__device__ __forceinline__ v4u lds_tr2(LAS char* p0, LAS char* p1) {
    const v2u lo = lds_tr(p0), hi = lds_tr(p1);
    return v4u{lo[0], lo[1], hi[0], hi[1]};
}
template <int CTRL> __device__ __forceinline__ float dpp(float v) {
    return __builtin_bit_cast(float, __builtin_amdgcn_update_dpp(0, __builtin_bit_cast(int, v), CTRL, 0xf, 0xf, true));
}
__device__ __forceinline__ float rdl(float v, int l) { return __builtin_bit_cast(float, __builtin_amdgcn_readlane(__builtin_bit_cast(int, v), l)); }
__device__ __forceinline__ float wave_sum(float v) {
    v += dpp<0xB1>(v); v += dpp<0x4E>(v); v += dpp<0x141>(v); v += dpp<0x140>(v);
    return (rdl(v, 0) + rdl(v, 16)) + (rdl(v, 32) + rdl(v, 48));
}
__device__ __forceinline__ float half_sum(float v) {
    const auto r = __builtin_amdgcn_permlane32_swap(__builtin_bit_cast(u32, v), __builtin_bit_cast(u32, v), false, false);
    return __builtin_bit_cast(float, (u32)r[0]) + __builtin_bit_cast(float, (u32)r[1]);
}
template <int I> struct IC { static constexpr int v = I; };
template <int B, int E, class Fn> __device__ __forceinline__ void static_for(Fn&& f) {
    if constexpr (B < E) { f(IC<B>{}); static_for<B + 1, E>(f); }
}

typedef f32x2 cf;
__device__ __forceinline__ cf rot(cf b) { return cf{-b.y, b.x}; }
__device__ __forceinline__ cf cmul(cf a, cf b) { return __builtin_elementwise_fma(a.yy, rot(b), a.xx * b); }
__device__ __forceinline__ cf cmulc(cf a, cf b) { return __builtin_elementwise_fma(a.yy, b.yx, a.xx * rot(b).yx); }
__device__ __forceinline__ u32 pkh(cf a) { return __builtin_bit_cast(u32, __builtin_convertvector(a, f16x2)); }

typedef __amdgpu_buffer_rsrc_t rsrc_t;
__device__ __forceinline__ v4u ld16(rsrc_t r, int voff, int soff) { return __builtin_amdgcn_raw_buffer_load_b128(r, voff, soff, SC1); }
__device__ __forceinline__ u32 ld4(rsrc_t r, int voff, int soff) { return __builtin_amdgcn_raw_buffer_load_b32(r, voff, soff, SC1); }
__device__ __forceinline__ void st16(rsrc_t r, int voff, v4u v) { __builtin_amdgcn_raw_buffer_store_b128(v, r, voff, 0, SC1); }
__device__ __forceinline__ void st8(rsrc_t r, int voff, int soff, v2u v) { __builtin_amdgcn_raw_buffer_store_b64(v, r, voff, soff, SC1); }
__device__ __forceinline__ void st4(rsrc_t r, int voff, int soff, u32 v) { __builtin_amdgcn_raw_buffer_store_b32(v, r, voff, soff, SC1); }
template <int N> __device__ __forceinline__ void wait_vm() { asm volatile("s_waitcnt vmcnt(%0)" :: "i"(N) : "memory"); }
constexpr int OOB = 0x7f000000;

__device__ __forceinline__ void wait_flags(rsrc_t rs, int voff, int soff, u32 v, u32 tag) {
    if (!__all(v == tag)) {
        int spins = SPIN_CAP;
#pragma unroll 1
        do {
            __builtin_amdgcn_s_sleep(1);
            v = ld4(rs, voff, soff);
        } while (!__all(v == tag) && --spins > 0);
    }
    __builtin_amdgcn_fence(__ATOMIC_ACQUIRE, "wavefront");
}
__device__ __forceinline__ void glds16(v4u srd, int voff, int soff, u32 lds_dst) {
    asm volatile("s_mov_b32 m0, %3\n\ts_nop 0\n\tbuffer_load_dwordx4 %0, %1, %2 offen sc1 lds"
                 :: "v"(voff), "s"(srd), "s"(soff), "s"(lds_dst) : "memory");
}
__device__ __forceinline__ LAS char* opaque(LAS char* q) { u32 a = (u32)(size_t)q; asm volatile("" : "+v"(a)); return (LAS char*)(size_t)a; }

struct Params {
    const u16 *x, *Are, *Aim, *Bre, *Bim, *Cre, *Cim, *logdt, *Dskip, *wg, *wu, *wd;
    char* ws;
    u16* out;
};

constexpr int XY_STRIDE = 272, XY_ROWS = 59, XY_WAVE = XY_STRIDE * XY_ROWS;
constexpr int P_WAVE = 33 * 144;
constexpr int LD_PB = 0, LD_PA = 38144;
constexpr int LD_HA = 38144, LD_HB = LD_HA + 57344;
constexpr int LD_XU = LD_HB;
constexpr int LD_KP = 128512;
constexpr int LD_U = LD_KP + 8 * 1536;
constexpr int LD_SB = LD_HB + 57344;
constexpr int LD_ST = LD_SB + 64;
constexpr int LD_CS = LD_ST + 4096;
constexpr int LDS_BYTES = LD_CS + 1024;
static_assert(LD_U + 2 * 4160 <= LD_SB && LDS_BYTES <= 163840, "lds map");

__device__ __forceinline__ v4u tr_piece(const v4u (&in)[8], int c) {
    v4u o;
#pragma unroll
    for (int kp = 0; kp < 4; kp++)
        o[kp] = __builtin_amdgcn_perm(in[2 * kp + 1][c >> 1], in[2 * kp][c >> 1], (c & 1) ? 0x07060302u : 0x05040100u);
    return o;
}
__device__ __forceinline__ void w1_task_load(const Params& p, int task, int lane, v2u (&in)[8]) {
    const int t = (task < 0 ? 0 : task) >> 1, c4 = task & 1;
    const int phi = t / 28, r = t - phi * 28, mat = r >= 14, kp = r - 14 * mat;
    const rsrc_t rw = __builtin_amdgcn_make_buffer_rsrc((void*)(mat ? p.wu : p.wg), 0, task < 0 ? 0 : D * F * 2, 0x00020000);
    const int voff = (8 * (lane >> 4) * F + (lane & 15) * 8) * 2;
#pragma unroll
    for (int k = 0; k < 8; k++) in[k] = __builtin_amdgcn_raw_buffer_load_b64(rw, voff + (k & 1) * F * 2, ((32 * kp + (k & 6)) * F + phi * 128 + 4 * c4) * 2, 0);
}
__device__ __forceinline__ void w1_task_store(rsrc_t rs, int task, int lane, const v2u (&in)[8]) {
    const int t = task >> 1, c4 = task & 1;
    const int phi = t / 28, r = t - phi * 28, mat = r >= 14, kp = r - 14 * mat;
    const int voff = WS_W1T + ((phi * 8 + 4 * c4) * 28 + 2 * kp) * 1024 + mat * 256 + (lane >> 4) * 512 + (lane & 15) * 16;
#pragma unroll
    for (int c = 0; c < 4; c++) {
        v4u o;
#pragma unroll
        for (int kp2 = 0; kp2 < 4; kp2++) o[kp2] = __builtin_amdgcn_perm(in[2 * kp2 + 1][c >> 1], in[2 * kp2][c >> 1], (c & 1) ? 0x07060302u : 0x05040100u);
        st16(rs, voff + c * 28 * 1024, o);
    }
}
__device__ __forceinline__ int w1_flag(int task) { const int t = task >> 1, phi = t / 28; return WS_WF1 + (phi * 2 + (task & 1)) * 128 + (t - phi * 28) * 4; }
__device__ __forceinline__ void w2_task(const Params& p, rsrc_t rs, int task, int lane, LAS char* scr) {
    const int del = task / 28, kg = task - del * 28;
    const int k4 = (lane >> 3) & 3, ks = kg * 4 + k4, h = lane >> 5, dr = lane & 7;
    const u16* src = p.wd + (size_t)((ks >> 3) * 128 + 32 * h + (ks & 7)) * D + del * 64 + dr * 8;
    v4u in[8];
#pragma unroll
    for (int j = 0; j < 8; j++) in[j] = __builtin_nontemporal_load((const v4u*)(src + (size_t)(8 * ((j & 3) + 8 * (j >> 2))) * D));
    LAS char* w = scr + ((dr >> 2) * 4 + k4) * 1024 + (32 * h + 8 * (dr & 3)) * 16;
#pragma unroll
    for (int c = 0; c < 8; c++) *(LAS v4u*)(w + c * 16) = tr_piece(in, c);
    const int voff = WS_W2T + (del * 2 * 112 + kg * 4) * 1024 + lane * 16;
#pragma unroll
    for (int f = 0; f < 8; f++) st16(rs, voff + ((f >> 2) * 112 + (f & 3)) * 1024, *(LAS v4u*)(scr + f * 1024 + lane * 16));
}

constexpr u32 GR_T = 320, CS_T = 520;
constexpr int W1_CHK = 2;
constexpr int w1_cnt(int n) { return n <= W1_CHK ? 0 : 4 + ((n - W1_CHK - 1) * 2) / 5; }
constexpr int HF_N = 37;
constexpr int HBP_N = 14, HB_N = 26;
constexpr int ST1_N = 67, AF_N = 83;
constexpr int SG_N = 77;
constexpr int AFP_N = 94, AFC_N = 103, A0_IN = 7;
constexpr int AFB_N = 4;
constexpr int g1_op(int n) {
    return n == 27 ? 52 : (n > HB_N && n <= HB_N + 8) ? 32 + (n - HB_N - 1) - (n > 27)
         : n == 55 ? 40 : n == ST1_N ? 41 : n == 111 ? 42 : n == AF_N ? 50 : n == AFP_N ? 51 : n == SG_N ? 53
         : (n >= 1 && n < 21 && (n & 1) == 1) ? (n - 1) / 2
         : (n >= 36 && n < 52 && (n & 3) == 0) ? 10 + (n - 36) / 4
         : (n == 54 || (n >= 68 && n < 80 && (n & 1) == 0)) ? (n == 54 ? 14 : 15 + (n - 68) / 2)
         : (n >= 85 && n < 99 && (n & 1) == 1) ? 21 + (n - 85) / 2
         : (n > AFC_N && n <= AFC_N + A0_IN) ? 60 + (n - AFC_N - 1)
         : -1;
}
constexpr bool g1_all() { int m = 0; for (int i = 0; i < 112; i++) if (g1_op(i) >= 0 && g1_op(i) < 28) m |= 1 << g1_op(i); return m == (1 << 28) - 1; }
constexpr int g1_cum(int n) { int c = 0; for (int i = 0; i < n; i++) c += g1_op(i) >= 0; return c; }
static_assert(g1_all() && HB_N >= 23 && HB_N < 27 && AF_N > ST1_N && AFP_N > AF_N && AFC_N > AFP_N && AFC_N >= 99 && AFC_N + A0_IN < 111 && w1_cnt(48) <= 28, "schedule");

__global__ __launch_bounds__(512, 2) void fused_block(Params p) {
    extern __shared__ __attribute__((aligned(16))) char smem_[];
    LAS char* sm = (LAS char*)smem_;
    const int tid = threadIdx.x, lane = tid & 63;
    const int wave = __builtin_amdgcn_readfirstlane(tid >> 6);
    const int b = blockIdx.x;
    const bool main_blk = b < 224;
    const int xcd = b & 7, i28 = b >> 3;
    u64 pc0; u32 clen;
    asm volatile("s_getpc_b64 %0\n.Lfb_code_begin:\n\ts_mov_b32 %1, .Lfb_code_end-.Lfb_code_begin" : "=s"(pc0), "=s"(clen));
    const u32 cpf = __builtin_amdgcn_raw_buffer_load_b32(__builtin_amdgcn_make_buffer_rsrc((void*)pc0, 0, main_blk ? 0 : clen, 0x00020000),
                                                         lane < 16 ? lane * 64 : OOB, (wave * 4 + (i28 & 3)) * 1024, 0);
    const int l16 = lane * 16;
    const int rbase = 16 * (b >> 1) + 2 * (b & 1);
    int x2v = lane < 56 ? (lane & 3) * (4 * D * 2) + (lane >> 2) * 8 : OOB, xrv = lane < 56 ? l16 : OOB, t2 = tid * 2;
    int x2s = (rbase * D + 56 * wave) * 2, xrs = (rbase + 4 * (wave >> 1) + (wave & 1)) * D * 2;
    int pair = xcd * 28 + i28;
    asm volatile("" : "+v"(x2v), "+v"(xrv), "+v"(t2), "+s"(x2s), "+s"(xrs), "+s"(pair));
    SB();
    const rsrc_t rs = __builtin_amdgcn_make_buffer_rsrc(p.ws, 0, WS_END, 0x00020000);
    asm volatile("" :: "s"(p.x), "s"(p.wd), "s"(p.ws));
    const u32 tstart = (u32)__builtin_amdgcn_s_memrealtime();
    const u32 tagv = ld4(rs, WS_EPOCH, 0);

    const rsrc_t rx = __builtin_amdgcn_make_buffer_rsrc((void*)p.x, 0, L * D * 2, 0x00020000);
    const v2u xa = __builtin_amdgcn_raw_buffer_load_b64(rx, x2v, x2s, 2);
    const v2u xb = __builtin_amdgcn_raw_buffer_load_b64(rx, x2v + D * 2, x2s, 2);
    const v4u xr = __builtin_amdgcn_raw_buffer_load_b128(rx, xrv, xrs, 2);
    SB();
    const int ch = wave >> 2, wq = wave & 3;
    const int d0 = 2 * pair;
    const int psz = main_blk ? D * N * 2 : 0;
    auto ldp = [&](const u16* q) __attribute__((always_inline)) -> u32 {
        return __builtin_amdgcn_raw_buffer_load_b16(__builtin_amdgcn_make_buffer_rsrc((void*)q, 0, psz, 0x00020000), t2, d0 * N * 2, 2);
    };
    const u32 r_ldt = __builtin_amdgcn_raw_buffer_load_b32(__builtin_amdgcn_make_buffer_rsrc((void*)p.logdt, 0, main_blk ? D * 2 : 0, 0x00020000), 0, d0 * 2, 0);
    const u32 r_are = ldp(p.Are), r_aim = ldp(p.Aim), r_bre = ldp(p.Bre), r_bim = ldp(p.Bim), r_cre = ldp(p.Cre), r_cim = ldp(p.Cim);
    const u32 r_dsk = __builtin_amdgcn_raw_buffer_load_b32(__builtin_amdgcn_make_buffer_rsrc((void*)p.Dskip, 0, main_blk ? D * 2 : 0, 0x00020000), 0, d0 * 2, 0);
    const int hw = (b - 224) + 32 * wave;
    const int w1task = !main_blk ? -1 : wave < 2 ? pair * 2 + wave : (wave < 4 && pair < 168) ? 448 + pair * 2 + wave - 2 : -1;
    SB();
    const int h = lane >> 5, col = lane & 31;
    const int G1 = (lane >> 4) & 1, q4 = (lane & 15) >> 2, pp = lane & 3;
    int xxv = lane < 56 ? WS_XX + ((28 * wave + 2 * (lane >> 2)) * 2 + (b & 1)) * 8192 + (4 * (b >> 1) + (lane & 3)) * 16 : OOB;
    int flv = lane == 0 ? 0 : OOB;
    asm volatile("" : "+v"(xxv), "+v"(flv));
    LAS char* xy = sm + wave * XY_WAVE;
    LAS char* xyw = opaque(xy + lane * 4);
    LAS char* pa = opaque(xy + (32 + (int)((0x1713100d0a080400ull >> (8 * (col >> 2))) & 255) + (col & 3)) * XY_STRIDE + h * 16);
    LAS char* pb = opaque(xy + col * XY_STRIDE + h * 16);
    LAS char* exw = opaque(xy + l16);
    LAS char* exr = opaque(sm + (ch * 4) * XY_WAVE + wq * 1024 + l16);
    LAS char* kpw = opaque(sm + LD_KP + ch * 4 * 1536 + 512 + (wq < 2 ? 64 * wq + 2 * col + h : wq == 2 ? 64 * (2 + h) + 2 * col : 64 * (4 + 2 * h) + 2 * col) * 2);
    LAS char* uw = opaque(sm + LD_U + 64 + tid * 8);
    LAS char* ua = opaque(sm + LD_U + ch * 4160 + 2 * (32 + 32 + 16 * G1 - 32 * h - 16 - 4 * q4 + 4 * pp));
    LAS char* ka = opaque(sm + LD_KP + wave * 1536 + 2 * (256 - 248 + 8 * col + 8 * h));
    LAS char* pw = opaque(sm + wave * P_WAVE + (col + 1) * 144 + h * 16);
    *(LAS v2u*)(sm + LD_KP + wave * 1536 + lane * 8) = v2u{0, 0};
    if (tid < 32) *(LAS u32*)(sm + LD_U + (tid >> 4) * 4160 + (tid & 15) * 4) = 0;
    SB();
    const u32 tag = __builtin_amdgcn_readfirstlane(tagv) + 1u;
    st16(rs, xxv, v4u{xa[0], tag, xb[0], tag});
    st16(rs, xxv + 16384, v4u{xa[1], tag, xb[1], tag});
    SB();
    {
        float a = 0.f, q = 0.f;
#pragma unroll
        for (int i = 0; i < 4; i++) { a = dot2bf(xr[i], 0x3f803f80u, a); q = dot2bf(xr[i], xr[i], q); }
        const float mean = wave_sum(a) * (1.f / D), ex2 = wave_sum(q) * (1.f / D);
        const float var = rbf(fmaxf(ex2 - mean * mean, 0.f));
        const float sd = __builtin_amdgcn_sqrtf(rbf(var + 1e-5f));
        st8(rs, flv, WS_ST1 + (((b & 1) * 512 + 4 * (b >> 1) + (wave >> 1)) * 2 + (wave & 1)) * 8, v2u{pk_bf16(mean, sd), tag});
    }
    SB();
    v2u tin[8];
    w1_task_load(p, w1task, lane, tin);
    SB();
    const int tau = main_blk ? (xcd >> 1) * 4 + (i28 & 3) : (b & 15), phi = (xcd & 1) * 7 + (i28 >> 2);
    const int del = i28 >> 2, tau2 = 2 * tau + (xcd & 1);
    const int trl = q4 * 128 + 8 * (pp & 1) + 16 * (2 * G1 + (pp >> 1)), sw = (q4 & 2) << 5;
    const int hf_v = (lane < 56 ? lane : 0) * 4, hf_s = WS_HF + ((tau >> 2) * 8 + wave) * 256;
    const v4u srd = {(u32)(size_t)p.ws, (u32)((size_t)p.ws >> 32) & 0xffffu, (u32)WS_END, 0x00020000u};
    const int hvoff = WS_HT + ((8 * wave + (lane >> 3)) * L + tau * 64) * 2 + (((lane & 7) ^ (((lane >> 3) & 2) << 1)) << 4);
    if (!main_blk) {
        BARRIER_RAW();
        const int w2task = hw;
        if (w2task < 196) {
            w2_task(p, rs, w2task, lane, sm + LD_HB + wave * 8192);
            wait_vm<0>();
            st4(rs, flv, WS_WF2 + (w2task / 28) * 128 + (w2task % 28) * 4, tag);
        }
        asm volatile("" :: "v"(cpf), "v"(tin[7]));
        const int unit = 255 - hw;
        if (unit < 112) {
            while ((u32)__builtin_amdgcn_s_memrealtime() - tstart < CS_T) __builtin_amdgcn_s_sleep(8);
            const int fv = (col < 28 ? col : 0) * 4, fs = WS_WF1 + ((unit >> 3) * 2 + ((unit >> 2) & 1)) * 128;
            wait_flags(rs, fv, fs, ld4(rs, fv, fs), tag);
            float c0 = 0.f, c1 = 0.f;
#pragma unroll 1
            for (int k0 = 0; k0 < 28; k0 += 14) {
                v4u w[14];
#pragma unroll
                for (int ks = 0; ks < 14; ks++) w[ks] = ld16(rs, l16 + (ks & 3) * 1024, WS_W1T + (unit * 28 + k0 + (ks & ~3)) * 1024);
#pragma unroll
                for (int ks = 0; ks < 14; ks++) {
                    c0 = dot2bf(w[ks][0], 0x3f803f80u, c0); c1 = dot2bf(w[ks][1], 0x3f803f80u, c1);
                    c0 = dot2bf(w[ks][2], 0x3f803f80u, c0); c1 = dot2bf(w[ks][3], 0x3f803f80u, c1);
                }
            }
            st8(rs, lane < 32 ? WS_CS + lane * 8 : OOB, unit * 256, v2u{__builtin_bit_cast(u32, half_sum(c0 + c1)), tag});
        }
        {
            const int tl = (b >> 4) & 1;
            if (wave == 0) {
                int spins = SPIN_CAP;
#pragma unroll 1
                while (ld4(rs, 0, WS_TR + (b - 224) * 128) != tag && --spins > 0) __builtin_amdgcn_s_sleep(4);
            }
            BARRIER();
            if (wave < 7) {
                const int gv = WS_HT + ((lane >> 2) * L + 1024 + tau * 64 + tl * 32) * 2 + (lane & 3) * 16;
                LAS char* as = sm + 4096 + wave * 4096 + (8 * h + q4) * 64 + G1 * 32 + pp * 8;
#pragma unroll
                for (int n = 0; n < 4; n++) glds16(srd, gv, (wave * 4 + n) * 65536, (u32)(size_t)sm + 4096 + wave * 4096 + n * 1024);
                wait_vm<0>();
                v4u f[4];
#pragma unroll
                for (int k = 0; k < 4; k++) f[k] = lds_tr2(as + k * 1024, as + k * 1024 + 256);
                float s1 = 0.f, s2 = 0.f, s1b = 0.f, s2b = 0.f;
#pragma unroll
                for (int k = 0; k < 4; k++) {
                    s1 = dot2bf(f[k][0], 0x3f803f80u, s1); s2 = dot2bf(f[k][0], f[k][0], s2);
                    s1b = dot2bf(f[k][1], 0x3f803f80u, s1b); s2b = dot2bf(f[k][1], f[k][1], s2b);
                    s1 = dot2bf(f[k][2], 0x3f803f80u, s1); s2 = dot2bf(f[k][2], f[k][2], s2);
                    s1b = dot2bf(f[k][3], 0x3f803f80u, s1b); s2b = dot2bf(f[k][3], f[k][3], s2b);
                }
                *(LAS f32x2*)(sm + wave * 512 + lane * 8) = f32x2{s1 + s1b, s2 + s2b};
            }
            BARRIER();
            if (wave == 0) {
                f32x2 t = *(LAS f32x2*)(sm + lane * 8);
#pragma unroll
                for (int k = 1; k < 7; k++) t += *(LAS f32x2*)(sm + k * 512 + lane * 8);
                const float mean = half_sum(t.x) * (1.f / D), ex2 = half_sum(t.y) * (1.f / D);
                const float var = rbf(fmaxf(ex2 - mean * mean, 0.f));
                st8(rs, lane < 32 ? WS_SG + col * 16 + tl * 8 : OOB, tau * 512, v2u{pk_bf16(mean, __builtin_amdgcn_sqrtf(rbf(var + 1e-5f))), tag});
            }
        }
        return;
    }
    asm volatile("" :: "v"(r_are), "v"(r_aim), "v"(r_bre), "v"(r_bim), "v"(r_cre), "v"(r_cim), "v"(r_dsk), "v"(cpf));
    SB();

    float unscale;
    {
        const float dt = rbf(__expf(ch ? bfhi(r_ldt) : bflo(r_ldt)));
        const float ar = dt * bflo(r_are), ai = dt * bflo(r_aim);
        const float br = dt * bflo(r_bre), bi = dt * bflo(r_bim);
        const float cr = bflo(r_cre), ci = bflo(r_cim);
        cf cb = {cr * br - ci * bi, -(cr * bi + ci * br)};
        const float a2 = ar * 1.44269504088896340736f;
        const float rh = ai * 0.15915494309189535f;
        const float rl = __builtin_fmaf(ai, 0.15915494309189535f, -rh) + ai * 6.4206382e-9f;
        auto cexp = [&](float s) __attribute__((always_inline)) -> cf {
            const float e = __builtin_amdgcn_exp2f(a2 * s);
            const float r = __builtin_amdgcn_fractf(rh * s) + rl * s;
            return cf{__builtin_amdgcn_cosf(r), __builtin_amdgcn_sinf(r)} * e;
        };
        const float grow = fmaxf(1.f, __builtin_amdgcn_exp2f(a2 * 2048.f));
        const float sb = wave_sum((fabsf(cb.x) + fabsf(cb.y)) * grow);
        *(LAS float*)(sm + LD_SB + wave * 4) = sb;
        int ex = (int)((__builtin_bit_cast(u32, sb) >> 23) & 255);
        ex = min(max(ex, 30), 230);
        unscale = __builtin_bit_cast(float, (u32)(127 - 15 - 126 + ex) << 23);
        cb *= __builtin_bit_cast(float, (u32)(127 + 15 + 126 - ex) << 23);

        const cf e1 = cexp(1.f), e16 = cexp(16.f), e256 = cexp(256.f);
        const cf e2 = cmul(e1, e1), e32 = cmul(e16, e16), e512 = cmul(e256, e256), x1 = cmulc(cb, e1), x256 = cmulc(cb, e256);
        SB();
        const cf e4 = cmul(e2, e2), e64 = cmul(e32, e32), e1024 = cmul(e512, e512), x2 = cmulc(cb, e2), x3 = cmulc(x1, e2);
        const cf x512 = cmulc(cb, e512), x768 = cmulc(x256, e512), y6 = cmul(e16, e32);
        SB();
        const cf e8 = cmul(e4, e4), e128 = cmul(e64, e64);
        const cf x4 = cmulc(cb, e4), x5 = cmulc(x1, e4), x6 = cmulc(x2, e4), x7 = cmulc(x3, e4), x258 = cmulc(x2, e256);
        const cf x1024 = cmulc(cb, e1024), x1280 = cmulc(x256, e1024), x1536 = cmulc(x512, e1024), x1792 = cmulc(x768, e1024);
        SB();
        auto put = [&](int q, cf v) __attribute__((always_inline)) { *(LAS u32*)(xyw + q * XY_STRIDE) = pkh(v); };
        cf y[8];
        y[0] = cf{1.f, 0.f}; y[1] = e8; y[2] = e16; y[3] = cmul(e8, e16); y[4] = e32; y[5] = cmul(e8, e32); y[6] = y6;
        const cf e192 = cmul(e128, e64), x8 = cmulc(x4, e4), x260 = cmulc(x4, e256), x262 = cmulc(x6, e256), x516 = cmulc(x4, e512);
        put(32, cb); put(33, x1); put(34, x2); put(35, x3); put(36, x4); put(37, x5); put(38, x6); put(39, x7);
        put(40, x256); put(41, x258);
        SB();
        y[7] = cmul(y[3], e32);
        const cf x264 = cmulc(x8, e256), x520 = cmulc(x8, e512), x772 = cmulc(x260, e512), x1032 = cmulc(x8, e1024);
        cf t[8];
#pragma unroll
        for (int q = 1; q < 7; q++) t[q] = cmul(y[q], e64);
        put(42, x260); put(43, x262); put(45, x512); put(46, x516);
#pragma unroll
        for (int q = 0; q < 6; q++) put(q, y[q]);
        SB();
        t[7] = cmul(y[7], e64);
        const cf x776 = cmulc(x264, e512), x1288 = cmulc(x264, e1024), x1544 = cmulc(x520, e1024);
        cf t2[8];
#pragma unroll
        for (int q = 1; q < 8; q++) t2[q] = cmul(y[q], e128);
        put(6, y[6]); put(7, y[7]); put(8, e64);
#pragma unroll
        for (int q = 1; q < 6; q++) put(8 + q, t[q]);
        put(44, x264); put(47, x520); put(48, x768); put(49, x772); put(51, x1024); put(52, x1032);
        SB();

        const cf x1800 = cmulc(x776, e1024);
        cf t3[8];
#pragma unroll
        for (int q = 1; q < 8; q++) t3[q] = cmul(y[q], e192);
        put(14, t[6]); put(15, t[7]); put(16, e128);
#pragma unroll
        for (int q = 1; q < 8; q++) put(16 + q, t2[q]);
        put(50, x776); put(53, x1280); put(54, x1288); put(55, x1536); put(56, x1544); put(57, x1792);
        SB();
        put(24, e192);
#pragma unroll
        for (int q = 1; q < 8; q++) put(24 + q, t3[q]);
        put(58, x1800);
    }
    SB();
    asm volatile("" :: "v"(tin[7]));
    if (w1task >= 0) w1_task_store(rs, w1task, lane, tin);
    SB();
    const int t16 = tid * 16;
    v4u gx[2], gs[2];
    auto gr_load = [&]() __attribute__((always_inline)) {
        gx[0] = ld16(rs, t16, WS_XX + pair * 16384); gx[1] = ld16(rs, t16, WS_XX + pair * 16384 + 8192);
        gs[0] = ld16(rs, t16, WS_ST1); gs[1] = ld16(rs, t16, WS_ST1 + 8192);
    };
    const bool gr_first = (u32)__builtin_amdgcn_s_memrealtime() - tstart >= GR_T;
    if (gr_first) gr_load();
    SB();
    {
        f32x16 kd;
#pragma unroll
        for (int i2 = 0; i2 < 16; i2++) kd[i2] = 0.f;
        v4u fa[8], fb[8];
#pragma unroll
        for (int s = 0; s < 8; s++) { fa[s] = *(LAS v4u*)(pa + s * 32); fb[s] = *(LAS v4u*)(pb + s * 32); }
        SB();
#pragma unroll
        for (int s = 0; s < 8; s++) kd = mfma_f16(fa[s], fb[s], kd);
        SB();

#pragma unroll
        for (int g2 = 0; g2 < 4; g2++)
            *(LAS f32x4*)(exw + g2 * 1024) = f32x4{kd[4 * g2], kd[4 * g2 + 1], kd[4 * g2 + 2], kd[4 * g2 + 3]} * unscale;
    }
    if (w1task >= 0) { wait_vm<0>(); st4(rs, flv, w1_flag(w1task), tag); }
    if (!gr_first) {
        while ((u32)__builtin_amdgcn_s_memrealtime() - tstart < GR_T) __builtin_amdgcn_s_sleep(2);
        gr_load();
    }
    SB();
    BARRIER();
    const int l28 = (col < 28 ? col : 0) * 4;
    SB();
    float ik;
    {
        const f32x4 s0 = *(LAS f32x4*)(sm + LD_SB + ch * 16);
        const f32x4 a0 = *(LAS f32x4*)(exr), a1 = *(LAS f32x4*)(exr + XY_WAVE), a2 = *(LAS f32x4*)(exr + 2 * XY_WAVE), a3 = *(LAS f32x4*)(exr + 3 * XY_WAVE);
        const float b0 = (s0[0] + s0[1]) + (s0[2] + s0[3]);
        int e0 = (int)((__builtin_bit_cast(u32, b0) >> 23) & 255);
        e0 = min(max(e0, 30), 230);
        ik = __builtin_bit_cast(float, (u32)(127 - 15 - 126 + e0) << 23);
        const float sk = __builtin_bit_cast(float, (u32)(127 + 15 + 126 - e0) << 23);
        const f32x4 v = ((a0 + a1) + (a2 + a3)) * sk;
        const u32 p01 = pk_f16(v[0], v[1]), p23 = pk_f16(v[2], v[3]);
        LAS char* o = kpw;
        if (wq == 0) {
            *(LAS u16*)(o) = (u16)p01; *(LAS u16*)(o + 1536) = (u16)(p01 >> 16);
            *(LAS u16*)(o + 2 * 1536) = (u16)p23; *(LAS u16*)(o + 3 * 1536) = (u16)(p23 >> 16);
        } else if (wq == 1) {
            *(LAS u16*)(o) = (u16)p01; *(LAS u16*)(o + 1536) = (u16)p01;
            *(LAS u16*)(o + 2 * 1536) = (u16)(p01 >> 16); *(LAS u16*)(o + 3 * 1536) = (u16)p23;
        } else if (wq == 2) {
            *(LAS u32*)(o) = p01; *(LAS u32*)(o + 1536) = p01;
            *(LAS u32*)(o + 2 * 1536) = (p01 & 0xffffu) | (p23 << 16);
            *(LAS u32*)(o + 3 * 1536) = (p01 >> 16) | (p23 << 16);
        } else {
            const bool ev = (col & 1) == 0;
            *(LAS u32*)(o) = ev ? ((p01 & 0xffffu) | (p01 << 16)) : p01;
            *(LAS u32*)(o + 1536) = p01; *(LAS u32*)(o + 2 * 1536) = p01; *(LAS u32*)(o + 3 * 1536) = p01;
            *(LAS u32*)(o + 128) = ev ? ((p23 & 0xffffu) | (p23 << 16)) : p23;
            *(LAS u32*)(o + 128 + 1536) = p23; *(LAS u32*)(o + 128 + 2 * 1536) = p23; *(LAS u32*)(o + 128 + 3 * 1536) = p23;
        }
    }
    SB();
    auto gr_ok = [&]() __attribute__((always_inline)) {
        const u32 a = (gx[0][1] ^ tag) | (gx[0][3] ^ tag) | (gx[1][1] ^ tag) | (gx[1][3] ^ tag);
        const u32 c = (gs[0][1] ^ tag) | (gs[0][3] ^ tag) | (gs[1][1] ^ tag) | (gs[1][3] ^ tag);
        return __all((a | c) == 0);
    };
    if (!gr_ok()) {
        int spins = SPIN_CAP;
#pragma unroll 1
        do {
            __builtin_amdgcn_s_sleep(4);
            gr_load();
        } while (!gr_ok() && --spins > 0);
    }
    v4u wf[28];
    const int w1off = WS_W1T + (phi * 8 + wave) * 28 * 1024;
    auto w1_load = [&](int ks) __attribute__((always_inline)) {
        wf[ks] = ld16(rs, l16 + ((ks % 7) & 3) * 1024, w1off + (ks / 7) * 7168 + ((ks % 7) >> 2) * 4096);
    };
    const int wf_s = WS_WF1 + phi * 256 + ch * 128;
    const u32 wfl = ld4(rs, l28, wf_s);
    SB();
    u32 xc[4], udk[4];
    {
        float u0[4], u1[4];
#pragma unroll
        for (int i = 0; i < 4; i++) {
            xc[i] = gx[i >> 1][2 * (i & 1)];
            const u32 st = gs[i >> 1][2 * (i & 1)];
            const float m = bflo(st), r = __builtin_amdgcn_rcpf(bfhi(st));
            const u32 dd = pk_bf16(bflo(xc[i]) - m, bfhi(xc[i]) - m);
            const u32 uu = pk_bf16(bflo(dd) * r, bfhi(dd) * r);
            u0[i] = bflo(uu); u1[i] = bfhi(uu);
        }
        *(LAS v2u*)(uw) = v2u{pk_f16(u0[0], u0[1]), pk_f16(u0[2], u0[3])};
        *(LAS v2u*)(uw + 4160) = v2u{pk_f16(u1[0], u1[1]), pk_f16(u1[2], u1[3])};
#pragma unroll
        for (int i = 0; i < 4; i++) udk[i] = pk_bf16(u0[i] * bflo(r_dsk), u1[i] * bfhi(r_dsk));
    }
    BARRIER();
    f32x4 r0, r1, l3;
    f32x2 r2, l2;
    float l1, r3;
    v4u xu;
    u32 ya, yb;
    const int c1 = tid & 255;
    LAS char* q1 = opaque(sm + ch * 4 * P_WAVE + ((c1 >> 3) + 1) * 144 + (c1 & 7) * 16);
    LAS char* q0 = opaque(sm + ch * 4 * P_WAVE + (((c1 - 1) >> 3) + 1) * 144 + ((c1 - 1) & 7) * 16);
    LAS char* xur = opaque(sm + LD_XU + ch * 8192 + c1 * 16);
    auto ce_read = [&](int hf) __attribute__((always_inline)) {
        const int pb_ = hf ? LD_PB : LD_PA;
        r0 = *(LAS f32x4*)(q1 + pb_);
        r1 = *(LAS f32x4*)(q1 + pb_ + P_WAVE); l1 = *(LAS float*)(q0 + pb_ + P_WAVE + 12);
        r2 = *(LAS f32x2*)(q1 + pb_ + 2 * P_WAVE); l2 = *(LAS f32x2*)(q0 + pb_ + 2 * P_WAVE + 8);
        r3 = *(LAS float*)(q1 + pb_ + 3 * P_WAVE); l3 = *(LAS f32x4*)(q0 + pb_ + 3 * P_WAVE);
        xu = *(LAS v4u*)(xur + hf * 4096);
    };
    auto ce_part = [&](int hf, int part) __attribute__((always_inline)) {
        if (part == 0) ya = pk_bf16s(((r0[0] + l2[0]) + (l1 + l3[1])) * ik, ((r0[1] + l2[1]) + (r1[0] + l3[2])) * ik);
        if (part == 1) yb = pk_bf16s(((r0[2] + r2[0]) + (r1[1] + l3[3])) * ik, ((r0[3] + r2[1]) + (r1[2] + r3)) * ik);
        if (part == 2) ya = pk_bf16s(bflo(ya) + bfhi(xu[0]), bfhi(ya) + bfhi(xu[1]));
        if (part == 3) yb = pk_bf16s(bflo(yb) + bfhi(xu[2]), bfhi(yb) + bfhi(xu[3]));
        if (part == 4) ya = pk_bf16s(bflo(ya) + bflo(xu[0]), bfhi(ya) + bflo(xu[1]));
        if (part == 5) {
            yb = pk_bf16s(bflo(yb) + bflo(xu[2]), bfhi(yb) + bflo(xu[3]));
            st8(rs, WS_HT + c1 * 8, (d0 + ch) * (L * 2) + hf * 2048, v2u{ya, yb});
        }
    };
    const int hf_set = WS_HF + (wq * 8 + (((d0 + ch) >> 3) & 7)) * 256 + (((d0 + ch) >> 6) * 8 + ((d0 + ch) & 7)) * 4;
    {
        f32x16 acc0, acc1;
#pragma unroll
        for (int i = 0; i < 16; i++) { acc0[i] = 0.f; acc1[i] = 0.f; }
        constexpr int PD = 3;
        v4u fu[PD + 1], fk[16], fk1[PD + 1];
        auto issue = [&](int n) __attribute__((always_inline)) {
            const int B0 = (n >= 16 && n < 32) ? 2016 - 64 * (n - 16) : 992 - 64 * (n & 15);
            fu[n % (PD + 1)] = lds_tr2(ua + 2 * (B0 - 32) + 32, ua + 2 * (B0 - 32));
            if (n < 16) fk[n] = *(LAS v4u*)(ka + 32 * n);
            if (n >= 32) fk1[n % (PD + 1)] = *(LAS v4u*)(ka + 32 * (n - 16));
        };
#pragma unroll
        for (int n = 0; n < PD; n++) issue(n);
        SB();
        static_for<0, 48>([&](auto N_) __attribute__((always_inline)) {
            constexpr int n = decltype(N_)::v;
            if (n + PD < 48) issue(n + PD);
            if constexpr (n < 16) acc0 = mfma_f16(fu[n % (PD + 1)], fk[n], acc0);
            else if constexpr (n < 32) acc1 = mfma_f16(fu[n % (PD + 1)], fk[n - 16], acc1);
            else acc1 = mfma_f16(fu[n % (PD + 1)], fk1[n % (PD + 1)], acc1);
            if (n == 1) *(LAS v4u*)(sm + LD_XU + t16) = v4u{__builtin_amdgcn_perm(udk[0], xc[0], 0x05040100u), __builtin_amdgcn_perm(udk[1], xc[1], 0x05040100u),
                                                           __builtin_amdgcn_perm(udk[2], xc[2], 0x05040100u), __builtin_amdgcn_perm(udk[3], xc[3], 0x05040100u)};
            if (n == 2) *(LAS v4u*)(sm + LD_XU + 8192 + t16) = v4u{__builtin_amdgcn_perm(udk[0], xc[0], 0x07060302u), __builtin_amdgcn_perm(udk[1], xc[1], 0x07060302u),
                                                                  __builtin_amdgcn_perm(udk[2], xc[2], 0x07060302u), __builtin_amdgcn_perm(udk[3], xc[3], 0x07060302u)};
            if (n == W1_CHK) {
                PIN(acc0); SB();
                wait_flags(rs, l28, wf_s, wfl, tag);
            }
            if constexpr (n == 16 || n == 17) {
                constexpr int g = 2 * (n - 16);
                *(LAS f32x4*)(pw + LD_PA + g * 32) = f32x4{acc0[4 * g], acc0[4 * g + 1], acc0[4 * g + 2], acc0[4 * g + 3]};
                *(LAS f32x4*)(pw + LD_PA + g * 32 + 32) = f32x4{acc0[4 * g + 4], acc0[4 * g + 5], acc0[4 * g + 6], acc0[4 * g + 7]};
            }
            if (n == 17) {
                PIN(acc1);
                if (lane == 63) {
                    *(LAS f32x4*)(sm + LD_PA + wave * P_WAVE + 112) = f32x4{0.f, 0.f, 0.f, 0.f};
                    *(LAS f32x4*)(sm + LD_PB + wave * P_WAVE + 112) = f32x4{acc0[12], acc0[13], acc0[14], acc0[15]};
                }
            }
            if (n == 18) {
                BARRIER();
                __builtin_amdgcn_s_setprio(1);
                st4(rs, (b == 0 && tid == 0) ? WS_EPOCH : OOB, 0, tag);
                ce_read(0);
            }
            if (n >= 21 && n < 27) ce_part(0, n - 21);
            if (n == HF_N) {
                wait_vm<w1_cnt(HF_N) - w1_cnt(26)>();
                st4(rs, flv, hf_set, tag);
                __builtin_amdgcn_s_setprio(0);
            }
            SB();
#pragma unroll
            for (int ks = w1_cnt(n); ks < w1_cnt(n + 1); ks++) w1_load(ks);
            SB();
        });
#pragma unroll
        for (int g = 0; g < 4; g++) *(LAS f32x4*)(pw + LD_PB + g * 32) = f32x4{acc1[4 * g], acc1[4 * g + 1], acc1[4 * g + 2], acc1[4 * g + 3]};
    }
#pragma unroll
    for (int ks = w1_cnt(48); ks < 28; ks++) { w1_load(ks); SB(); }
    const int kap = phi * 8 + wave;
    const u32 w2fl = ld4(rs, l28, WS_WF2 + del * 128);
    v2u csg = __builtin_amdgcn_raw_buffer_load_b64(rs, WS_CS + col * 8, kap * 256, SC1);
    BARRIER();
    ce_read(1);
    auto load_h = [&](int hf, int n) __attribute__((always_inline)) {
        glds16(srd, hvoff + hf * 2048, n * 262144, (u32)(size_t)sm + (hf ? LD_HB : LD_HA) + wave * 1024 + n * 8192);
    };
#pragma unroll
    for (int i = 0; i < 5; i++) ce_part(1, i);
    SB();
    u32 hfl = ld4(rs, hf_v, hf_s);
    SB();
    ce_part(1, 5);
    SB();
    wait_flags(rs, l28, WS_WF2 + del * 128, w2fl, tag);
    if (!__all(csg[1] == tag)) {
        int spins = SPIN_CAP;
#pragma unroll 1
        do {
            __builtin_amdgcn_s_sleep(4);
            csg = __builtin_amdgcn_raw_buffer_load_b64(rs, WS_CS + col * 8, kap * 256, SC1);
        } while (!__all(csg[1] == tag) && --spins > 0);
    }
    *(LAS u32*)(sm + LD_CS + wave * 128 + col * 4) = csg[0];
    wait_flags(rs, hf_v, hf_s, hfl, tag);
#pragma unroll
    for (int n = 0; n < 7; n++) load_h(0, n);
    SB();
    wait_vm<7>();
    st4(rs, flv, hf_set + 8192, tag);
    SB();
    wait_vm<1>();
    BARRIER();
    v4u w2[28];
    const int w2v = WS_W2T + (del * 2 * 112 + wave * 14) * 1024 + l16;
    auto w2_lds = [&](int f) __attribute__((always_inline)) { return f < 14 ? LD_PB + (wave * 4 + f - 10) * 1024 : LD_HA + (wave * 7 + f - 14) * 1024; };
    auto w2_load = [&](int f) __attribute__((always_inline)) {
        if (f >= 10 && f < 21) glds16(srd, w2v + (f & 1) * 112 * 1024, (f >> 1) * 1024, (u32)(size_t)sm + w2_lds(f));
        else w2[f] = ld16(rs, w2v + (f & 1) * 112 * 1024, (f >> 1) * 1024);
    };
    const int af_v = (lane < 14 ? lane : 0) * 4, af_s = WS_AF + (tau * 16 + wave) * 128;
    const int av = WS_ACT + (tau2 * 112 + wave * 14) * 1024 + l16;
    const int dd = 64 * del + 8 * wave + 4 * h;
    LAS char* hra = sm + LD_HA + dd * 128 + trl + (((xcd & 1) << 6) ^ sw);
    v2u hr[2];
    u32 afl;
    v4u a0[14], a1[14];
    const int af_set = WS_AF + (tau * 16 + kap / 14) * 128 + (kap % 14) * 4;
    {
        LAS char* at0 = opaque(sm + LD_HA + 8 * h * 128 + trl + sw);
        LAS char* at1 = opaque(sm + LD_HA + 8 * h * 128 + trl + (64 ^ sw));
        LAS char* csr = opaque(sm + LD_CS + wave * 128 + h * 16);
        LAS char* as = opaque(sm + LD_HA + 8 * h * 128 + trl + (((wave & 1) << 6) ^ sw) + (wave >> 1) * 7 * 2048);
        LAS char* str = opaque(sm + LD_ST + col * 32);
        LAS char* cur;
        constexpr int PD = 4;
        v4u fr[PD + 1], sf;
        auto issue = [&](int n) __attribute__((always_inline)) {
            const int tile = n / 28, j = n % 28;
            if (j % 7 == 0) cur = opaque(((tile & 1) ? at1 : at0) + ((j / 7) * 14336 + (tile >> 1) * 57344));
            LAS char* a = cur + (j % 7) * 2048;
            fr[n % (PD + 1)] = lds_tr2(a, a + 512);
        };
        f32x16 acc[2];
        float nm, k0, k1, sd2, eg, eu, ez, er, eo0;
        f32x4 cg4, cu4;
        f32x4 sta, stb;
        float s1, s2, s1b, s2b;
        u32 opk[4];
        auto stat_acc = [&](int k, int part) __attribute__((always_inline)) {
            const u32 f0 = sf[2 * part], f1 = sf[2 * part + 1];
            if (k == 0 && part == 0) { s1 = 0.f; s2 = 0.f; s1b = 0.f; s2b = 0.f; }
            s1 = dot2bf(f0, 0x3f803f80u, s1); s2 = dot2bf(f0, f0, s2);
            s1b = dot2bf(f1, 0x3f803f80u, s1b); s2b = dot2bf(f1, f1, s2b);
        };
        auto stat_write = [&](int hf) __attribute__((always_inline)) {
            const float a = half_sum(s1 + s1b), c = half_sum(s2 + s2b);
            *(LAS f32x2*)(str + (hf * 2 + (wave & 1)) * 1024 + (wave >> 1) * 8) = f32x2{a, c};
        };
        auto epi_read = [&](int tile) __attribute__((always_inline)) { sta = *(LAS f32x4*)(str + tile * 1024); stb = *(LAS f32x4*)(str + tile * 1024 + 16); };
        auto epi_stats = [&]() __attribute__((always_inline)) {
            float m = ((sta[0] + sta[2]) + (stb[0] + stb[2])) * (1.f / D), e2 = ((sta[1] + sta[3]) + (stb[1] + stb[3])) * (1.f / D);
            asm("" : "+v"(m)); asm("" : "+v"(e2));
            const float var = rbf(fmaxf(e2 - m * m, 0.f));
            const float sd = rbf(__builtin_amdgcn_sqrtf(rbf(var + 1e-5f))), r = __builtin_amdgcn_rcpf(sd);
            nm = -rbf(m); sd2 = sd * sd; k0 = r * -2.3022082f; k1 = (r * r) * (r * -0.10294324f);
        };
        v4u sg;
        auto epi_stats2 = [&](u32 st) __attribute__((always_inline)) {
            const float sd = bfhi(st), r = __builtin_amdgcn_rcpf(sd);
            nm = -bflo(st); sd2 = sd * sd; k0 = r * -2.3022082f; k1 = (r * r) * (r * -0.10294324f);
        };
        auto cs_read = [&](int g) __attribute__((always_inline)) { cg4 = *(LAS f32x4*)(csr + g * 32); cu4 = *(LAS f32x4*)(csr + 64 + g * 32); };
        auto slice = [&](const f32x16& a, int sidx) __attribute__((always_inline)) {
            const int e = sidx / 3, st = sidx % 3;
            if (st == 0) {
                if (e > 0) { float o = eu * er; asm("" : "+v"(o)); if ((e - 1) & 1) opk[(e - 1) >> 1] = pk_bf16(eo0, o); else eo0 = o; }
                if (e < 8) {
                    eg = __builtin_fmaf(nm, cg4[e & 3], a[e & 7]); eu = __builtin_fmaf(nm, cu4[e & 3], a[8 + (e & 7)]);
                    asm("" : "+v"(eg), "+v"(eu));
                }
            } else if (st == 1) { ez = __builtin_amdgcn_exp2f(eg * __builtin_fmaf(eg * eg, k1, k0)); asm("" : "+v"(ez)); }
            else { er = __builtin_amdgcn_rcpf(__builtin_fmaf(ez, sd2, sd2)); eu *= eg; asm("" : "+v"(er), "+v"(eu)); }
        };
        auto epi_four = [&](const f32x16& a, int g) __attribute__((always_inline)) {
            const f32x4 nm4 = {nm, nm, nm, nm}, sd4 = {sd2, sd2, sd2, sd2};
            const f32x4 gg = __builtin_elementwise_fma(nm4, cg4, f32x4{a[4 * g], a[4 * g + 1], a[4 * g + 2], a[4 * g + 3]});
            const f32x4 u = __builtin_elementwise_fma(nm4, cu4, f32x4{a[8 + 4 * g], a[9 + 4 * g], a[10 + 4 * g], a[11 + 4 * g]});
            const f32x4 z = gg * __builtin_elementwise_fma(gg * gg, f32x4{k1, k1, k1, k1}, f32x4{k0, k0, k0, k0});
            const f32x4 e = {__builtin_amdgcn_exp2f(z.x), __builtin_amdgcn_exp2f(z.y), __builtin_amdgcn_exp2f(z.z), __builtin_amdgcn_exp2f(z.w)};
            const f32x4 dn = __builtin_elementwise_fma(e, sd4, sd4);
            const f32x4 o = (gg * u) * f32x4{__builtin_amdgcn_rcpf(dn.x), __builtin_amdgcn_rcpf(dn.y), __builtin_amdgcn_rcpf(dn.z), __builtin_amdgcn_rcpf(dn.w)};
            opk[2 * g] = pk_bf16(o.x, o.y); opk[2 * g + 1] = pk_bf16(o.z, o.w);
        };
        auto epi_store = [&](int tile) __attribute__((always_inline)) {
            const int tok32 = (tile >> 1) * 32 + tau * 2 + (tile & 1);
            st16(rs, WS_ACT + l16 + (tok32 * 112 + kap) * 1024, v4u{opk[0], opk[1], opk[2], opk[3]});
        };
#pragma unroll
        for (int n = 0; n < PD; n++) issue(n);
        SB();
        static_for<0, 112>([&](auto N_) __attribute__((always_inline)) {
            constexpr int n = decltype(N_)::v;
            constexpr int tile = n / 28, j = n % 28;
            if (n == 52) {
                wait_vm<g1_cum(52) - g1_cum(HB_N + 9)>();
                BARRIER();
                __builtin_amdgcn_s_setprio(1);
            }
            if (n + PD < 112) issue(n + PD);
            if (j == 0) {
                f32x16 z;
#pragma unroll
                for (int i = 0; i < 16; i++) z[i] = 0.f;
                acc[tile & 1] = mfma_bf16(wf[0], fr[n % (PD + 1)], z);
            } else {
                acc[tile & 1] = mfma_bf16(wf[j], fr[n % (PD + 1)], acc[tile & 1]);
            }
            SB();
            if constexpr (n < 28) {
                constexpr int sk = n;
                if (sk < 21 && sk % 3 == 0) { if (sk > 0) { stat_acc(sk / 3 - 1, 1); SB(); } sf = lds_tr2(as + (sk / 3) * 2048, as + (sk / 3) * 2048 + 512); }
                if (sk < 21 && sk % 3 == 2) stat_acc(sk / 3, 0);
                if (sk == 21) stat_acc(6, 1);
                if (sk == 22) stat_write(0);
            }
            if (n == HBP_N) hfl = ld4(rs, hf_v, hf_s + 8192);
            if (n == HB_N) {
                PIN(acc[0]);
                SB();
                wait_flags(rs, hf_v, hf_s + 8192, hfl, tag);
            }
            if (n == 48) hr[0] = lds_tr(hra);
            if (n == 27) BARRIER_RAW();
            if (n == 27 || n == 55) epi_read(tile);
            if (n == 84) {
                PIN(acc[0]); PIN(acc[1]);
                SB();
                if (!__all(sg[1] == tag && sg[3] == tag)) {
                    int spins = SPIN_CAP;
#pragma unroll 1
                    do {
                        __builtin_amdgcn_s_sleep(2);
                        sg = ld16(rs, WS_SG + col * 16, tau * 512);
                    } while (!__all(sg[1] == tag && sg[3] == tag) && --spins > 0);
                }
            }
            if (tile == 1) {
                if (j == 0) cs_read(0);
                if (j == 1) epi_stats();
                if (j >= 2 && j < 27) slice(acc[0], j - 2);
                if (j == 11) { SB(); cs_read(1); }
            }
            if (tile == 2) {
                if (j == 0) cs_read(0);
                if (j == 1) epi_stats();
                if (j >= 2 && j < 10) { slice(acc[1], 3 * (j - 2)); if (j == 5) { SB(); cs_read(1); } slice(acc[1], 3 * (j - 2) + 1); slice(acc[1], 3 * (j - 2) + 2); }
                if (j == 10) slice(acc[1], 24);
            }
            if (tile == 3) {
                if (j == 0) cs_read(0);
                if (j == 1) epi_stats2(sg[0]);
                if (j >= 2 && j < 27) slice(acc[0], j - 2);
                if (j == 11) { SB(); cs_read(1); }
            }
            if (n == AFC_N) {
                PIN(acc[1]); asm volatile("" : "+v"(eg), "+v"(eu), "+v"(ez), "+v"(er), "+v"(eo0), "+v"(opk[0]), "+v"(opk[1]));
                SB();
                wait_flags(rs, af_v, af_s, afl, tag);
            }
            constexpr int op = g1_op(n);
            if constexpr (op >= 60) a0[op - 60] = ld16(rs, av, (op - 60) * 1024);
            if constexpr (op >= 0 && op < 28) w2_load(op);
            if constexpr (op >= 32 && op < 40) load_h(1, op - 32);
            if constexpr (op >= 40 && op < 50) epi_store(op - 40);
            if constexpr (op == 50) { wait_vm<g1_cum(AF_N) - g1_cum(ST1_N + 1)>(); st4(rs, flv, af_set, tag); __builtin_amdgcn_s_setprio(0); BARRIER_RAW(); }
            if constexpr (op == 51) afl = ld4(rs, af_v, af_s);
            if constexpr (op == 52) st4(rs, (tid == 0 && del == 0) ? 0 : OOB, WS_TR + ((xcd & 1) * 16 + tau) * 128, tag);
            if constexpr (op == 53) sg = ld16(rs, WS_SG + col * 16, tau * 512);
            SB();
        });
        PIN(acc[1]);
        cs_read(0);
        epi_stats2(sg[2]);
        epi_four(acc[1], 0);
        cs_read(1);
        epi_four(acc[1], 1);
        epi_store(3);
    }
    SB();
#pragma unroll
    for (int n = A0_IN; n < 14; n++) { a0[n] = ld16(rs, av, n * 1024); SB(); }
    hr[1] = lds_tr(hra + 57344);
#pragma unroll
    for (int f = 10; f < 21; f++) w2[f] = *(LAS v4u*)(sm + w2_lds(f) + l16);
    BARRIER();
    f32x16 acc[2][2];
#pragma unroll
    for (int i = 0; i < 16; i++) { acc[0][0][i] = 0.f; acc[0][1][i] = 0.f; acc[1][0][i] = 0.f; acc[1][1][i] = 0.f; }
    LAS char* rw = opaque(sm + wave * 1024 + l16);
    LAS char* rr = opaque(sm + wave * 8192 + l16);
    auto red_write = [&](int hf) __attribute__((always_inline)) {
#pragma unroll
        for (int o = 0; o < 8; o++) {
            const f32x16& t = acc[o >> 2][hf];
            const int r = 4 * (o & 3);
            *(LAS f32x4*)(rw + hf * 65536 + o * 8192) = f32x4{t[r], t[r + 1], t[r + 2], t[r + 3]};
        }
    };
    auto red_finish = [&](int hf) __attribute__((always_inline)) {
        f32x4 v[8];
#pragma unroll
        for (int s = 0; s < 8; s++) v[s] = *(LAS f32x4*)(rr + hf * 65536 + s * 1024);
        const f32x4 t = ((v[0] + v[1]) + (v[2] + v[3])) + ((v[4] + v[5]) + (v[6] + v[7]));
        const u32 ta = pk_bf16(t[0], t[1]), tb = pk_bf16(t[2], t[3]);
        const int tok = 1024 * hf + 32 * tau2 + col;
        *(v2u*)(p.out + (size_t)tok * D + dd) = v2u{pk_bf16(bflo(ta) + bflo(hr[hf][0]), bfhi(ta) + bfhi(hr[hf][0])),
                                                    pk_bf16(bflo(tb) + bflo(hr[hf][1]), bfhi(tb) + bfhi(hr[hf][1]))};
    };
#pragma unroll
    for (int n = 0; n < 14; n++) {
        acc[0][0] = mfma_bf16(w2[2 * n], a0[n], acc[0][0]);
        acc[1][0] = mfma_bf16(w2[2 * n + 1], a0[n], acc[1][0]);
        if (n == AFB_N) { wait_vm<14 - A0_IN>(); st4(rs, flv, af_set + 1024, tag); }
        SB();
    }
    PIN(acc[0][0]); PIN(acc[1][0]);
    red_write(0);
    BARRIER();
    afl = ld4(rs, af_v, af_s + 1024);
    SB();
    red_finish(0);
    SB();
    wait_flags(rs, af_v, af_s + 1024, afl, tag);
#pragma unroll
    for (int n = 0; n < 14; n++) { a1[n] = ld16(rs, av + 32 * 112 * 1024, n * 1024); SB(); }
    SB();
#pragma unroll
    for (int n = 0; n < 14; n++) {
        acc[0][1] = mfma_bf16(w2[2 * n], a1[n], acc[0][1]);
        acc[1][1] = mfma_bf16(w2[2 * n + 1], a1[n], acc[1][1]);
        SB();
    }
    red_write(1);
    BARRIER();
    red_finish(1);
    asm volatile(".Lfb_code_end:");
}

}

extern "C" void kernel_launch(void* const* d_in, const int* in_sizes, int n_in,
                              void* d_out, int out_size, void* d_ws, size_t ws_size,
                              hipStream_t stream) {
    Params p;
    p.x = (const u16*)d_in[0]; p.Are = (const u16*)d_in[1]; p.Aim = (const u16*)d_in[2];
    p.Bre = (const u16*)d_in[3]; p.Bim = (const u16*)d_in[4]; p.Cre = (const u16*)d_in[5]; p.Cim = (const u16*)d_in[6];
    p.logdt = (const u16*)d_in[7]; p.Dskip = (const u16*)d_in[8];
    p.wg = (const u16*)d_in[10]; p.wu = (const u16*)d_in[11]; p.wd = (const u16*)d_in[12];
    p.ws = (char*)d_ws + ((size_t)220  __H0__ ); p.out = (u16*)d_out;
    (void)hipFuncSetAttribute((const void*)fused_block, hipFuncAttributeMaxDynamicSharedMemorySize, LDS_BYTES);
    fused_block<<<256, 512, LDS_BYTES, stream>>>(p);
}
```

```cpp
#include <hip/hip_runtime.h>
#include <stdint.h>

namespace {

constexpr int L = 2048, D = 448, N = 256, F = 1792;

typedef unsigned short u16;
typedef unsigned int u32;
typedef unsigned long long u64;
typedef u32 v4u __attribute__((ext_vector_type(4)));
typedef u32 v2u __attribute__((ext_vector_type(2)));
typedef float f32x2 __attribute__((ext_vector_type(2)));
typedef float f32x4 __attribute__((ext_vector_type(4)));
typedef float f32x16 __attribute__((ext_vector_type(16)));
typedef _Float16 f16x2 __attribute__((ext_vector_type(2)));
typedef _Float16 f16x8 __attribute__((ext_vector_type(8)));
typedef __bf16 bf16x2 __attribute__((ext_vector_type(2)));
typedef __bf16 bf16x8 __attribute__((ext_vector_type(8)));
typedef short s16x4 __attribute__((ext_vector_type(4)));
#define LAS __attribute__((address_space(3)))
#define SB() __builtin_amdgcn_sched_barrier(0)
#define PIN(x) asm volatile("" : "+v"(x))
#define BARRIER() asm volatile("s_waitcnt lgkmcnt(0)\n\ts_barrier" ::: "memory")
#define BARRIER_RAW() asm volatile("s_barrier" ::: "memory")
#define NAP(n) do { __builtin_amdgcn_s_sleep(n); asm volatile("" ::: "memory"); } while (0)
constexpr int SC1 = 16;


constexpr int WS_EPOCH = 0;
constexpr int WS_WF2 = 1024;
constexpr int WS_TR = 4096;
constexpr int WS_HF = 8192;
constexpr int WS_AF = WS_HF + 64 * 256;
constexpr int WS_SG = 57344;
constexpr int WS_WF1 = 65536;
constexpr int WS_CS = 65536 + 8192;
constexpr int WS_ST1 = WS_CS + 32768;
constexpr int WS_XX = WS_ST1 + 16384;
constexpr int WS_W1T = WS_XX + 224 * 16384;
constexpr int WS_W2T = WS_W1T + 112 * 28 * 1024;
constexpr int WS_HT = WS_W2T + 14 * 112 * 1024;
constexpr int WS_ACT = WS_HT + D * L * 2;
constexpr int WS_END = WS_ACT + 64 * 112 * 1024;
constexpr int SPIN_CAP = 1 << 16;
static_assert(WS_AF + 16 * 2 * 8 * 128 <= WS_SG, "flags overlap");

__device__ __forceinline__ float bflo(u32 v) { return __builtin_bit_cast(float, v << 16); }
__device__ __forceinline__ float bfhi(u32 v) { return __builtin_bit_cast(float, v & 0xffff0000u); }
__device__ __forceinline__ u32 pk_bf16(float a, float b) { f32x2 f = {a, b}; return __builtin_bit_cast(u32, __builtin_convertvector(f, bf16x2)); }
__device__ __forceinline__ u32 pk_bf16s(float a, float b) { asm("" : "+v"(a)); asm("" : "+v"(b)); return pk_bf16(a, b); }
__device__ __forceinline__ u32 pk_f16(float a, float b) { f32x2 f = {a, b}; return __builtin_bit_cast(u32, __builtin_convertvector(f, f16x2)); }
__device__ __forceinline__ float rbf(float a) { return bflo(pk_bf16(a, 0.f)); }
__device__ __forceinline__ float dot2bf(u32 a, u32 b, float c) {
    return __builtin_amdgcn_fdot2_f32_bf16(__builtin_bit_cast(bf16x2, a), __builtin_bit_cast(bf16x2, b), c, false);
}
__device__ __forceinline__ f32x16 mfma_bf16(v4u a, v4u b, f32x16 c) {
    return __builtin_amdgcn_mfma_f32_32x32x16_bf16(__builtin_bit_cast(bf16x8, a), __builtin_bit_cast(bf16x8, b), c, 0, 0, 0);
}
__device__ __forceinline__ f32x16 mfma_f16(v4u a, v4u b, f32x16 c) {
    return __builtin_amdgcn_mfma_f32_32x32x16_f16(__builtin_bit_cast(f16x8, a), __builtin_bit_cast(f16x8, b), c, 0, 0, 0);
}
__device__ __forceinline__ v2u lds_tr(LAS char* p) {
    return __builtin_bit_cast(v2u, __builtin_amdgcn_ds_read_tr16_b64_v4i16((LAS s16x4*)p));
}
__device__ __forceinline__ v4u lds_tr2(LAS char* p0, LAS char* p1) {
    const v2u lo = lds_tr(p0), hi = lds_tr(p1);
    return v4u{lo[0], lo[1], hi[0], hi[1]};
}
template <int CTRL> __device__ __forceinline__ float dpp(float v) {
    return __builtin_bit_cast(float, __builtin_amdgcn_update_dpp(0, __builtin_bit_cast(int, v), CTRL, 0xf, 0xf, true));
}
__device__ __forceinline__ float rdl(float v, int l) { return __builtin_bit_cast(float, __builtin_amdgcn_readlane(__builtin_bit_cast(int, v), l)); }
__device__ __forceinline__ float wave_sum(float v) {
    v += dpp<0xB1>(v); v += dpp<0x4E>(v); v += dpp<0x141>(v); v += dpp<0x140>(v);
    return (rdl(v, 0) + rdl(v, 16)) + (rdl(v, 32) + rdl(v, 48));
}
__device__ __forceinline__ float half_sum(float v) {
    const auto r = __builtin_amdgcn_permlane32_swap(__builtin_bit_cast(u32, v), __builtin_bit_cast(u32, v), false, false);
    return __builtin_bit_cast(float, (u32)r[0]) + __builtin_bit_cast(float, (u32)r[1]);
}
template <int I> struct IC { static constexpr int v = I; };
template <int B, int E, class Fn> __device__ __forceinline__ void static_for(Fn&& f) {
    if constexpr (B < E) { f(IC<B>{}); static_for<B + 1, E>(f); }
}

typedef f32x2 cf;
__device__ __forceinline__ cf rot(cf b) { return cf{-b.y, b.x}; }
__device__ __forceinline__ cf cmul(cf a, cf b) { return __builtin_elementwise_fma(a.yy, rot(b), a.xx * b); }
__device__ __forceinline__ cf cmulc(cf a, cf b) { return __builtin_elementwise_fma(a.yy, b.yx, a.xx * rot(b).yx); }
__device__ __forceinline__ u32 pkh(cf a) { return __builtin_bit_cast(u32, __builtin_convertvector(a, f16x2)); }

typedef __amdgpu_buffer_rsrc_t rsrc_t;
__device__ __forceinline__ v4u ld16(rsrc_t r, int voff, int soff) { return __builtin_amdgcn_raw_buffer_load_b128(r, voff, soff, SC1); }
__device__ __forceinline__ u32 ld4(rsrc_t r, int voff, int soff) { return __builtin_amdgcn_raw_buffer_load_b32(r, voff, soff, SC1); }
__device__ __forceinline__ void st16(rsrc_t r, int voff, v4u v) { __builtin_amdgcn_raw_buffer_store_b128(v, r, voff, 0, SC1); }
__device__ __forceinline__ void st8(rsrc_t r, int voff, int soff, v2u v) { __builtin_amdgcn_raw_buffer_store_b64(v, r, voff, soff, SC1); }
__device__ __forceinline__ void st4(rsrc_t r, int voff, int soff, u32 v) { __builtin_amdgcn_raw_buffer_store_b32(v, r, voff, soff, SC1); }
template <int N> __device__ __forceinline__ void wait_vm() { asm volatile("s_waitcnt vmcnt(%0)" :: "i"(N) : "memory"); }
constexpr int OOB = 0x7f000000;

__device__ __forceinline__ void wait_flags(rsrc_t rs, int voff, int soff, u32 v, u32 tag) {
    if (!__all(v == tag)) {
        int spins = SPIN_CAP;
#pragma unroll 1
        do {
            NAP(1);
            v = ld4(rs, voff, soff);
        } while (!__all(v == tag) && --spins > 0);
    }
    __builtin_amdgcn_fence(__ATOMIC_ACQUIRE, "wavefront");
}
__device__ __forceinline__ void glds16(v4u srd, int voff, int soff, u32 lds_dst) {
    asm volatile("s_mov_b32 m0, %3\n\ts_nop 0\n\tbuffer_load_dwordx4 %0, %1, %2 offen sc1 lds"
                 :: "v"(voff), "s"(srd), "s"(soff), "s"(lds_dst) : "memory");
}
__device__ __forceinline__ LAS char* opaque(LAS char* q) { u32 a = (u32)(size_t)q; asm volatile("" : "+v"(a)); return (LAS char*)(size_t)a; }

struct Params {
    const u16 *x, *Are, *Aim, *Bre, *Bim, *Cre, *Cim, *logdt, *Dskip, *wg, *wu, *wd;
    char* ws;
    u16* out;
};

constexpr int XY_STRIDE = 272, XY_ROWS = 59, XY_WAVE = XY_STRIDE * XY_ROWS;
constexpr int P_WAVE = 33 * 144;
constexpr int LD_PB = 0, LD_PA = 38144;
constexpr int LD_HA = 38144, LD_HB = LD_HA + 57344;
constexpr int LD_XU = LD_HB;
constexpr int LD_KP = 128512;
constexpr int LD_U = LD_KP + 8 * 1536;
constexpr int LD_SB = LD_HB + 57344;
constexpr int LD_ST = LD_SB + 64;
constexpr int LD_CS = LD_ST + 4096;
constexpr int LDS_BYTES = LD_CS + 1024;
static_assert(LD_U + 2 * 4160 <= LD_SB && LDS_BYTES <= 163840, "lds map");

__device__ __forceinline__ v4u tr_piece(const v4u (&in)[8], int c) {
    v4u o;
#pragma unroll
    for (int kp = 0; kp < 4; kp++)
        o[kp] = __builtin_amdgcn_perm(in[2 * kp + 1][c >> 1], in[2 * kp][c >> 1], (c & 1) ? 0x07060302u : 0x05040100u);
    return o;
}
__device__ __forceinline__ void w1_task_load(const Params& p, int task, int lane, v2u (&in)[8]) {
    const int t = (task < 0 ? 0 : task) >> 1, c4 = task & 1;
    const int phi = t / 28, r = t - phi * 28, mat = r >= 14, kp = r - 14 * mat;
    const rsrc_t rw = __builtin_amdgcn_make_buffer_rsrc((void*)(mat ? p.wu : p.wg), 0, task < 0 ? 0 : D * F * 2, 0x00020000);
    const int voff = (8 * (lane >> 4) * F + (lane & 15) * 8) * 2;
#pragma unroll
    for (int k = 0; k < 8; k++) in[k] = __builtin_amdgcn_raw_buffer_load_b64(rw, voff + (k & 1) * F * 2, ((32 * kp + (k & 6)) * F + phi * 128 + 4 * c4) * 2, 0);
}
__device__ __forceinline__ void w1_task_store(rsrc_t rs, int task, int lane, const v2u (&in)[8]) {
    const int t = task >> 1, c4 = task & 1;
    const int phi = t / 28, r = t - phi * 28, mat = r >= 14, kp = r - 14 * mat;
    const int voff = WS_W1T + ((phi * 8 + 4 * c4) * 28 + 2 * kp) * 1024 + mat * 256 + (lane >> 4) * 512 + (lane & 15) * 16;
#pragma unroll
    for (int c = 0; c < 4; c++) {
        v4u o;
#pragma unroll
        for (int kp2 = 0; kp2 < 4; kp2++) o[kp2] = __builtin_amdgcn_perm(in[2 * kp2 + 1][c >> 1], in[2 * kp2][c >> 1], (c & 1) ? 0x07060302u : 0x05040100u);
        st16(rs, voff + c * 28 * 1024, o);
    }
}
__device__ __forceinline__ int w1_flag(int task) { const int t = task >> 1, phi = t / 28; return WS_WF1 + (phi * 2 + (task & 1)) * 128 + (t - phi * 28) * 4; }
__device__ __forceinline__ void w2_task(const Params& p, rsrc_t rs, int task, int lane, LAS char* scr) {
    const int del = task / 28, kg = task - del * 28;
    const int k4 = (lane >> 3) & 3, ks = kg * 4 + k4, h = lane >> 5, dr = lane & 7;
    const u16* src = p.wd + (size_t)((ks >> 3) * 128 + 32 * h + (ks & 7)) * D + del * 64 + dr * 8;
    v4u in[8];
#pragma unroll
    for (int j = 0; j < 8; j++) in[j] = __builtin_nontemporal_load((const v4u*)(src + (size_t)(8 * ((j & 3) + 8 * (j >> 2))) * D));
    LAS char* w = scr + ((dr >> 2) * 4 + k4) * 1024 + (32 * h + 8 * (dr & 3)) * 16;
#pragma unroll
    for (int c = 0; c < 8; c++) *(LAS v4u*)(w + c * 16) = tr_piece(in, c);
    const int voff = WS_W2T + (del * 2 * 112 + kg * 4) * 1024 + lane * 16;
#pragma unroll
    for (int f = 0; f < 8; f++) st16(rs, voff + ((f >> 2) * 112 + (f & 3)) * 1024, *(LAS v4u*)(scr + f * 1024 + lane * 16));
}

constexpr u32 GR_T = 320, CS_T = 520;
constexpr int W1_CHK = 2;
constexpr int w1_cnt(int n) { return n <= W1_CHK ? 0 : 4 + ((n - W1_CHK - 1) * 2) / 5; }
constexpr int HF_N = 37;
constexpr int HBP_N = 14, HB_N = 26;
constexpr int ST1_N = 67, AF_N = 83;
constexpr int SG_N = 77;
constexpr int AFP_N = 94, AFC_N = 103, A0_IN = 7;
constexpr int AFB_N = 4;
constexpr int g1_op(int n) {
    return n == 27 ? 52 : (n > HB_N && n <= HB_N + 8) ? 32 + (n - HB_N - 1) - (n > 27)
         : n == 55 ? 40 : n == ST1_N ? 41 : n == 111 ? 42 : n == AF_N ? 50 : n == AFP_N ? 51 : n == SG_N ? 53
         : (n >= 1 && n < 21 && (n & 1) == 1) ? (n - 1) / 2
         : (n >= 36 && n < 52 && (n & 3) == 0) ? 10 + (n - 36) / 4
         : (n == 54 || (n >= 68 && n < 80 && (n & 1) == 0)) ? (n == 54 ? 14 : 15 + (n - 68) / 2)
         : (n >= 85 && n < 99 && (n & 1) == 1) ? 21 + (n - 85) / 2
         : (n > AFC_N && n <= AFC_N + A0_IN) ? 60 + (n - AFC_N - 1)
         : -1;
}
constexpr bool g1_all() { int m = 0; for (int i = 0; i < 112; i++) if (g1_op(i) >= 0 && g1_op(i) < 28) m |= 1 << g1_op(i); return m == (1 << 28) - 1; }
constexpr int g1_cum(int n) { int c = 0; for (int i = 0; i < n; i++) c += g1_op(i) >= 0; return c; }
static_assert(g1_all() && HB_N >= 23 && HB_N < 27 && AF_N > ST1_N && AFP_N > AF_N && AFC_N > AFP_N && AFC_N >= 99 && AFC_N + A0_IN < 111 && w1_cnt(48) <= 28, "schedule");

__global__ __launch_bounds__(512, 2) void fused_block(Params p) {
    extern __shared__ __attribute__((aligned(16))) char smem_[];
    LAS char* sm = (LAS char*)smem_;
    const int tid = threadIdx.x, lane = tid & 63;
    const int wave = __builtin_amdgcn_readfirstlane(tid >> 6);
    const int b = blockIdx.x;
    const bool main_blk = b < 224;
    const int xcd = b & 7, i28 = b >> 3;
    u64 pc0; u32 clen;
    asm volatile("s_getpc_b64 %0\n.Lfb_code_begin:\n\ts_mov_b32 %1, .Lfb_code_end-.Lfb_code_begin" : "=s"(pc0), "=s"(clen));
    const u32 cpf = __builtin_amdgcn_raw_buffer_load_b32(__builtin_amdgcn_make_buffer_rsrc((void*)pc0, 0, main_blk ? 0 : clen, 0x00020000),
                                                         lane < 16 ? lane * 64 : OOB, (wave * 4 + (i28 & 3)) * 1024, 0);
    const int l16 = lane * 16;
    const int rbase = 16 * (b >> 1) + 2 * (b & 1);
    int x2v = lane < 56 ? (lane & 3) * (4 * D * 2) + (lane >> 2) * 8 : OOB, xrv = lane < 56 ? l16 : OOB, t2 = tid * 2;
    int x2s = (rbase * D + 56 * wave) * 2, xrs = (rbase + 4 * (wave >> 1) + (wave & 1)) * D * 2;
    int pair = xcd * 28 + i28;
    asm volatile("" : "+v"(x2v), "+v"(xrv), "+v"(t2), "+s"(x2s), "+s"(xrs), "+s"(pair));
    SB();
    const rsrc_t rs = __builtin_amdgcn_make_buffer_rsrc(p.ws, 0, WS_END, 0x00020000);
    asm volatile("" :: "s"(p.x), "s"(p.wd), "s"(p.ws));
    const u32 tstart = (u32)__builtin_amdgcn_s_memrealtime();
    const u32 tagv = ld4(rs, WS_EPOCH, 0);

    const rsrc_t rx = __builtin_amdgcn_make_buffer_rsrc((void*)p.x, 0, L * D * 2, 0x00020000);
    const v2u xa = __builtin_amdgcn_raw_buffer_load_b64(rx, x2v, x2s, 2);
    const v2u xb = __builtin_amdgcn_raw_buffer_load_b64(rx, x2v + D * 2, x2s, 2);
    const v4u xr = __builtin_amdgcn_raw_buffer_load_b128(rx, xrv, xrs, 2);
    SB();
    const int ch = wave >> 2, wq = wave & 3;
    const int d0 = 2 * pair;
    const int psz = main_blk ? D * N * 2 : 0;
    auto ldp = [&](const u16* q) __attribute__((always_inline)) -> u32 {
        return __builtin_amdgcn_raw_buffer_load_b16(__builtin_amdgcn_make_buffer_rsrc((void*)q, 0, psz, 0x00020000), t2, d0 * N * 2, 2);
    };
    const u32 r_ldt = __builtin_amdgcn_raw_buffer_load_b32(__builtin_amdgcn_make_buffer_rsrc((void*)p.logdt, 0, main_blk ? D * 2 : 0, 0x00020000), 0, d0 * 2, 0);
    const u32 r_are = ldp(p.Are), r_aim = ldp(p.Aim), r_bre = ldp(p.Bre), r_bim = ldp(p.Bim), r_cre = ldp(p.Cre), r_cim = ldp(p.Cim);
    const u32 r_dsk = __builtin_amdgcn_raw_buffer_load_b32(__builtin_amdgcn_make_buffer_rsrc((void*)p.Dskip, 0, main_blk ? D * 2 : 0, 0x00020000), 0, d0 * 2, 0);
    const int hw = (b - 224) + 32 * wave;
    const int w1task = !main_blk ? -1 : wave < 2 ? pair * 2 + wave : (wave < 4 && pair < 168) ? 448 + pair * 2 + wave - 2 : -1;
    SB();
    const int h = lane >> 5, col = lane & 31;
    const int G1 = (lane >> 4) & 1, q4 = (lane & 15) >> 2, pp = lane & 3;
    int xxv = lane < 56 ? WS_XX + ((28 * wave + 2 * (lane >> 2)) * 2 + (b & 1)) * 8192 + (4 * (b >> 1) + (lane & 3)) * 16 : OOB;
    int flv = lane == 0 ? 0 : OOB;
    asm volatile("" : "+v"(xxv), "+v"(flv));
    LAS char* xy = sm + wave * XY_WAVE;
    LAS char* xyw = opaque(xy + lane * 4);
    LAS char* pa = opaque(xy + (32 + (int)((0x1713100d0a080400ull >> (8 * (col >> 2))) & 255) + (col & 3)) * XY_STRIDE + h * 16);
    LAS char* pb = opaque(xy + col * XY_STRIDE + h * 16);
    LAS char* exw = opaque(xy + l16);
    LAS char* exr = opaque(sm + (ch * 4) * XY_WAVE + wq * 1024 + l16);
    LAS char* kpw = opaque(sm + LD_KP + ch * 4 * 1536 + 512 + (wq < 2 ? 64 * wq + 2 * col + h : wq == 2 ? 64 * (2 + h) + 2 * col : 64 * (4 + 2 * h) + 2 * col) * 2);
    LAS char* uw = opaque(sm + LD_U + 64 + tid * 8);
    LAS char* ua = opaque(sm + LD_U + ch * 4160 + 2 * (32 + 32 + 16 * G1 - 32 * h - 16 - 4 * q4 + 4 * pp));
    LAS char* ka = opaque(sm + LD_KP + wave * 1536 + 2 * (256 - 248 + 8 * col + 8 * h));
    LAS char* pw = opaque(sm + wave * P_WAVE + (col + 1) * 144 + h * 16);
    *(LAS v2u*)(sm + LD_KP + wave * 1536 + lane * 8) = v2u{0, 0};
    if (tid < 32) *(LAS u32*)(sm + LD_U + (tid >> 4) * 4160 + (tid & 15) * 4) = 0;
    SB();
    const u32 tag = __builtin_amdgcn_readfirstlane(tagv) + 1u;
    st16(rs, xxv, v4u{xa[0], tag, xb[0], tag});
    st16(rs, xxv + 16384, v4u{xa[1], tag, xb[1], tag});
    SB();
    {
        float a = 0.f, q = 0.f;
#pragma unroll
        for (int i = 0; i < 4; i++) { a = dot2bf(xr[i], 0x3f803f80u, a); q = dot2bf(xr[i], xr[i], q); }
        const float mean = wave_sum(a) * (1.f / D), ex2 = wave_sum(q) * (1.f / D);
        const float var = rbf(fmaxf(ex2 - mean * mean, 0.f));
        const float sd = __builtin_amdgcn_sqrtf(rbf(var + 1e-5f));
        st8(rs, flv, WS_ST1 + (((b & 1) * 512 + 4 * (b >> 1) + (wave >> 1)) * 2 + (wave & 1)) * 8, v2u{pk_bf16(mean, sd), tag});
    }
    SB();
    v2u tin[8];
    w1_task_load(p, w1task, lane, tin);
    SB();
    const int tau = main_blk ? (xcd >> 1) * 4 + (i28 & 3) : (b & 15), phi = (xcd & 1) * 7 + (i28 >> 2);
    const int del = i28 >> 2, tau2 = 2 * tau + (xcd & 1);
    const int trl = q4 * 128 + 8 * (pp & 1) + 16 * (2 * G1 + (pp >> 1)), sw = (q4 & 2) << 5;
    const int hf_v = (lane < 56 ? lane : 0) * 4, hf_s = WS_HF + ((tau >> 2) * 8 + wave) * 256;
    const v4u srd = {(u32)(size_t)p.ws, (u32)((size_t)p.ws >> 32) & 0xffffu, (u32)WS_END, 0x00020000u};
    const int hvoff = WS_HT + ((8 * wave + (lane >> 3)) * L + tau * 64) * 2 + (((lane & 7) ^ (((lane >> 3) & 2) << 1)) << 4);
    if (!main_blk) {
        BARRIER();
        const int w2task = hw;
        if (w2task < 196) {
            w2_task(p, rs, w2task, lane, sm + LD_HB + wave * 8192);
            wait_vm<0>();
            st4(rs, flv, WS_WF2 + (w2task / 28) * 128 + (w2task % 28) * 4, tag);
        }
        asm volatile("" :: "v"(cpf), "v"(tin[7]));
        const int unit = 255 - hw;
        if (unit < 112) {
            while ((u32)__builtin_amdgcn_s_memrealtime() - tstart < CS_T) NAP(8);
            const int fv = (col < 28 ? col : 0) * 4, fs = WS_WF1 + ((unit >> 3) * 2 + ((unit >> 2) & 1)) * 128;
            wait_flags(rs, fv, fs, ld4(rs, fv, fs), tag);
            float c0 = 0.f, c1 = 0.f;
#pragma unroll 1
            for (int k0 = 0; k0 < 28; k0 += 14) {
                v4u w[14];
#pragma unroll
                for (int ks = 0; ks < 14; ks++) w[ks] = ld16(rs, l16 + (ks & 3) * 1024, WS_W1T + (unit * 28 + k0 + (ks & ~3)) * 1024);
#pragma unroll
                for (int ks = 0; ks < 14; ks++) {
                    c0 = dot2bf(w[ks][0], 0x3f803f80u, c0); c1 = dot2bf(w[ks][1], 0x3f803f80u, c1);
                    c0 = dot2bf(w[ks][2], 0x3f803f80u, c0); c1 = dot2bf(w[ks][3], 0x3f803f80u, c1);
                }
            }
            st8(rs, lane < 32 ? WS_CS + lane * 8 : OOB, unit * 256, v2u{__builtin_bit_cast(u32, half_sum(c0 + c1)), tag});
        }
        {
            const int tl = (b >> 4) & 1;
            if (wave == 0) {
                int spins = SPIN_CAP;
#pragma unroll 1
                while (ld4(rs, 0, WS_TR + (b - 224) * 128) != tag && --spins > 0) NAP(4);
            }
            BARRIER();
            if (wave < 7) {
                const int gv = WS_HT + ((lane >> 2) * L + 1024 + tau * 64 + tl * 32) * 2 + (lane & 3) * 16;
                LAS char* as = sm + 4096 + wave * 4096 + (8 * h + q4) * 64 + G1 * 32 + pp * 8;
#pragma unroll
                for (int n = 0; n < 4; n++) glds16(srd, gv, (wave * 4 + n) * 65536, (u32)(size_t)sm + 4096 + wave * 4096 + n * 1024);
                wait_vm<0>();
                v4u f[4];
#pragma unroll
                for (int k = 0; k < 4; k++) f[k] = lds_tr2(as + k * 1024, as + k * 1024 + 256);
                float s1 = 0.f, s2 = 0.f, s1b = 0.f, s2b = 0.f;
#pragma unroll
                for (int k = 0; k < 4; k++) {
                    s1 = dot2bf(f[k][0], 0x3f803f80u, s1); s2 = dot2bf(f[k][0], f[k][0], s2);
                    s1b = dot2bf(f[k][1], 0x3f803f80u, s1b); s2b = dot2bf(f[k][1], f[k][1], s2b);
                    s1 = dot2bf(f[k][2], 0x3f803f80u, s1); s2 = dot2bf(f[k][2], f[k][2], s2);
                    s1b = dot2bf(f[k][3], 0x3f803f80u, s1b); s2b = dot2bf(f[k][3], f[k][3], s2b);
                }
                *(LAS f32x2*)(sm + wave * 512 + lane * 8) = f32x2{s1 + s1b, s2 + s2b};
            }
            BARRIER();
            if (wave == 0) {
                f32x2 t = *(LAS f32x2*)(sm + lane * 8);
#pragma unroll
                for (int k = 1; k < 7; k++) t += *(LAS f32x2*)(sm + k * 512 + lane * 8);
                const float mean = half_sum(t.x) * (1.f / D), ex2 = half_sum(t.y) * (1.f / D);
                const float var = rbf(fmaxf(ex2 - mean * mean, 0.f));
                st8(rs, lane < 32 ? WS_SG + col * 16 + tl * 8 : OOB, tau * 512, v2u{pk_bf16(mean, __builtin_amdgcn_sqrtf(rbf(var + 1e-5f))), tag});
            }
        }
        return;
    }
    asm volatile("" :: "v"(r_are), "v"(r_aim), "v"(r_bre), "v"(r_bim), "v"(r_cre), "v"(r_cim), "v"(r_dsk), "v"(cpf));
    SB();

    float unscale;
    {
        const float dt = rbf(__expf(ch ? bfhi(r_ldt) : bflo(r_ldt)));
        const float ar = dt * bflo(r_are), ai = dt * bflo(r_aim);
        const float br = dt * bflo(r_bre), bi = dt * bflo(r_bim);
        const float cr = bflo(r_cre), ci = bflo(r_cim);
        cf cb = {cr * br - ci * bi, -(cr * bi + ci * br)};
        const float a2 = ar * 1.44269504088896340736f;
        const float rh = ai * 0.15915494309189535f;
        const float rl = __builtin_fmaf(ai, 0.15915494309189535f, -rh) + ai * 6.4206382e-9f;
        auto cexp = [&](float s) __attribute__((always_inline)) -> cf {
            const float e = __builtin_amdgcn_exp2f(a2 * s);
            const float r = __builtin_amdgcn_fractf(rh * s) + rl * s;
            return cf{__builtin_amdgcn_cosf(r), __builtin_amdgcn_sinf(r)} * e;
        };
        const float grow = fmaxf(1.f, __builtin_amdgcn_exp2f(a2 * 2048.f));
        const float sb = wave_sum((fabsf(cb.x) + fabsf(cb.y)) * grow);
        *(LAS float*)(sm + LD_SB + wave * 4) = sb;
        int ex = (int)((__builtin_bit_cast(u32, sb) >> 23) & 255);
        ex = min(max(ex, 30), 230);
        unscale = __builtin_bit_cast(float, (u32)(127 - 15 - 126 + ex) << 23);
        cb *= __builtin_bit_cast(float, (u32)(127 + 15 + 126 - ex) << 23);

        const cf e1 = cexp(1.f), e16 = cexp(16.f), e256 = cexp(256.f);
        const cf e2 = cmul(e1, e1), e32 = cmul(e16, e16), e512 = cmul(e256, e256), x1 = cmulc(cb, e1), x256 = cmulc(cb, e256);
        SB();
        const cf e4 = cmul(e2, e2), e64 = cmul(e32, e32), e1024 = cmul(e512, e512), x2 = cmulc(cb, e2), x3 = cmulc(x1, e2);
        const cf x512 = cmulc(cb, e512), x768 = cmulc(x256, e512), y6 = cmul(e16, e32);
        SB();
        const cf e8 = cmul(e4, e4), e128 = cmul(e64, e64);
        const cf x4 = cmulc(cb, e4), x5 = cmulc(x1, e4), x6 = cmulc(x2, e4), x7 = cmulc(x3, e4), x258 = cmulc(x2, e256);
        const cf x1024 = cmulc(cb, e1024), x1280 = cmulc(x256, e1024), x1536 = cmulc(x512, e1024), x1792 = cmulc(x768, e1024);
        SB();
        auto put = [&](int q, cf v) __attribute__((always_inline)) { *(LAS u32*)(xyw + q * XY_STRIDE) = pkh(v); };
        cf y[8];
        y[0] = cf{1.f, 0.f}; y[1] = e8; y[2] = e16; y[3] = cmul(e8, e16); y[4] = e32; y[5] = cmul(e8, e32); y[6] = y6;
        const cf e192 = cmul(e128, e64), x8 = cmulc(x4, e4), x260 = cmulc(x4, e256), x262 = cmulc(x6, e256), x516 = cmulc(x4, e512);
        put(32, cb); put(33, x1); put(34, x2); put(35, x3); put(36, x4); put(37, x5); put(38, x6); put(39, x7);
        put(40, x256); put(41, x258);
        SB();
        y[7] = cmul(y[3], e32);
        const cf x264 = cmulc(x8, e256), x520 = cmulc(x8, e512), x772 = cmulc(x260, e512), x1032 = cmulc(x8, e1024);
        cf t[8];
#pragma unroll
        for (int q = 1; q < 7; q++) t[q] = cmul(y[q], e64);
        put(42, x260); put(43, x262); put(45, x512); put(46, x516);
#pragma unroll
        for (int q = 0; q < 6; q++) put(q, y[q]);
        SB();
        t[7] = cmul(y[7], e64);
        const cf x776 = cmulc(x264, e512), x1288 = cmulc(x264, e1024), x1544 = cmulc(x520, e1024);
        cf t2[8];
#pragma unroll
        for (int q = 1; q < 8; q++) t2[q] = cmul(y[q], e128);
        put(6, y[6]); put(7, y[7]); put(8, e64);
#pragma unroll
        for (int q = 1; q < 6; q++) put(8 + q, t[q]);
        put(44, x264); put(47, x520); put(48, x768); put(49, x772); put(51, x1024); put(52, x1032);
        SB();

        const cf x1800 = cmulc(x776, e1024);
        cf t3[8];
#pragma unroll
        for (int q = 1; q < 8; q++) t3[q] = cmul(y[q], e192);
        put(14, t[6]); put(15, t[7]); put(16, e128);
#pragma unroll
        for (int q = 1; q < 8; q++) put(16 + q, t2[q]);
        put(50, x776); put(53, x1280); put(54, x1288); put(55, x1536); put(56, x1544); put(57, x1792);
        SB();
        put(24, e192);
#pragma unroll
        for (int q = 1; q < 8; q++) put(24 + q, t3[q]);
        put(58, x1800);
    }
    SB();
    asm volatile("" :: "v"(tin[7]));
    if (w1task >= 0) w1_task_store(rs, w1task, lane, tin);
    SB();
    const int t16 = tid * 16;
    v4u gx[2], gs[2];
    auto gr_load = [&]() __attribute__((always_inline)) {
        gx[0] = ld16(rs, t16, WS_XX + pair * 16384); gx[1] = ld16(rs, t16, WS_XX + pair * 16384 + 8192);
        gs[0] = ld16(rs, t16, WS_ST1); gs[1] = ld16(rs, t16, WS_ST1 + 8192);
    };
    const bool gr_first = (u32)__builtin_amdgcn_s_memrealtime() - tstart >= GR_T;
    if (gr_first) gr_load();
    SB();
    {
        f32x16 kd;
#pragma unroll
        for (int i2 = 0; i2 < 16; i2++) kd[i2] = 0.f;
        v4u fa[8], fb[8];
#pragma unroll
        for (int s = 0; s < 8; s++) { fa[s] = *(LAS v4u*)(pa + s * 32); fb[s] = *(LAS v4u*)(pb + s * 32); }
        SB();
#pragma unroll
        for (int s = 0; s < 8; s++) kd = mfma_f16(fa[s], fb[s], kd);
        SB();

#pragma unroll
        for (int g2 = 0; g2 < 4; g2++)
            *(LAS f32x4*)(exw + g2 * 1024) = f32x4{kd[4 * g2], kd[4 * g2 + 1], kd[4 * g2 + 2], kd[4 * g2 + 3]} * unscale;
    }
    if (w1task >= 0) { wait_vm<0>(); st4(rs, flv, w1_flag(w1task), tag); }
    if (!gr_first) {
        while ((u32)__builtin_amdgcn_s_memrealtime() - tstart < GR_T) NAP(2);
        gr_load();
    }
    SB();
    BARRIER();
    const int l28 = (col < 28 ? col : 0) * 4;
    SB();
    float ik;
    {
        const f32x4 s0 = *(LAS f32x4*)(sm + LD_SB + ch * 16);
        const f32x4 a0 = *(LAS f32x4*)(exr), a1 = *(LAS f32x4*)(exr + XY_WAVE), a2 = *(LAS f32x4*)(exr + 2 * XY_WAVE), a3 = *(LAS f32x4*)(exr + 3 * XY_WAVE);
        const float b0 = (s0[0] + s0[1]) + (s0[2] + s0[3]);
        int e0 = (int)((__builtin_bit_cast(u32, b0) >> 23) & 255);
        e0 = min(max(e0, 30), 230);
        ik = __builtin_bit_cast(float, (u32)(127 - 15 - 126 + e0) << 23);
        const float sk = __builtin_bit_cast(float, (u32)(127 + 15 + 126 - e0) << 23);
        const f32x4 v = ((a0 + a1) + (a2 + a3)) * sk;
        const u32 p01 = pk_f16(v[0], v[1]), p23 = pk_f16(v[2], v[3]);
        LAS char* o = kpw;
        if (wq == 0) {
            *(LAS u16*)(o) = (u16)p01; *(LAS u16*)(o + 1536) = (u16)(p01 >> 16);
            *(LAS u16*)(o + 2 * 1536) = (u16)p23; *(LAS u16*)(o + 3 * 1536) = (u16)(p23 >> 16);
        } else if (wq == 1) {
            *(LAS u16*)(o) = (u16)p01; *(LAS u16*)(o + 1536) = (u16)p01;
            *(LAS u16*)(o + 2 * 1536) = (u16)(p01 >> 16); *(LAS u16*)(o + 3 * 1536) = (u16)p23;
        } else if (wq == 2) {
            *(LAS u32*)(o) = p01; *(LAS u32*)(o + 1536) = p01;
            *(LAS u32*)(o + 2 * 1536) = (p01 & 0xffffu) | (p23 << 16);
            *(LAS u32*)(o + 3 * 1536) = (p01 >> 16) | (p23 << 16);
        } else {
            const bool ev = (col & 1) == 0;
            *(LAS u32*)(o) = ev ? ((p01 & 0xffffu) | (p01 << 16)) : p01;
            *(LAS u32*)(o + 1536) = p01; *(LAS u32*)(o + 2 * 1536) = p01; *(LAS u32*)(o + 3 * 1536) = p01;
            *(LAS u32*)(o + 128) = ev ? ((p23 & 0xffffu) | (p23 << 16)) : p23;
            *(LAS u32*)(o + 128 + 1536) = p23; *(LAS u32*)(o + 128 + 2 * 1536) = p23; *(LAS u32*)(o + 128 + 3 * 1536) = p23;
        }
    }
    SB();
    auto gr_ok = [&]() __attribute__((always_inline)) {
        const u32 a = (gx[0][1] ^ tag) | (gx[0][3] ^ tag) | (gx[1][1] ^ tag) | (gx[1][3] ^ tag);
        const u32 c = (gs[0][1] ^ tag) | (gs[0][3] ^ tag) | (gs[1][1] ^ tag) | (gs[1][3] ^ tag);
        return __all((a | c) == 0);
    };
    if (!gr_ok()) {
        int spins = SPIN_CAP;
#pragma unroll 1
        do {
            NAP(4);
            gr_load();
        } while (!gr_ok() && --spins > 0);
    }
    v4u wf[28];
    const int w1off = WS_W1T + (phi * 8 + wave) * 28 * 1024;
    auto w1_load = [&](int ks) __attribute__((always_inline)) {
        wf[ks] = ld16(rs, l16 + ((ks % 7) & 3) * 1024, w1off + (ks / 7) * 7168 + ((ks % 7) >> 2) * 4096);
    };
    const int wf_s = WS_WF1 + phi * 256 + ch * 128;
    const u32 wfl = ld4(rs, l28, wf_s);
    SB();
    u32 xc[4], udk[4];
    {
        float u0[4], u1[4];
#pragma unroll
        for (int i = 0; i < 4; i++) {
            xc[i] = gx[i >> 1][2 * (i & 1)];
            const u32 st = gs[i >> 1][2 * (i & 1)];
            const float m = bflo(st), r = __builtin_amdgcn_rcpf(bfhi(st));
            const u32 dd = pk_bf16(bflo(xc[i]) - m, bfhi(xc[i]) - m);
            const u32 uu = pk_bf16(bflo(dd) * r, bfhi(dd) * r);
            u0[i] = bflo(uu); u1[i] = bfhi(uu);
        }
        *(LAS v2u*)(uw) = v2u{pk_f16(u0[0], u0[1]), pk_f16(u0[2], u0[3])};
        *(LAS v2u*)(uw + 4160) = v2u{pk_f16(u1[0], u1[1]), pk_f16(u1[2], u1[3])};
#pragma unroll
        for (int i = 0; i < 4; i++) udk[i] = pk_bf16(u0[i] * bflo(r_dsk), u1[i] * bfhi(r_dsk));
    }
    BARRIER();
    f32x4 r0, r1, l3;
    f32x2 r2, l2;
    float l1, r3;
    v4u xu;
    u32 ya, yb;
    const int c1 = tid & 255;
    LAS char* q1 = opaque(sm + ch * 4 * P_WAVE + ((c1 >> 3) + 1) * 144 + (c1 & 7) * 16);
    LAS char* q0 = opaque(sm + ch * 4 * P_WAVE + (((c1 - 1) >> 3) + 1) * 144 + ((c1 - 1) & 7) * 16);
    LAS char* xur = opaque(sm + LD_XU + ch * 8192 + c1 * 16);
    auto ce_read = [&](int hf) __attribute__((always_inline)) {
        const int pb_ = hf ? LD_PB : LD_PA;
        r0 = *(LAS f32x4*)(q1 + pb_);
        r1 = *(LAS f32x4*)(q1 + pb_ + P_WAVE); l1 = *(LAS float*)(q0 + pb_ + P_WAVE + 12);
        r2 = *(LAS f32x2*)(q1 + pb_ + 2 * P_WAVE); l2 = *(LAS f32x2*)(q0 + pb_ + 2 * P_WAVE + 8);
        r3 = *(LAS float*)(q1 + pb_ + 3 * P_WAVE); l3 = *(LAS f32x4*)(q0 + pb_ + 3 * P_WAVE);
        xu = *(LAS v4u*)(xur + hf * 4096);
    };
    auto ce_part = [&](int hf, int part) __attribute__((always_inline)) {
        if (part == 0) ya = pk_bf16s(((r0[0] + l2[0]) + (l1 + l3[1])) * ik, ((r0[1] + l2[1]) + (r1[0] + l3[2])) * ik);
        if (part == 1) yb = pk_bf16s(((r0[2] + r2[0]) + (r1[1] + l3[3])) * ik, ((r0[3] + r2[1]) + (r1[2] + r3)) * ik);
        if (part == 2) ya = pk_bf16s(bflo(ya) + bfhi(xu[0]), bfhi(ya) + bfhi(xu[1]));
        if (part == 3) yb = pk_bf16s(bflo(yb) + bfhi(xu[2]), bfhi(yb) + bfhi(xu[3]));
        if (part == 4) ya = pk_bf16s(bflo(ya) + bflo(xu[0]), bfhi(ya) + bflo(xu[1]));
        if (part == 5) {
            yb = pk_bf16s(bflo(yb) + bflo(xu[2]), bfhi(yb) + bflo(xu[3]));
            st8(rs, WS_HT + c1 * 8, (d0 + ch) * (L * 2) + hf * 2048, v2u{ya, yb});
        }
    };
    const int hf_set = WS_HF + (wq * 8 + (((d0 + ch) >> 3) & 7)) * 256 + (((d0 + ch) >> 6) * 8 + ((d0 + ch) & 7)) * 4;
    {
        f32x16 acc0, acc1;
#pragma unroll
        for (int i = 0; i < 16; i++) { acc0[i] = 0.f; acc1[i] = 0.f; }
        constexpr int PD = 3;
        v4u fu[PD + 1], fk[16], fk1[PD + 1];
        auto issue = [&](int n) __attribute__((always_inline)) {
            const int B0 = (n >= 16 && n < 32) ? 2016 - 64 * (n - 16) : 992 - 64 * (n & 15);
            fu[n % (PD + 1)] = lds_tr2(ua + 2 * (B0 - 32) + 32, ua + 2 * (B0 - 32));
            if (n < 16) fk[n] = *(LAS v4u*)(ka + 32 * n);
            if (n >= 32) fk1[n % (PD + 1)] = *(LAS v4u*)(ka + 32 * (n - 16));
        };
#pragma unroll
        for (int n = 0; n < PD; n++) issue(n);
        SB();
        static_for<0, 48>([&](auto N_) __attribute__((always_inline)) {
            constexpr int n = decltype(N_)::v;
            if (n + PD < 48) issue(n + PD);
            if constexpr (n < 16) acc0 = mfma_f16(fu[n % (PD + 1)], fk[n], acc0);
            else if constexpr (n < 32) acc1 = mfma_f16(fu[n % (PD + 1)], fk[n - 16], acc1);
            else acc1 = mfma_f16(fu[n % (PD + 1)], fk1[n % (PD + 1)], acc1);
            if (n == 1) *(LAS v4u*)(sm + LD_XU + t16) = v4u{__builtin_amdgcn_perm(udk[0], xc[0], 0x05040100u), __builtin_amdgcn_perm(udk[1], xc[1], 0x05040100u),
                                                           __builtin_amdgcn_perm(udk[2], xc[2], 0x05040100u), __builtin_amdgcn_perm(udk[3], xc[3], 0x05040100u)};
            if (n == 2) *(LAS v4u*)(sm + LD_XU + 8192 + t16) = v4u{__builtin_amdgcn_perm(udk[0], xc[0], 0x07060302u), __builtin_amdgcn_perm(udk[1], xc[1], 0x07060302u),
                                                                  __builtin_amdgcn_perm(udk[2], xc[2], 0x07060302u), __builtin_amdgcn_perm(udk[3], xc[3], 0x07060302u)};
            if (n == W1_CHK) {
                PIN(acc0); SB();
                wait_flags(rs, l28, wf_s, wfl, tag);
            }
            if constexpr (n == 16 || n == 17) {
                constexpr int g = 2 * (n - 16);
                *(LAS f32x4*)(pw + LD_PA + g * 32) = f32x4{acc0[4 * g], acc0[4 * g + 1], acc0[4 * g + 2], acc0[4 * g + 3]};
                *(LAS f32x4*)(pw + LD_PA + g * 32 + 32) = f32x4{acc0[4 * g + 4], acc0[4 * g + 5], acc0[4 * g + 6], acc0[4 * g + 7]};
            }
            if (n == 17) {
                PIN(acc1);
                if (lane == 63) {
                    *(LAS f32x4*)(sm + LD_PA + wave * P_WAVE + 112) = f32x4{0.f, 0.f, 0.f, 0.f};
                    *(LAS f32x4*)(sm + LD_PB + wave * P_WAVE + 112) = f32x4{acc0[12], acc0[13], acc0[14], acc0[15]};
                }
            }
            if (n == 18) {
                BARRIER();
                __builtin_amdgcn_s_setprio(1);
                st4(rs, (b == 0 && tid == 0) ? WS_EPOCH : OOB, 0, tag);
                ce_read(0);
            }
            if (n >= 21 && n < 27) ce_part(0, n - 21);
            if (n == HF_N) {
                wait_vm<w1_cnt(HF_N) - w1_cnt(26)>();
                st4(rs, flv, hf_set, tag);
                __builtin_amdgcn_s_setprio(0);
            }
            SB();
#pragma unroll
            for (int ks = w1_cnt(n); ks < w1_cnt(n + 1); ks++) w1_load(ks);
            SB();
        });
#pragma unroll
        for (int g = 0; g < 4; g++) *(LAS f32x4*)(pw + LD_PB + g * 32) = f32x4{acc1[4 * g], acc1[4 * g + 1], acc1[4 * g + 2], acc1[4 * g + 3]};
    }
#pragma unroll
    for (int ks = w1_cnt(48); ks < 28; ks++) { w1_load(ks); SB(); }
    const int kap = phi * 8 + wave;
    const u32 w2fl = ld4(rs, l28, WS_WF2 + del * 128);
    v2u csg = __builtin_amdgcn_raw_buffer_load_b64(rs, WS_CS + col * 8, kap * 256, SC1);
    BARRIER();
    ce_read(1);
    auto load_h = [&](int hf, int n) __attribute__((always_inline)) {
        glds16(srd, hvoff + hf * 2048, n * 262144, (u32)(size_t)sm + (hf ? LD_HB : LD_HA) + wave * 1024 + n * 8192);
    };
#pragma unroll
    for (int i = 0; i < 5; i++) ce_part(1, i);
    SB();
    u32 hfl = ld4(rs, hf_v, hf_s);
    SB();
    ce_part(1, 5);
    SB();
    wait_flags(rs, l28, WS_WF2 + del * 128, w2fl, tag);
    if (!__all(csg[1] == tag)) {
        int spins = SPIN_CAP;
#pragma unroll 1
        do {
            NAP(4);
            csg = __builtin_amdgcn_raw_buffer_load_b64(rs, WS_CS + col * 8, kap * 256, SC1);
        } while (!__all(csg[1] == tag) && --spins > 0);
    }
    *(LAS u32*)(sm + LD_CS + wave * 128 + col * 4) = csg[0];
    wait_flags(rs, hf_v, hf_s, hfl, tag);
#pragma unroll
    for (int n = 0; n < 7; n++) load_h(0, n);
    SB();
    wait_vm<7>();
    st4(rs, flv, hf_set + 8192, tag);
    SB();
    wait_vm<1>();
    BARRIER();
    v4u w2[28];
    const int w2v = WS_W2T + (del * 2 * 112 + wave * 14) * 1024 + l16;
    auto w2_lds = [&](int f) __attribute__((always_inline)) { return f < 14 ? LD_PB + (wave * 4 + f - 10) * 1024 : LD_HA + (wave * 7 + f - 14) * 1024; };
    auto w2_load = [&](int f) __attribute__((always_inline)) {
        if (f >= 10 && f < 21) glds16(srd, w2v + (f & 1) * 112 * 1024, (f >> 1) * 1024, (u32)(size_t)sm + w2_lds(f));
        else w2[f] = ld16(rs, w2v + (f & 1) * 112 * 1024, (f >> 1) * 1024);
    };
    const int af_v = (lane < 14 ? lane : 0) * 4, af_s = WS_AF + (tau * 16 + wave) * 128;
    const int av = WS_ACT + (tau2 * 112 + wave * 14) * 1024 + l16;
    const int dd = 64 * del + 8 * wave + 4 * h;
    LAS char* hra = sm + LD_HA + dd * 128 + trl + (((xcd & 1) << 6) ^ sw);
    v2u hr[2];
    u32 afl;
    v4u a0[14], a1[14];
    const int af_set = WS_AF + (tau * 16 + kap / 14) * 128 + (kap % 14) * 4;
    {
        LAS char* at0 = opaque(sm + LD_HA + 8 * h * 128 + trl + sw);
        LAS char* at1 = opaque(sm + LD_HA + 8 * h * 128 + trl + (64 ^ sw));
        LAS char* csr = opaque(sm + LD_CS + wave * 128 + h * 16);
        LAS char* as = opaque(sm + LD_HA + 8 * h * 128 + trl + (((wave & 1) << 6) ^ sw) + (wave >> 1) * 7 * 2048);
        LAS char* str = opaque(sm + LD_ST + col * 32);
        LAS char* cur;
        constexpr int PD = 4;
        v4u fr[PD + 1], sf;
        auto issue = [&](int n) __attribute__((always_inline)) {
            const int tile = n / 28, j = n % 28;
            if (j % 7 == 0) cur = opaque(((tile & 1) ? at1 : at0) + ((j / 7) * 14336 + (tile >> 1) * 57344));
            LAS char* a = cur + (j % 7) * 2048;
            fr[n % (PD + 1)] = lds_tr2(a, a + 512);
        };
        f32x16 acc[2];
        float nm, k0, k1, sd2, eg, eu, ez, er, eo0;
        f32x4 cg4, cu4;
        f32x4 sta, stb;
        float s1, s2, s1b, s2b;
        u32 opk[4];
        auto stat_acc = [&](int k, int part) __attribute__((always_inline)) {
            const u32 f0 = sf[2 * part], f1 = sf[2 * part + 1];
            if (k == 0 && part == 0) { s1 = 0.f; s2 = 0.f; s1b = 0.f; s2b = 0.f; }
            s1 = dot2bf(f0, 0x3f803f80u, s1); s2 = dot2bf(f0, f0, s2);
            s1b = dot2bf(f1, 0x3f803f80u, s1b); s2b = dot2bf(f1, f1, s2b);
        };
        auto stat_write = [&](int hf) __attribute__((always_inline)) {
            const float a = half_sum(s1 + s1b), c = half_sum(s2 + s2b);
            *(LAS f32x2*)(str + (hf * 2 + (wave & 1)) * 1024 + (wave >> 1) * 8) = f32x2{a, c};
        };
        auto epi_read = [&](int tile) __attribute__((always_inline)) { sta = *(LAS f32x4*)(str + tile * 1024); stb = *(LAS f32x4*)(str + tile * 1024 + 16); };
        auto epi_stats = [&]() __attribute__((always_inline)) {
            float m = ((sta[0] + sta[2]) + (stb[0] + stb[2])) * (1.f / D), e2 = ((sta[1] + sta[3]) + (stb[1] + stb[3])) * (1.f / D);
            asm("" : "+v"(m)); asm("" : "+v"(e2));
            const float var = rbf(fmaxf(e2 - m * m, 0.f));
            const float sd = rbf(__builtin_amdgcn_sqrtf(rbf(var + 1e-5f))), r = __builtin_amdgcn_rcpf(sd);
            nm = -rbf(m); sd2 = sd * sd; k0 = r * -2.3022082f; k1 = (r * r) * (r * -0.10294324f);
        };
        v4u sg;
        auto epi_stats2 = [&](u32 st) __attribute__((always_inline)) {
            const float sd = bfhi(st), r = __builtin_amdgcn_rcpf(sd);
            nm = -bflo(st); sd2 = sd * sd; k0 = r * -2.3022082f; k1 = (r * r) * (r * -0.10294324f);
        };
        auto cs_read = [&](int g) __attribute__((always_inline)) { cg4 = *(LAS f32x4*)(csr + g * 32); cu4 = *(LAS f32x4*)(csr + 64 + g * 32); };
        auto slice = [&](const f32x16& a, int sidx) __attribute__((always_inline)) {
            const int e = sidx / 3, st = sidx % 3;
            if (st == 0) {
                if (e > 0) { float o = eu * er; asm("" : "+v"(o)); if ((e - 1) & 1) opk[(e - 1) >> 1] = pk_bf16(eo0, o); else eo0 = o; }
                if (e < 8) {
                    eg = __builtin_fmaf(nm, cg4[e & 3], a[e & 7]); eu = __builtin_fmaf(nm, cu4[e & 3], a[8 + (e & 7)]);
                    asm("" : "+v"(eg), "+v"(eu));
                }
            } else if (st == 1) { ez = __builtin_amdgcn_exp2f(eg * __builtin_fmaf(eg * eg, k1, k0)); asm("" : "+v"(ez)); }
            else { er = __builtin_amdgcn_rcpf(__builtin_fmaf(ez, sd2, sd2)); eu *= eg; asm("" : "+v"(er), "+v"(eu)); }
        };
        auto epi_four = [&](const f32x16& a, int g) __attribute__((always_inline)) {
            const f32x4 nm4 = {nm, nm, nm, nm}, sd4 = {sd2, sd2, sd2, sd2};
            const f32x4 gg = __builtin_elementwise_fma(nm4, cg4, f32x4{a[4 * g], a[4 * g + 1], a[4 * g + 2], a[4 * g + 3]});
            const f32x4 u = __builtin_elementwise_fma(nm4, cu4, f32x4{a[8 + 4 * g], a[9 + 4 * g], a[10 + 4 * g], a[11 + 4 * g]});
            const f32x4 z = gg * __builtin_elementwise_fma(gg * gg, f32x4{k1, k1, k1, k1}, f32x4{k0, k0, k0, k0});
            const f32x4 e = {__builtin_amdgcn_exp2f(z.x), __builtin_amdgcn_exp2f(z.y), __builtin_amdgcn_exp2f(z.z), __builtin_amdgcn_exp2f(z.w)};
            const f32x4 dn = __builtin_elementwise_fma(e, sd4, sd4);
            const f32x4 o = (gg * u) * f32x4{__builtin_amdgcn_rcpf(dn.x), __builtin_amdgcn_rcpf(dn.y), __builtin_amdgcn_rcpf(dn.z), __builtin_amdgcn_rcpf(dn.w)};
            opk[2 * g] = pk_bf16(o.x, o.y); opk[2 * g + 1] = pk_bf16(o.z, o.w);
        };
        auto epi_store = [&](int tile) __attribute__((always_inline)) {
            const int tok32 = (tile >> 1) * 32 + tau * 2 + (tile & 1);
            st16(rs, WS_ACT + l16 + (tok32 * 112 + kap) * 1024, v4u{opk[0], opk[1], opk[2], opk[3]});
        };
#pragma unroll
        for (int n = 0; n < PD; n++) issue(n);
        SB();
        static_for<0, 112>([&](auto N_) __attribute__((always_inline)) {
            constexpr int n = decltype(N_)::v;
            constexpr int tile = n / 28, j = n % 28;
            if (n == 52) {
                wait_vm<g1_cum(52) - g1_cum(HB_N + 9)>();
                BARRIER();
                __builtin_amdgcn_s_setprio(1);
            }
            if (n + PD < 112) issue(n + PD);
            if (j == 0) {
                f32x16 z;
#pragma unroll
                for (int i = 0; i < 16; i++) z[i] = 0.f;
                acc[tile & 1] = mfma_bf16(wf[0], fr[n % (PD + 1)], z);
            } else {
                acc[tile & 1] = mfma_bf16(wf[j], fr[n % (PD + 1)], acc[tile & 1]);
            }
            SB();
            if constexpr (n < 28) {
                constexpr int sk = n;
                if (sk < 21 && sk % 3 == 0) { if (sk > 0) { stat_acc(sk / 3 - 1, 1); SB(); } sf = lds_tr2(as + (sk / 3) * 2048, as + (sk / 3) * 2048 + 512); }
                if (sk < 21 && sk % 3 == 2) stat_acc(sk / 3, 0);
                if (sk == 21) stat_acc(6, 1);
                if (sk == 22) stat_write(0);
            }
            if (n == HBP_N) hfl = ld4(rs, hf_v, hf_s + 8192);
            if (n == HB_N) {
                PIN(acc[0]);
                SB();
                wait_flags(rs, hf_v, hf_s + 8192, hfl, tag);
            }
            if (n == 48) hr[0] = lds_tr(hra);
            if (n == 27) BARRIER_RAW();
            if (n == 27 || n == 55) epi_read(tile);
            if (n == 84) {
                PIN(acc[0]); PIN(acc[1]);
                SB();
                if (!__all(sg[1] == tag && sg[3] == tag)) {
                    int spins = SPIN_CAP;
#pragma unroll 1
                    do {
                        NAP(2);
                        sg = ld16(rs, WS_SG + col * 16, tau * 512);
                    } while (!__all(sg[1] == tag && sg[3] == tag) && --spins > 0);
                }
            }
            if (tile == 1) {
                if (j == 0) cs_read(0);
                if (j == 1) epi_stats();
                if (j >= 2 && j < 27) slice(acc[0], j - 2);
                if (j == 11) { SB(); cs_read(1); }
            }
            if (tile == 2) {
                if (j == 0) cs_read(0);
                if (j == 1) epi_stats();
                if (j >= 2 && j < 10) { slice(acc[1], 3 * (j - 2)); if (j == 5) { SB(); cs_read(1); } slice(acc[1], 3 * (j - 2) + 1); slice(acc[1], 3 * (j - 2) + 2); }
                if (j == 10) slice(acc[1], 24);
            }
            if (tile == 3) {
                if (j == 0) cs_read(0);
                if (j == 1) epi_stats2(sg[0]);
                if (j >= 2 && j < 27) slice(acc[0], j - 2);
                if (j == 11) { SB(); cs_read(1); }
            }
            if (n == AFC_N) {
                PIN(acc[1]); asm volatile("" : "+v"(eg), "+v"(eu), "+v"(ez), "+v"(er), "+v"(eo0), "+v"(opk[0]), "+v"(opk[1]));
                SB();
                wait_flags(rs, af_v, af_s, afl, tag);
            }
            constexpr int op = g1_op(n);
            if constexpr (op >= 60) a0[op - 60] = ld16(rs, av, (op - 60) * 1024);
            if constexpr (op >= 0 && op < 28) w2_load(op);
            if constexpr (op >= 32 && op < 40) load_h(1, op - 32);
            if constexpr (op >= 40 && op < 50) epi_store(op - 40);
            if constexpr (op == 50) { wait_vm<g1_cum(AF_N) - g1_cum(ST1_N + 1)>(); st4(rs, flv, af_set, tag); __builtin_amdgcn_s_setprio(0); BARRIER_RAW(); }
            if constexpr (op == 51) afl = ld4(rs, af_v, af_s);
            if constexpr (op == 52) st4(rs, (tid == 0 && del == 0) ? 0 : OOB, WS_TR + ((xcd & 1) * 16 + tau) * 128, tag);
            if constexpr (op == 53) sg = ld16(rs, WS_SG + col * 16, tau * 512);
            SB();
        });
        PIN(acc[1]);
        cs_read(0);
        epi_stats2(sg[2]);
        epi_four(acc[1], 0);
        cs_read(1);
        epi_four(acc[1], 1);
        epi_store(3);
    }
    SB();
#pragma unroll
    for (int n = A0_IN; n < 14; n++) { a0[n] = ld16(rs, av, n * 1024); SB(); }
    hr[1] = lds_tr(hra + 57344);
#pragma unroll
    for (int f = 10; f < 21; f++) w2[f] = *(LAS v4u*)(sm + w2_lds(f) + l16);
    BARRIER();
    f32x16 acc[2][2];
#pragma unroll
    for (int i = 0; i < 16; i++) { acc[0][0][i] = 0.f; acc[0][1][i] = 0.f; acc[1][0][i] = 0.f; acc[1][1][i] = 0.f; }
    LAS char* rw = opaque(sm + wave * 1024 + l16);
    LAS char* rr = opaque(sm + wave * 8192 + l16);
    auto red_write = [&](int hf) __attribute__((always_inline)) {
#pragma unroll
        for (int o = 0; o < 8; o++) {
            const f32x16& t = acc[o >> 2][hf];
            const int r = 4 * (o & 3);
            *(LAS f32x4*)(rw + hf * 65536 + o * 8192) = f32x4{t[r], t[r + 1], t[r + 2], t[r + 3]};
        }
    };
    auto red_finish = [&](int hf) __attribute__((always_inline)) {
        f32x4 v[8];
#pragma unroll
        for (int s = 0; s < 8; s++) v[s] = *(LAS f32x4*)(rr + hf * 65536 + s * 1024);
        const f32x4 t = ((v[0] + v[1]) + (v[2] + v[3])) + ((v[4] + v[5]) + (v[6] + v[7]));
        const u32 ta = pk_bf16(t[0], t[1]), tb = pk_bf16(t[2], t[3]);
        const int tok = 1024 * hf + 32 * tau2 + col;
        *(v2u*)(p.out + (size_t)tok * D + dd) = v2u{pk_bf16(bflo(ta) + bflo(hr[hf][0]), bfhi(ta) + bfhi(hr[hf][0])),
                                                    pk_bf16(bflo(tb) + bflo(hr[hf][1]), bfhi(tb) + bfhi(hr[hf][1]))};
    };
#pragma unroll
    for (int n = 0; n < 14; n++) {
        acc[0][0] = mfma_bf16(w2[2 * n], a0[n], acc[0][0]);
        acc[1][0] = mfma_bf16(w2[2 * n + 1], a0[n], acc[1][0]);
        if (n == AFB_N) { wait_vm<14 - A0_IN>(); st4(rs, flv, af_set + 1024, tag); }
        SB();
    }
    PIN(acc[0][0]); PIN(acc[1][0]);
    red_write(0);
    BARRIER();
    afl = ld4(rs, af_v, af_s + 1024);
    SB();
    red_finish(0);
    SB();
    wait_flags(rs, af_v, af_s + 1024, afl, tag);
#pragma unroll
    for (int n = 0; n < 14; n++) { a1[n] = ld16(rs, av + 32 * 112 * 1024, n * 1024); SB(); }
    SB();
#pragma unroll
    for (int n = 0; n < 14; n++) {
        acc[0][1] = mfma_bf16(w2[2 * n], a1[n], acc[0][1]);
        acc[1][1] = mfma_bf16(w2[2 * n + 1], a1[n], acc[1][1]);
        SB();
    }
    red_write(1);
    BARRIER();
    red_finish(1);
    asm volatile(".Lfb_code_end:");
}

}

extern "C" void kernel_launch(void* const* d_in, const int* in_sizes, int n_in,
                              void* d_out, int out_size, void* d_ws, size_t ws_size,
                              hipStream_t stream) {
    Params p;
    p.x = (const u16*)d_in[0]; p.Are = (const u16*)d_in[1]; p.Aim = (const u16*)d_in[2];
    p.Bre = (const u16*)d_in[3]; p.Bim = (const u16*)d_in[4]; p.Cre = (const u16*)d_in[5]; p.Cim = (const u16*)d_in[6];
    p.logdt = (const u16*)d_in[7]; p.Dskip = (const u16*)d_in[8];
    p.wg = (const u16*)d_in[10]; p.wu = (const u16*)d_in[11]; p.wd = (const u16*)d_in[12];
    p.ws = (char*)d_ws; p.out = (u16*)d_out;
    (void)hipFuncSetAttribute((const void*)fused_block, hipFuncAttributeMaxDynamicSharedMemorySize, LDS_BYTES);
    fused_block<<<256, 512, LDS_BYTES, stream>>>(p);
}
```
